```python
import torch

def _softmax(x, axis=-1):
    return torch.softmax(x, dim=axis)
def _gelu(x):
    return torch.nn.functional.gelu(x, approximate="tanh")
def _silu(x):
    return torch.nn.functional.silu(x)
def _sigmoid(x):
    return torch.sigmoid(x)
def _layernorm(x, axis=-1, eps=1e-5):
    m = x.mean(dim=axis, keepdim=True)
    v = x.var(dim=axis, keepdim=True, unbiased=False)
    return (x - m) / torch.sqrt(v + eps)
def _scatter_add(tgt, idx, src, axis=0):
    return tgt.clone().index_add_(axis, idx, src)
def _segment_sum(src, seg, n):
    out = torch.zeros((n,) + src.shape[1:], dtype=src.dtype)
    return out.index_add_(0, seg, src)
def _pad(x, pw):
    flat = tuple(v for pair in reversed(pw) for v in pair)
    return torch.nn.functional.pad(x, flat)

kernel_name = 'concat_heads__ssm_conv_h3__linear_attn_block__prompt'

def setup_inputs():

    _w = (lambda a: (lambda t: t.to(torch.bfloat16) if t.is_floating_point() else t)(torch.as_tensor(a)))
    def _A_setup():
        B, S, D = 1, 2048, 1024
        ks = 4
        return {
            "q": _w((lambda *s: torch.randn(*s, dtype=torch.bfloat16) * 0.05)(B, S, D)),
            "k": _w((lambda *s: torch.randn(*s, dtype=torch.bfloat16) * 0.4)(B, S, D)),
            "v": _w((lambda *s: torch.randn(*s, dtype=torch.bfloat16) * 0.4)(B, S, D)),
            "shift_k": _w((lambda *s: torch.randn(*s, dtype=torch.bfloat16) * 0.4)(D, ks) * 0.5),
            "ssm_k": _w((lambda *s: torch.randn(*s, dtype=torch.bfloat16) * 1.6)(D, S) * (1.0 / S ** 0.5)),
            "ssm_D": _w((lambda *s: torch.randn(*s, dtype=torch.bfloat16) * 0.4)(D)),
        }
    _A_in = _A_setup()

    def _B_setup():
        B, S, D = 1, 2048, 1024
        return {
            "x": _w((lambda *s: torch.randn(*s, dtype=torch.bfloat16) * 0.05)(B, S, D)),
            "wqkv": _w((lambda *s: torch.randn(*s, dtype=torch.bfloat16) * 0.8)(D, 3 * D)),
            "wo": _w((lambda *s: torch.randn(*s, dtype=torch.bfloat16) * 0.05)(D, D)),
        }
    _B_in = _B_setup()

    return {
        "A_q": _A_in["q"],
        "A_k": _A_in["k"],
        "A_v": _A_in["v"],
        "A_shift_k": _A_in["shift_k"],
        "A_ssm_k": _A_in["ssm_k"],
        "A_ssm_D": _A_in["ssm_D"],
        "B_x": _B_in["x"],
        "B_wqkv": _B_in["wqkv"],
        "B_wo": _B_in["wo"],
    }

def reference(A_q, A_k, A_v, A_shift_k, A_ssm_k, A_ssm_D, B_x, B_wqkv, B_wo):
    def _A_ref(q, k, v, shift_k, ssm_k, ssm_D):
        B, S, D = q.shape
        ks = shift_k.shape[-1]
        k_pad = _pad(k, ((0, 0), (ks - 1, 0), (0, 0)))
        k_shift = torch.zeros_like(k)
        for t in range(ks):
            k_shift = k_shift + k_pad[:, t:t + S, :] * shift_k[:, ks - 1 - t][None, None, :]
        kv = k_shift * v
        L = 2 * S
        kv_pad = _pad(kv, ((0, 0), (0, S), (0, 0)))
        kf_pad = _pad(ssm_k, ((0, 0), (0, S)))
        kv_f = (lambda x, axes=(-2,-1): torch.fft.rfft2(x.float(), dim=axes))(kv_pad[:, None, :, :], axes=(1, 2))
        kf_f = (lambda x, axes=(-2,-1): torch.fft.rfft2(x.float(), dim=axes))(kf_pad[:, None, :], axes=(1, 2))
        kf_f = (lambda x, *d: x.permute(*d))(kf_f, 1, 2, 0)
        y_f = kv_f * kf_f[None, :, :, :]
        y = (lambda x, s, axes=(-2,-1): torch.fft.irfft2(x, s=s, dim=axes).to(torch.bfloat16))(y_f, (1, L), axes=(1, 2))[:, 0, :S, :]
        y = y + kv * ssm_D[None, None, :]
        return q * y

    def _B_ref(x, wqkv, wo):
        B, S, D = x.shape
        H = 32; Dh = D // H
        qkv = (lambda x, *d: x.permute(*d))((x @ wqkv).reshape(B, S, 3, H, Dh), 2, 0, 3, 1, 4)
        q, k, v = qkv[0], qkv[1], qkv[2]
        phi = lambda t: torch.where(t > 0, t + 1, torch.exp(t)) + 1e-6
        pq, pk = phi(q), phi(k)
        kv = torch.einsum("bhsd,bhse->bhde", pk, v)
        z = torch.einsum("bhsd->bhd", pk)
        num = torch.einsum("bhqd,bhde->bhqe", pq, kv)
        den = torch.einsum("bhqd,bhd->bhq", pq, z)[..., None] + 1e-6
        o = num / den
        return (lambda x, *d: x.permute(*d))(o, 0, 2, 1, 3).reshape(B, S, D) @ wo

    a = _A_ref(A_q, A_k, A_v, A_shift_k, A_ssm_k, A_ssm_D)
    b = _B_ref(B_x, B_wqkv, B_wo)
    return torch.concatenate([a, b], axis=-1)
```

```cpp
#include <hip/hip_runtime.h>
#include <hip/hip_bf16.h>
typedef unsigned short u16;
__device__ __forceinline__ float b2f(u16 x){ return __uint_as_float(((unsigned)x)<<16); }
__device__ __forceinline__ u16 f2b(float f){ __hip_bfloat16 h = __float2bfloat16(f); return *(u16*)&h; }
__device__ __forceinline__ float rb(float f){ return b2f(f2b(f)); }
constexpr int S=2048, D=1024;
__global__ void k_kv(const u16* k, const u16* v, const u16* sk, u16* kvT){
  int d = blockIdx.x*256+threadIdx.x; int s = blockIdx.y;
  float acc=0.f;
  for(int j=3;j>=0;--j){ float kk = (s-j>=0)? b2f(k[(s-j)*D+d]):0.f; acc = rb(acc + rb(kk*b2f(sk[d*4+j]))); }
  kvT[d*S+s] = f2b(acc*b2f(v[s*D+d]));
}
__global__ void k_conv(const u16* kvT, const u16* h, const u16* q, const u16* Dv, u16* out){
  __shared__ float xs[S], hs[S];
  int d = blockIdx.x;
  for(int i=threadIdx.x;i<S;i+=256){ xs[i]=b2f(kvT[d*S+i]); hs[i]=b2f(h[d*S+i]); }
  __syncthreads();
  for(int s=threadIdx.x;s<S;s+=256){
    float a=0.f; for(int t=0;t<=s;++t) a += hs[t]*xs[s-t];
    float y = rb(rb(a) + rb(xs[s]*b2f(Dv[d])));
    out[s*2*D+d] = f2b(b2f(q[s*D+d])*y);
  }
}
__global__ void k_gemm(const u16* A, const u16* B, u16* C, int N, int K, int ldc){
  int n = blockIdx.x*256+threadIdx.x; int m = blockIdx.y;
  float a=0.f; for(int k=0;k<K;++k) a += b2f(A[m*K+k])*b2f(B[k*N+n]);
  C[m*ldc+n]=f2b(a);
}
__device__ __forceinline__ float phi(float t){ float r = t>0.f? rb(t+1.f) : rb(expf(t)); return rb(r+1e-6f); }
__global__ void k_kvz(const u16* qkv, u16* kv, u16* z){
  int h = blockIdx.x; int d = threadIdx.x>>5, e = threadIdx.x&31;
  float a=0.f, zz=0.f;
  for(int s=0;s<S;++s){ float pk = phi(b2f(qkv[s*3*D + D + h*32+d])); a += pk*b2f(qkv[s*3*D+2*D+h*32+e]); zz+=pk; }
  kv[h*1024+d*32+e]=f2b(a); if(e==0) z[h*32+d]=f2b(zz);
}
__global__ void k_o(const u16* qkv, const u16* kv, const u16* z, u16* o){
  int c = blockIdx.x*256+threadIdx.x; int s = blockIdx.y; int h=c>>5, e=c&31;
  float num=0.f, den=0.f;
  for(int d=0;d<32;++d){ float pq = phi(b2f(qkv[s*3*D+h*32+d])); num += pq*b2f(kv[h*1024+d*32+e]); den += pq*b2f(z[h*32+d]); }
  float dn = rb(rb(den)+1e-6f);
  o[s*D+c] = f2b(rb(num)/dn);
}
extern "C" void kernel_launch(void* const* d_in, const int* in_sizes, int n_in, void* d_out, int out_size, void* d_ws, size_t ws_size, hipStream_t stream) {
  const u16 *q=(const u16*)d_in[0], *k=(const u16*)d_in[1], *v=(const u16*)d_in[2], *sk=(const u16*)d_in[3], *h=(const u16*)d_in[4], *Dv=(const u16*)d_in[5], *x=(const u16*)d_in[6], *wqkv=(const u16*)d_in[7], *wo=(const u16*)d_in[8];
  u16* out=(u16*)d_out; u16* ws=(u16*)d_ws;
  u16* kvT=ws; u16* qkv=ws+S*D; u16* kv=qkv+S*3*D; u16* z=kv+32*1024; u16* o=z+1024;
  k_kv<<<dim3(4,S),256,0,stream>>>(k,v,sk,kvT);
  k_conv<<<D,256,0,stream>>>(kvT,h,q,Dv,out);
  k_gemm<<<dim3(12,S),256,0,stream>>>(x,wqkv,qkv,3*D,D,3*D);
  k_kvz<<<32,1024,0,stream>>>(qkv,kv,z);
  k_o<<<dim3(4,S),256,0,stream>>>(qkv,kv,z,o);
  k_gemm<<<dim3(4,S),256,0,stream>>>(o,wo,out+D,D,D,2*D);
}
```

```cpp
#include <hip/hip_runtime.h>
#include <hip/hip_bf16.h>

typedef unsigned short u16;
typedef unsigned int u32;
typedef unsigned long long u64;
typedef float f32x2 __attribute__((ext_vector_type(2)));
typedef float f32x4 __attribute__((ext_vector_type(4)));
typedef u32 u32x2 __attribute__((ext_vector_type(2)));
typedef u32 u32x4 __attribute__((ext_vector_type(4)));
typedef short s16x4 __attribute__((ext_vector_type(4)));
typedef short s16x8 __attribute__((ext_vector_type(8)));
typedef __bf16 bf16x2 __attribute__((ext_vector_type(2)));
typedef __bf16 bf16x8 __attribute__((ext_vector_type(8)));
typedef __amdgpu_buffer_rsrc_t rsrc_t;
#define LAS __attribute__((address_space(3)))
#define GAS __attribute__((address_space(1)))
#define DI __device__ __forceinline__

#define USE_MEMSET 1
#define CODE_PF 0
#define STAMPS 0

constexpr u32 WS_EPOCH = 0;
constexpr u32 WS_F1 = 4096;
constexpr u32 WS_F2 = WS_F1 + 8192;
constexpr u32 WS_FA = WS_F2 + 8192;
constexpr u32 WS_FY = WS_FA + 8192;
constexpr u32 WS_CTRL_END = WS_FY + 8192;
constexpr u32 WS_Z = 512u << 10;
constexpr u32 WS_PART = 1u << 20;
constexpr u32 WS_O = 4u << 20;
constexpr u32 WS_KVT = 8u << 20;
constexpr u32 WS_YT = 12u << 20;
constexpr u32 WS_BYTES = 16u << 20;
constexpr u32 WS_DBG = 32u << 20;

constexpr int SLOT = 40960;
constexpr int L_X1 = 98304;
constexpr int L_X2 = L_X1 + 4 * 2304;
constexpr int CH_STRIDE = 12960;
constexpr int L_CONV = 163840 - 4 * CH_STRIDE;
constexpr int C_O = 4608, C_E = C_O + 4160;
constexpr int L_FLAG = 163840 - 64;
constexpr int LDS_BYTES = 163840;
static_assert(L_X2 + 4096 <= L_CONV, "lds map");

constexpr int AUX_SC1 = 16;
constexpr u32 SPIN_CAP = 1u << 20;

DI f32x4 mfma(u32x4 a, u32x4 b, f32x4 c) {
  return __builtin_amdgcn_mfma_f32_16x16x32_bf16(__builtin_bit_cast(bf16x8, a), __builtin_bit_cast(bf16x8, b), c, 0, 0, 0);
}
DI u32 pk2(float lo, float hi) {
  bf16x2 r = __builtin_convertvector(f32x2{lo, hi}, bf16x2);
  return __builtin_bit_cast(u32, r);
}
DI float blo(u32 x) { return __uint_as_float(x << 16); }
DI float bhi(u32 x) { return __uint_as_float(x & 0xffff0000u); }
template <int N> DI void waitv() { asm volatile("s_waitcnt vmcnt(%0)" ::"n"(N) : "memory"); }
DI void waitl() { asm volatile("s_waitcnt lgkmcnt(0)" ::: "memory"); }
#define BAR() do { asm volatile("" ::: "memory"); __builtin_amdgcn_s_barrier(); asm volatile("" ::: "memory"); } while (0)
#define SCHED() __builtin_amdgcn_sched_barrier(0)
DI rsrc_t mkrsrc(const void* p, u32 bytes) { return __builtin_amdgcn_make_buffer_rsrc((void*)p, 0, bytes, 0x20000); }
DI void dma16(rsrc_t r, u32 lds, u32 voff, u32 soff) {
  asm volatile("s_mov_b32 m0, %0\n\ts_nop 0\n\tbuffer_load_dwordx4 %1, %2, %3 offen lds" ::"s"(lds), "v"(voff), "s"(r), "s"(soff) : "memory");
}
DI void ald64(u32x2& d, rsrc_t r, u32 voff, u32 soff) {
  asm volatile("buffer_load_dwordx2 %0, %1, %2, %3 offen" : "=v"(d) : "v"(voff), "s"(r), "s"(soff) : "memory");
}
DI void ald64_sc(u32x2& d, rsrc_t r, u32 voff, u32 soff) {
  asm volatile("buffer_load_dwordx2 %0, %1, %2, %3 offen sc1" : "=v"(d) : "v"(voff), "s"(r), "s"(soff) : "memory");
}
DI void ald128(u32x4& d, rsrc_t r, u32 voff, u32 soff) {
  asm volatile("buffer_load_dwordx4 %0, %1, %2, %3 offen" : "=v"(d) : "v"(voff), "s"(r), "s"(soff) : "memory");
}
DI void ald128_sc(u32x4& d, rsrc_t r, u32 voff, u32 soff) {
  asm volatile("buffer_load_dwordx4 %0, %1, %2, %3 offen sc1" : "=v"(d) : "v"(voff), "s"(r), "s"(soff) : "memory");
}
#define PIN(x) asm volatile("" : "+v"(x))
DI u32x4 ld128_sc(rsrc_t r, u32 voff, u32 soff) { return __builtin_amdgcn_raw_buffer_load_b128(r, voff, soff, AUX_SC1); }
DI void st128_sc(u32x4 v, rsrc_t r, u32 voff, u32 soff) { __builtin_amdgcn_raw_buffer_store_b128(v, r, voff, soff, AUX_SC1); }

DI void publish(rsrc_t ws, u32 off, u32x2 tag, int lane) {
  waitv<0>();
  if (lane == 0) __builtin_amdgcn_raw_buffer_store_b64(tag, ws, off, 0, AUX_SC1);
}
DI void await(rsrc_t ws, u32 off, int mask, u32x2 tag, int lane) {
  const u32 vo = off + 8u * (lane & mask);
#pragma unroll 1
  for (u32 spins = 0; spins < SPIN_CAP; ++spins) {
    u32x2 f;
    asm volatile("buffer_load_dwordx2 %0, %1, %2, 0 offen sc1\n\ts_waitcnt vmcnt(0)" : "=&v"(f) : "v"(vo), "s"(ws) : "memory");
    if (__all(f.x == tag.x && f.y == tag.y)) break;
    __builtin_amdgcn_s_sleep(1);
  }
  asm volatile("" ::: "memory");
}
DI void lds_signal(LAS char* smem, int word, int lane) {
  waitl();
  if (lane == 0) __hip_atomic_fetch_add((LAS u32*)(smem + L_FLAG) + word, 1u, __ATOMIC_RELAXED, __HIP_MEMORY_SCOPE_WORKGROUP);
}
DI void lds_await(LAS char* smem, int word, u32 want) {
  for (u32 spins = 0; spins < (SPIN_CAP << 3); ++spins) {
    const u32 v = __hip_atomic_load((LAS u32*)(smem + L_FLAG) + word, __ATOMIC_RELAXED, __HIP_MEMORY_SCOPE_WORKGROUP);
    if (__builtin_amdgcn_readfirstlane(v) >= want) break;
  }
  asm volatile("" ::: "memory");
}

#define STAMP(i)
#define STAMP_OUT()

struct Params {
  const u16 *q, *k, *v, *sk, *h, *dd, *x, *wqkv, *wo;
  u16* out;
  char* ws;
};

struct Frags { u32x4 x[4]; u32x4 w[6]; };
DI u32x4 tr_frag(LAS char* a, int d) {
  s16x4 lo = __builtin_amdgcn_ds_read_tr16_b64_v4i16((LAS s16x4*)a);
  s16x4 hi = __builtin_amdgcn_ds_read_tr16_b64_v4i16((LAS s16x4*)(a + d));
  return __builtin_bit_cast(u32x4, __builtin_shufflevector(lo, hi, 0, 1, 2, 3, 4, 5, 6, 7));
}
DI void ldfr(Frags& f, LAS char* xa, LAS char* wb0, LAS char* wb1, int ks) {
#pragma unroll
  for (int mt = 0; mt < 4; ++mt) f.x[mt] = *(LAS u32x4*)(xa + mt * 2048);
#pragma unroll
  for (int sg = 0; sg < 3; ++sg)
#pragma unroll
    for (int t = 0; t < 2; ++t) f.w[sg * 2 + t] = tr_frag((t ? wb1 : wb0) + sg * 8192 + ks * 4096, 512);
}
DI void mm(f32x4 (&acc)[4][6], const Frags& f) {
#pragma unroll
  for (int n = 0; n < 6; ++n)
#pragma unroll
    for (int m_ = 0; m_ < 4; ++m_) {
      const int mt = (n & 1) ? 3 - m_ : m_;
      if (n < 2) acc[mt][n] = mfma(f.w[n], f.x[mt], acc[mt][n]);
      else acc[mt][n] = mfma(f.x[mt], f.w[n], acc[mt][n]);
    }
}
DI void interleave_b1() {
#pragma unroll
  for (int i = 0; i < 16; ++i) { __builtin_amdgcn_sched_group_barrier(0x8, 1, 0); __builtin_amdgcn_sched_group_barrier(0x100, 1, 0); }
  __builtin_amdgcn_sched_group_barrier(0x8, 8, 0);
}
constexpr float EPS = 1e-6f;
DI float phi(float t) {
  const float e = __builtin_amdgcn_fmed3f(__builtin_amdgcn_exp2f(__builtin_fmaf(t, 1.44269504088896f, -EPS * 1.44269504088896f)), 0.f, 1.f);
  return e + __builtin_fmaxf(t, EPS);
}
DI u32x4 pack_phi(f32x4 a, f32x4 b) { return u32x4{pk2(phi(a[0]), phi(a[1])), pk2(phi(a[2]), phi(a[3])), pk2(phi(b[0]), phi(b[1])), pk2(phi(b[2]), phi(b[3]))}; }
DI u32x4 pack8(f32x4 a, f32x4 b) { return u32x4{pk2(a[0], a[1]), pk2(a[2], a[3]), pk2(b[0], b[1]), pk2(b[2], b[3])}; }
template <int CTRL> DI float dpp_add(float v) {
  return v + __int_as_float(__builtin_amdgcn_update_dpp(0, __float_as_int(v), CTRL, 0xf, 0xf, false));
}
DI float row_allsum(float v) { v = dpp_add<0x128>(v); v = dpp_add<0x124>(v); v = dpp_add<0x122>(v); v = dpp_add<0x121>(v); return v; }

struct CFr { u32x4 a[8]; u32x4 b[8]; };
DI void ldconv(CFr& f, LAS char* ha, LAS char* xb) {
#pragma unroll
  for (int i = 0; i < 8; ++i) {
    f.b[i] = *(LAS u32x4*)(xb + (7 - i) * 512);
    LAS u32* hp = (LAS u32*)(ha + i * 512);
    f.a[i] = u32x4{hp[0], hp[1], hp[2], hp[3]};
  }
}
DI void mmconv(f32x4 (&acc)[8], const CFr& f) {
#pragma unroll
  for (int n = 0; n < 8; ++n)
#pragma unroll
    for (int q = 0; q <= n; ++q) acc[n] = mfma(f.a[q], f.b[n - q], acc[n]);
}
DI void interleave_conv() {
#pragma unroll
  for (int i = 0; i < 24; ++i) { __builtin_amdgcn_sched_group_barrier(0x8, 1, 0); __builtin_amdgcn_sched_group_barrier(0x100, 1, 0); }
  __builtin_amdgcn_sched_group_barrier(0x8, 12, 0);
}

__global__ __launch_bounds__(512, 2) void fused_h3_linattn(Params p) {
  extern __shared__ __attribute__((aligned(16))) char smem_[];
  LAS char* smem = (LAS char*)smem_;
  const int bid = blockIdx.x, xcd = bid & 7, j = bid >> 3;
  const int rb = (xcd & 1) * 8 + (j & 7), cb = (xcd >> 1) * 4 + (j >> 3);
  const int rb2 = xcd * 2 + (j & 1), cb2 = j >> 1;
  const int hx = (cb2 & 1) * 16;
  const int wave = __builtin_amdgcn_readfirstlane(threadIdx.x >> 6), lane = threadIdx.x & 63;
  const rsrc_t ws = mkrsrc(p.ws, WS_BYTES);
  STAMP(0);

  if (wave >= 4) {
    const int hw = wave - 4;
    const rsrc_t rx = mkrsrc(p.x, 2048 * 1024 * 2), rw = mkrsrc(p.wqkv, 1024 * 3072 * 2);
    const int r8 = lane >> 3, c8 = lane & 7;
    u32 vx[2], vw[2];
#pragma unroll
    for (int e = 0; e < 2; ++e) {
      vx[e] = r8 * 2048 + ((c8 ^ (4 * e + (r8 >> 1))) * 16);
      vw[e] = r8 * 6144 + ((c8 ^ ((((r8 >> 1) & 1) | (e << 1)) << 1)) * 16);
    }
    const u32 sx0 = (rb * 128 + hw * 32) * 2048, sw0 = (hw * 16) * 6144 + cb * 128;
    const int rot = 4 * ((rb + cb) & 3);
    auto issue = [&](int s_) {
      const u32 s = (s_ + rot) & 15, base = ((s_ + 1) % 3) * SLOT;
#pragma unroll
      for (int e = 0; e < 4; ++e) dma16(rx, base + (hw * 4 + e) * 1024, vx[e & 1], sx0 + e * 8 * 2048 + s * 128);
#pragma unroll
      for (int sg = 0; sg < 3; ++sg)
#pragma unroll
        for (int e = 0; e < 2; ++e) dma16(rw, base + 16384 + sg * 8192 + (hw * 2 + e) * 1024, vw[e], sw0 + e * 8 * 6144 + sg * 2048 + s * 64 * 6144);
    };
    issue(0); issue(1);
    waitv<10>(); BAR();
    issue(2);
    const u32x2 tag = *(LAS u32x2*)(smem + L_FLAG + 48);
    waitv<10>(); BAR(); issue(3);
    waitv<10>(); BAR(); issue(4);
    const int cg = lane & 15, rg = lane >> 4;
    const int R0 = rb * 128 + hw * 32 + rg * 8;
    u32x2 kk[11], vv[8]; u32x4 tw[2];
    {
      const rsrc_t rk = mkrsrc(p.k, 2048 * 1024 * 2), rv = mkrsrc(p.v, 2048 * 1024 * 2), rs = mkrsrc(p.sk, 1024 * 4 * 2);
      const u32 vo = R0 * 2048 + cb * 128 + cg * 8;
      const u32 vh = R0 == 0 ? 0x40000000u : vo - 3 * 2048;
#pragma unroll
      for (int i = 0; i < 3; ++i) ald64(kk[i], rk, vh, i * 2048);
#pragma unroll
      for (int i = 0; i < 8; ++i) ald64(kk[3 + i], rk, vo, i * 2048);
#pragma unroll
      for (int i = 0; i < 8; ++i) ald64(vv[i], rv, vo, i * 2048);
      ald128(tw[0], rs, cb * 512 + cg * 32, 0); ald128(tw[1], rs, cb * 512 + cg * 32, 16);
    }
    waitv<31>(); BAR(); issue(5);
    waitv<31>(); BAR(); issue(6);
    const u32 kvt_vo = WS_KVT + cb * 262144 + (cg >> 2) * 65536 + (255 - (R0 >> 3)) * 64 + (cg & 3) * 16;
    auto shortconv = [&](int c) {
      float kf[11], wf[4], o[8];
#pragma unroll
      for (int i = 0; i < 11; ++i) kf[i] = (c & 1) ? bhi(kk[i][c >> 1]) : blo(kk[i][c >> 1]);
#pragma unroll
      for (int t = 0; t < 4; ++t) { const u32 d = tw[c >> 1][(c & 1) * 2 + (t >> 1)]; wf[t] = (t & 1) ? bhi(d) : blo(d); }
#pragma unroll
      for (int i = 0; i < 8; ++i) {
        float a = kf[i] * wf[3];
        a = __builtin_fmaf(kf[i + 1], wf[2], a); a = __builtin_fmaf(kf[i + 2], wf[1], a); a = __builtin_fmaf(kf[i + 3], wf[0], a);
        o[i] = a * ((c & 1) ? bhi(vv[i][c >> 1]) : blo(vv[i][c >> 1]));
      }
      st128_sc(u32x4{pk2(o[7], o[6]), pk2(o[5], o[4]), pk2(o[3], o[2]), pk2(o[1], o[0])}, ws, kvt_vo, c * 16384);
    };
    waitv<10>(); BAR(); issue(7);
#pragma unroll
    for (int i = 0; i < 11; ++i) PIN(kk[i]);
#pragma unroll
    for (int i = 0; i < 8; ++i) PIN(vv[i]);
    PIN(tw[0]); PIN(tw[1]);
    shortconv(0); SCHED();
    waitv<11>(); BAR(); issue(8); shortconv(1); SCHED();
    waitv<12>(); BAR(); issue(9); shortconv(2); SCHED();
    waitv<12>(); BAR(); issue(10); shortconv(3); SCHED();
    const int ch = cb * 64 + (rb >> 2) * 16 + hw * 4 + (rb & 3);
    u32x4 hh4[4]; u32x2 dd2;
    {
      const rsrc_t rh = mkrsrc(p.h, 1024 * 2048 * 2), rd = mkrsrc(p.dd, 1024 * 2);
#pragma unroll
      for (int n = 0; n < 4; ++n) ald128(hh4[n], rh, lane * 16, ch * 4096 + n * 1024);
      ald64(dd2, rd, 0, (ch >> 2) * 8);
    }
    waitv<17>(); BAR(); issue(11);
    waitv<16>(); BAR();
    publish(ws, WS_FA + ((cb * 16 + rb) * 4 + hw) * 8, tag, lane);
    issue(12);
    waitv<10>(); BAR(); issue(13);
    waitv<10>(); BAR(); issue(14);
    waitv<10>(); BAR();
    const u32 fa_off = WS_FA + (cb * 16 + (3 - hw) * 4) * 32;
    u32x2 fl;
    ald64_sc(fl, ws, fa_off + (lane & 15) * 8, 0);
    issue(15);
    waitv<10>(); PIN(fl);
    if (!__all(fl.x == tag.x && fl.y == tag.y)) await(ws, fa_off, 15, tag, lane);
    u32x4 xs[4];
#pragma unroll
    for (int n = 0; n < 4; ++n) ald128_sc(xs[n], ws, lane * 16, WS_KVT + cb * 262144 + rb * 16384 + (hw * 4 + n) * 1024);
    BAR();
    LAS char* cr = smem + L_CONV + hw * CH_STRIDE;
#pragma unroll
    for (int n = 0; n < 4; ++n) PIN(hh4[n]);
    PIN(dd2);
    {
      const u32x4 z4 = {0, 0, 0, 0};
      if (lane < 30) *(LAS u32x4*)(cr + 4096 + lane * 16) = z4;
      if (lane < 4) *(LAS u32x4*)(cr + C_O + (lane >> 1) * (C_E - C_O) + (lane & 1) * 16) = z4;
#pragma unroll
      for (int n = 0; n < 4; ++n) *(LAS u32x4*)(cr + C_O + 32 + n * 1024 + lane * 16) = hh4[n];
      waitl();
#pragma unroll
      for (int n = 0; n < 4; ++n) {
        const u32 dp = *(LAS u32*)(cr + C_O + 28 + n * 1024 + lane * 16);
        const u32x4 d = hh4[n];
        *(LAS u32x4*)(cr + C_E + 32 + n * 1024 + lane * 16) =
            u32x4{__builtin_amdgcn_alignbit(d[0], dp, 16), __builtin_amdgcn_alignbit(d[1], d[0], 16),
                  __builtin_amdgcn_alignbit(d[2], d[1], 16), __builtin_amdgcn_alignbit(d[3], d[2], 16)};
      }
    }
    waitv<4>(); BAR();
    waitv<0>();
#pragma unroll
    for (int n = 0; n < 4; ++n) PIN(xs[n]);
#pragma unroll
    for (int n = 0; n < 4; ++n) *(LAS u32x4*)(smem + L_CONV + (lane & 3) * CH_STRIDE + ((hw * 4 + n) * 16 + (lane >> 2)) * 16) = xs[n];
    waitl(); BAR();
    STAMP(1);
    const int R2 = rb * 128 + ((hw >> 1) * 4 + rg) * 16 + (hw & 1) * 8;
    u32x2 qq[8];
    {
      const rsrc_t rq = mkrsrc(p.q, 2048 * 1024 * 2);
#pragma unroll
      for (int i = 0; i < 8; ++i) ald64(qq[i], rq, R2 * 2048 + cb * 128 + cg * 8, i * 2048);
    }
    const rsrc_t rwo = mkrsrc(p.wo, 1024 * 1024 * 2);
    const u32 vwo = r8 * 2048 + ((c8 ^ (((r8 >> 1) & 3) << 1)) * 16);
    const int b16 = lane & 15, kg = lane >> 4;
    LAS char* ha = cr + ((b16 & 1) ? C_O : C_E) + 4 + 4 * (b16 >> 1) + 16 * kg;
    LAS char* xb = cr + (254 - 224 - 2 * b16 + kg) * 16;
    f32x4 acc[8];
#pragma unroll
    for (int n = 0; n < 8; ++n) acc[n] = f32x4{0, 0, 0, 0};
    CFr C0, C1;
    ldconv(C0, ha, xb);
#pragma unroll 1
    for (int it = 0; it < 3; ++it) {
#pragma unroll
      for (int e = 0; e < 4; ++e) { const int st = it * 8 + hw; dma16(rwo, st * 4096 + e * 1024, vwo, ((st ^ hx) * 32 + e * 8) * 2048 + cb2 * 128); }
      SCHED();
      ldconv(C1, ha + it * 128 + 64, xb + it * 128 + 64);
      mmconv(acc, C0); interleave_conv(); SCHED();
#pragma unroll
      for (int e = 0; e < 4; ++e) { const int st = it * 8 + 4 + hw; dma16(rwo, st * 4096 + e * 1024, vwo, ((st ^ hx) * 32 + e * 8) * 2048 + cb2 * 128); }
      SCHED();
      ldconv(C0, ha + it * 128 + 128, xb + it * 128 + 128);
      mmconv(acc, C1); interleave_conv(); SCHED();
    }
    ldconv(C1, ha + 448, xb + 448);
    mmconv(acc, C0); interleave_conv(); SCHED();
    u32x2 xv[8];
#pragma unroll
    for (int n = 0; n < 8; ++n) xv[n] = *(LAS u32x2*)(cr + (2044 - 256 * n - 16 * b16 - 4 * kg) * 2);
    mmconv(acc, C1);
    SCHED();
    waitv<0>();
    lds_signal(smem, 8, lane);
    STAMP(2);
#pragma unroll
    for (int e = 0; e < 8; ++e) { const int st = 24 + hw * 2 + (e >> 2); dma16(rwo, L_CONV + hw * CH_STRIDE + e * 1024, vwo, ((st ^ hx) * 32 + (e & 3) * 8) * 2048 + cb2 * 128); }
    {
      const u32 dw = (ch & 2) ? dd2.y : dd2.x;
      const float dv = (ch & 1) ? bhi(dw) : blo(dw);
      u32x2 y[8];
#pragma unroll
      for (int n = 0; n < 8; ++n)
        y[n] = u32x2{pk2(__builtin_fmaf(bhi(xv[n].y), dv, acc[n][0]), __builtin_fmaf(blo(xv[n].y), dv, acc[n][1])),
                     pk2(__builtin_fmaf(bhi(xv[n].x), dv, acc[n][2]), __builtin_fmaf(blo(xv[n].x), dv, acc[n][3]))};
      const u32 yvo = WS_YT + cb * 262144 + ((kg & 1) * 2 + (b16 >> 3)) * 16384 + (ch & 63) * 256 + (kg >> 1) * 128 + (b16 & 7) * 16;
#pragma unroll
      for (int n = 0; n < 8; n += 2) {
        auto s0 = __builtin_amdgcn_permlane16_swap(y[n].x, y[n + 1].x, false, false);
        auto s1 = __builtin_amdgcn_permlane16_swap(y[n].y, y[n + 1].y, false, false);
        st128_sc(u32x4{s0[0], s1[0], s0[1], s1[1]}, ws, yvo, n * 32768);
      }
    }
    publish(ws, WS_FY + ((cb * 16 + rb) * 4 + hw) * 8, tag, lane);
    lds_signal(smem, 9, lane);
    STAMP(3);
    await(ws, WS_FY + cb * 512, 63, tag, lane);
    STAMP(4);
    {
      u32x4 yy[4];
      const u32 yo = WS_YT + cb * 262144 + rb * 16384 + (lane >> 2) * 1024 + (hw & 1) * 128 + ((hw >> 1) * 4 + (lane & 3)) * 16;
#pragma unroll
      for (int c = 0; c < 4; ++c) yy[c] = ld128_sc(ws, yo, c * 256);
      LAS char* sc = cr + 8192;
#pragma unroll
      for (int c = 0; c < 4; ++c) *(LAS u32x4*)(sc + c * 1024 + lane * 16) = yy[c];
      asm volatile("" ::: "memory");
#pragma unroll
      for (int c = 0; c < 4; ++c) yy[c] = *(LAS u32x4*)(sc + c * 1024 + (cg * 4 + rg) * 16);
#pragma unroll
      for (int i = 0; i < 8; ++i) PIN(qq[i]);
      const rsrc_t ro = mkrsrc(p.out, 2048 * 2048 * 2);
#pragma unroll
      for (int i = 0; i < 8; ++i) {
        float f[4];
#pragma unroll
        for (int c = 0; c < 4; ++c) {
          const float yv = (i & 1) ? bhi(yy[c][i >> 1]) : blo(yy[c][i >> 1]);
          const float qv = (c & 1) ? bhi(qq[i][c >> 1]) : blo(qq[i][c >> 1]);
          f[c] = yv * qv;
        }
        __builtin_amdgcn_raw_buffer_store_b64(u32x2{pk2(f[0], f[1]), pk2(f[2], f[3])}, ro, R2 * 4096 + cb * 128 + cg * 8, i * 4096, 0);
      }
    }
    STAMP(5);
    STAMP_OUT();
    return;
  }

  __builtin_amdgcn_s_setprio(1);
  u32x2 tag;
  {
    tag = u32x2{1u, 0u};
    if (wave == 0 && lane < 16) ((LAS u32*)(smem + L_FLAG))[lane] = lane == 12 ? tag.x : lane == 13 ? tag.y : 0u;
    waitl();
  }
  const int mh = wave & 1, hh = wave >> 1;
  const int r16 = lane & 15, g = lane >> 4, q4 = r16 >> 2, p4 = lane & 3;
  f32x4 acc[4][6];
  {
    const int xo = (64 * mh + r16) * 128 + ((g ^ (r16 >> 1)) * 16);
    const int f = ((q4 >> 1) & 1) | ((g & 1) << 1);
    const int wo = 16384 + (8 * g + q4) * 128 + (((4 * hh + (p4 >> 1)) ^ (f << 1)) * 16) + 8 * (p4 & 1);
#pragma unroll
    for (int a = 0; a < 4; ++a)
#pragma unroll
      for (int b = 0; b < 6; ++b) { const float i0 = b < 4 ? EPS : 0.f; acc[a][b] = f32x4{i0, i0, i0, i0}; PIN(acc[a][b]); }
    Frags F0, F1;
    BAR();
    STAMP(1);
    int so = SLOT;
    ldfr(F0, smem + so + xo, smem + so + wo, smem + so + (wo ^ 32), 0);
#pragma unroll 1
    for (int s = 0; s < 15; ++s) {
      ldfr(F1, smem + so + (xo ^ 64), smem + so + wo, smem + so + (wo ^ 32), 1);
      mm(acc, F0); interleave_b1(); SCHED();
      waitl(); BAR(); SCHED();
      so = (so == 2 * SLOT) ? 0 : so + SLOT;
      ldfr(F0, smem + so + xo, smem + so + wo, smem + so + (wo ^ 32), 0);
      mm(acc, F1); interleave_b1(); SCHED();
    }
    ldfr(F1, smem + so + (xo ^ 64), smem + so + wo, smem + so + (wo ^ 32), 1);
    mm(acc, F0); interleave_b1(); SCHED();
    waitl(); BAR(); SCHED();
    mm(acc, F1);
  }
  SCHED();
  STAMP(2);
  const u32 pslot = (cb * 2 + hh) * 2 + mh;
  {
    f32x4 kvp[2][2], zp[2];
    const f32x4 zero = {0, 0, 0, 0};
    const u32x4 ones = {0x3f803f80u, 0x3f803f80u, 0x3f803f80u, 0x3f803f80u};
#pragma unroll
    for (int u = 0; u < 2; ++u) {
      u32x4 pkf[2], vf[2];
#pragma unroll
      for (int t = 0; t < 2; ++t) { pkf[t] = pack_phi(acc[2 * u][2 + t], acc[2 * u + 1][2 + t]); vf[t] = pack8(acc[2 * u][4 + t], acc[2 * u + 1][4 + t]); }
#pragma unroll
      for (int dt = 0; dt < 2; ++dt) {
#pragma unroll
        for (int et = 0; et < 2; ++et) kvp[dt][et] = mfma(pkf[dt], vf[et], u ? kvp[dt][et] : zero);
        zp[dt] = mfma(pkf[dt], ones, u ? zp[dt] : zero);
      }
    }
    LAS char* mine = smem + L_X1 + wave * 2304;
    LAS char* theirs = smem + L_X1 + (wave ^ 1) * 2304;
    f32x4 keep[2];
#pragma unroll
    for (int dt = 0; dt < 2; ++dt) {
      keep[dt] = mh ? kvp[dt][1] : kvp[dt][0];
      *(LAS f32x4*)(mine + dt * 1024 + lane * 16) = mh ? kvp[dt][0] : kvp[dt][1];
      if (r16 == 0) *(LAS f32x4*)(mine + 2048 + dt * 64 + g * 16) = zp[dt];
    }
    lds_signal(smem, wave, lane);
    lds_await(smem, wave ^ 1, 1);
#pragma unroll
    for (int dt = 0; dt < 2; ++dt) {
      keep[dt] += *(LAS f32x4*)(theirs + dt * 1024 + lane * 16);
      zp[dt] += *(LAS f32x4*)(theirs + 2048 + dt * 64 + g * 16);
      st128_sc(__builtin_bit_cast(u32x4, keep[dt]), ws, lane * 16, WS_PART + pslot * 32768 + rb * 2048 + dt * 1024);
    }
    if (r16 < 2) st128_sc(__builtin_bit_cast(u32x4, r16 ? zp[1] : zp[0]), ws, r16 * 64 + g * 16, WS_Z + pslot * 2048 + rb * 128);
  }
  SCHED();
  u32x4 pq[4];
#pragma unroll
  for (int mt = 0; mt < 4; ++mt) { pq[mt] = pack_phi(acc[mt][0], acc[mt][1]); PIN(pq[mt]); }
  SCHED();
  publish(ws, WS_F1 + ((cb * 4 + wave) * 16 + rb) * 8, tag, lane);
  STAMP(3);
  await(ws, WS_F1 + (cb * 4 + wave) * 128, 15, tag, lane);
  STAMP(4);
  {
    u32x4 pr[16][2], zr[2];
#pragma unroll
    for (int dt = 0; dt < 2; ++dt) zr[dt] = ld128_sc(ws, r16 * 128 + dt * 64 + g * 16, WS_Z + pslot * 2048);
#pragma unroll
    for (int r = 0; r < 16; ++r)
#pragma unroll
      for (int dt = 0; dt < 2; ++dt) pr[r][dt] = ld128_sc(ws, lane * 16 + ((r & 1) * 2 + dt) * 1024, WS_PART + pslot * 32768 + (r >> 1) * 4096);
    SCHED();
    f32x4 zs[2], sum[2];
#pragma unroll
    for (int dt = 0; dt < 2; ++dt) {
#pragma unroll
      for (int r = 0; r < 4; ++r) zs[dt][r] = row_allsum(__uint_as_float(zr[dt][r]));
      sum[dt] = __builtin_bit_cast(f32x4, pr[0][dt]);
    }
#pragma unroll
    for (int r = 1; r < 16; ++r)
#pragma unroll
      for (int dt = 0; dt < 2; ++dt) sum[dt] += __builtin_bit_cast(f32x4, pr[r][dt]);
    const u32x4 zf = pack8(zs[0], zs[1]);
    *(LAS u32x4*)(smem + L_X2 + wave * 1024 + lane * 16) = pack8(sum[0], sum[1]);
    lds_signal(smem, 4 + wave, lane);
    lds_await(smem, 4 + (wave ^ 1), 1);
    const u32x4 kv0 = *(LAS u32x4*)(smem + L_X2 + hh * 2048 + lane * 16);
    const u32x4 kv1 = *(LAS u32x4*)(smem + L_X2 + hh * 2048 + 1024 + lane * 16);
    const f32x4 zero = {0, 0, 0, 0}, eps4 = {EPS, EPS, EPS, EPS};
#pragma unroll
    for (int mt = 0; mt < 4; ++mt) {
      const f32x4 den = mfma(zf, pq[mt], eps4);
      const f32x4 n0 = mfma(kv0, pq[mt], zero), n1 = mfma(kv1, pq[mt], zero);
      const float inv = __builtin_amdgcn_rcpf(den[0]);
      st128_sc(pack8(n0 * inv, n1 * inv), ws, lane * 16 + (mt & 1) * 1024, WS_O + rb * 262144 + (mh * 2 + (mt >> 1)) * 65536 + (cb * 2 + hh) * 2048);
    }
  }
  STAMP(5);
  publish(ws, WS_F2 + (((rb * 2 + mh) * 16 + cb) * 2 + hh) * 8, tag, lane);
  STAMP(6);
  await(ws, WS_F2 + (rb2 * 2 + (wave >> 1)) * 256, 31, tag, lane);
  STAMP(7);
  {
    constexpr int WIN = 16;
    u32x4 of[WIN][2];
    const u32 ob = WS_O + rb2 * 262144 + wave * 65536;
    auto ldo = [&](int st) {
#pragma unroll
      for (int m = 0; m < 2; ++m) of[st % WIN][m] = ld128_sc(ws, lane * 16 + ((st & 1) * 2 + m) * 1024, ob + (((st & ~1) ^ hx) * 2048));
    };
#pragma unroll
    for (int st = 0; st < WIN; ++st) ldo(st);
    SCHED();
    lds_await(smem, 8, 4);
    const int fw = (2 * g + (q4 >> 1)) & 3;
    int wb[4];
#pragma unroll
    for (int t = 0; t < 4; ++t) wb[t] = (4 * g + q4) * 128 + ((t ^ fw) * 32) + p4 * 8;
    auto ldw = [&](u32x4 (&w)[4], int st) {
      const int base = st < 24 ? st * 4096 : L_CONV + ((st - 24) >> 1) * CH_STRIDE + (st & 1) * 4096;
#pragma unroll
      for (int t = 0; t < 4; ++t) w[t] = tr_frag(smem + base + wb[t], 2048);
    };
    f32x4 oc[2][4];
#pragma unroll
    for (int m = 0; m < 2; ++m)
#pragma unroll
      for (int t = 0; t < 4; ++t) oc[m][t] = f32x4{0, 0, 0, 0};
    u32x4 wf[2][4];
    ldw(wf[0], 0);
#pragma unroll
    for (int st = 0; st < 32; ++st) {
      if (st == 23) lds_await(smem, 9, 4);
      if (st < 31) ldw(wf[(st + 1) & 1], st + 1);
#pragma unroll
      for (int t = 0; t < 4; ++t)
#pragma unroll
        for (int m = 0; m < 2; ++m) oc[m][t] = mfma(wf[st & 1][t], of[st % WIN][m], oc[m][t]);
#pragma unroll
      for (int i = 0; i < 8; ++i) { __builtin_amdgcn_sched_group_barrier(0x8, 1, 0); __builtin_amdgcn_sched_group_barrier(0x100, 1, 0); }
      SCHED();
      if (st + WIN < 32) { ldo(st + WIN); SCHED(); }
    }
    STAMP(8);
    const rsrc_t ro = mkrsrc(p.out, 2048 * 2048 * 2);
    LAS char* sc = smem + L_X1 + (wave ^ 1) * 2304;
#pragma unroll
    for (int m = 0; m < 2; ++m) {
#pragma unroll
      for (int t = 0; t < 4; ++t) *(LAS u32x2*)(sc + r16 * 144 + t * 32 + g * 8) = u32x2{pk2(oc[m][t][0], oc[m][t][1]), pk2(oc[m][t][2], oc[m][t][3])};
      asm volatile("" ::: "memory");
#pragma unroll
      for (int e = 0; e < 2; ++e) {
        const u32x4 r = *(LAS u32x4*)(sc + (e * 8 + (lane >> 3)) * 144 + (lane & 7) * 16);
        __builtin_amdgcn_raw_buffer_store_b128(r, ro, (lane >> 3) * 4096 + (lane & 7) * 16, (rb2 * 128 + wave * 32 + m * 16 + e * 8) * 4096 + 2048 + cb2 * 128, 0);
      }
      asm volatile("" ::: "memory");
    }
  }
  STAMP(9);
  STAMP_OUT();
}

extern "C" void kernel_launch(void* const* d_in, const int* in_sizes, int n_in, void* d_out, int out_size, void* d_ws, size_t ws_size,
                              hipStream_t stream) {
  Params p;
  p.q = (const u16*)d_in[0]; p.k = (const u16*)d_in[1]; p.v = (const u16*)d_in[2]; p.sk = (const u16*)d_in[3];
  p.h = (const u16*)d_in[4]; p.dd = (const u16*)d_in[5]; p.x = (const u16*)d_in[6]; p.wqkv = (const u16*)d_in[7];
  p.wo = (const u16*)d_in[8]; p.out = (u16*)d_out; p.ws = (char*)d_ws;
  (void)hipFuncSetAttribute((const void*)fused_h3_linattn, hipFuncAttributeMaxDynamicSharedMemorySize, LDS_BYTES);
  (void)hipMemsetAsync(d_ws, 0, WS_CTRL_END, stream);
  fused_h3_linattn<<<256, 512, LDS_BYTES, stream>>>(p);
}
```

```cpp
#include <hip/hip_runtime.h>
#include <hip/hip_bf16.h>
#include <cstdio>

typedef unsigned short u16;
typedef unsigned int u32;
typedef unsigned long long u64;
typedef float f32x2 __attribute__((ext_vector_type(2)));
typedef float f32x4 __attribute__((ext_vector_type(4)));
typedef u32 u32x2 __attribute__((ext_vector_type(2)));
typedef u32 u32x4 __attribute__((ext_vector_type(4)));
typedef short s16x4 __attribute__((ext_vector_type(4)));
typedef short s16x8 __attribute__((ext_vector_type(8)));
typedef __bf16 bf16x2 __attribute__((ext_vector_type(2)));
typedef __bf16 bf16x8 __attribute__((ext_vector_type(8)));
typedef __amdgpu_buffer_rsrc_t rsrc_t;
#define LAS __attribute__((address_space(3)))
#define GAS __attribute__((address_space(1)))
#define DI __device__ __forceinline__

#define USE_MEMSET 0
#define CODE_PF 0
#define STAMPS 0

constexpr u32 WS_EPOCH = 0;
constexpr u32 WS_F1 = 4096;
constexpr u32 WS_F2 = WS_F1 + 8192;
constexpr u32 WS_FA = WS_F2 + 8192;
constexpr u32 WS_FY = WS_FA + 8192;
constexpr u32 WS_CTRL_END = WS_FY + 8192;
constexpr u32 WS_Z = 512u << 10;
constexpr u32 WS_PART = 1u << 20;
constexpr u32 WS_O = 4u << 20;
constexpr u32 WS_KVT = 8u << 20;
constexpr u32 WS_YT = 12u << 20;
constexpr u32 WS_BYTES = 16u << 20;
constexpr u32 WS_DBG = 32u << 20;

constexpr int SLOT = 40960;
constexpr int L_X1 = 98304;
constexpr int L_X2 = L_X1 + 4 * 2304;
constexpr int CH_STRIDE = 12960;
constexpr int L_CONV = 163840 - 4 * CH_STRIDE;
constexpr int C_O = 4608, C_E = C_O + 4160;
constexpr int L_FLAG = 163840 - 64;
constexpr int LDS_BYTES = 163840;
static_assert(L_X2 + 4096 <= L_CONV, "lds map");

constexpr int AUX_SC1 = 16;
constexpr u32 SPIN_CAP = 1u << 20;

DI f32x4 mfma(u32x4 a, u32x4 b, f32x4 c) {
  return __builtin_amdgcn_mfma_f32_16x16x32_bf16(__builtin_bit_cast(bf16x8, a), __builtin_bit_cast(bf16x8, b), c, 0, 0, 0);
}
DI u32 pk2(float lo, float hi) {
  bf16x2 r = __builtin_convertvector(f32x2{lo, hi}, bf16x2);
  return __builtin_bit_cast(u32, r);
}
DI float blo(u32 x) { return __uint_as_float(x << 16); }
DI float bhi(u32 x) { return __uint_as_float(x & 0xffff0000u); }
template <int N> DI void waitv() { asm volatile("s_waitcnt vmcnt(%0)" ::"n"(N) : "memory"); }
DI void waitl() { asm volatile("s_waitcnt lgkmcnt(0)" ::: "memory"); }
#define BAR() do { asm volatile("" ::: "memory"); __builtin_amdgcn_s_barrier(); asm volatile("" ::: "memory"); } while (0)
#define SCHED() __builtin_amdgcn_sched_barrier(0)
DI rsrc_t mkrsrc(const void* p, u32 bytes) { return __builtin_amdgcn_make_buffer_rsrc((void*)p, 0, bytes, 0x20000); }
DI void dma16(rsrc_t r, u32 lds, u32 voff, u32 soff) {
  asm volatile("s_mov_b32 m0, %0\n\ts_nop 0\n\tbuffer_load_dwordx4 %1, %2, %3 offen lds" ::"s"(lds), "v"(voff), "s"(r), "s"(soff) : "memory");
}
DI void ald64(u32x2& d, rsrc_t r, u32 voff, u32 soff) {
  asm volatile("buffer_load_dwordx2 %0, %1, %2, %3 offen" : "=v"(d) : "v"(voff), "s"(r), "s"(soff) : "memory");
}
DI void ald64_sc(u32x2& d, rsrc_t r, u32 voff, u32 soff) {
  asm volatile("buffer_load_dwordx2 %0, %1, %2, %3 offen sc1" : "=v"(d) : "v"(voff), "s"(r), "s"(soff) : "memory");
}
DI void ald128(u32x4& d, rsrc_t r, u32 voff, u32 soff) {
  asm volatile("buffer_load_dwordx4 %0, %1, %2, %3 offen" : "=v"(d) : "v"(voff), "s"(r), "s"(soff) : "memory");
}
DI void ald128_sc(u32x4& d, rsrc_t r, u32 voff, u32 soff) {
  asm volatile("buffer_load_dwordx4 %0, %1, %2, %3 offen sc1" : "=v"(d) : "v"(voff), "s"(r), "s"(soff) : "memory");
}
#define PIN(x) asm volatile("" : "+v"(x))
DI u32x4 ld128_sc(rsrc_t r, u32 voff, u32 soff) { return __builtin_amdgcn_raw_buffer_load_b128(r, voff, soff, AUX_SC1); }
DI void st128_sc(u32x4 v, rsrc_t r, u32 voff, u32 soff) { __builtin_amdgcn_raw_buffer_store_b128(v, r, voff, soff, AUX_SC1); }

DI void publish(rsrc_t ws, u32 off, u32x2 tag, int lane) {
  waitv<0>();
  if (lane == 0) __builtin_amdgcn_raw_buffer_store_b64(tag, ws, off, 0, AUX_SC1);
}
DI void await(rsrc_t ws, u32 off, int mask, u32x2 tag, int lane) {
  const u32 vo = off + 8u * (lane & mask);
#pragma unroll 1
  for (u32 spins = 0; spins < SPIN_CAP; ++spins) {
    u32x2 f;
    asm volatile("buffer_load_dwordx2 %0, %1, %2, 0 offen sc1\n\ts_waitcnt vmcnt(0)" : "=&v"(f) : "v"(vo), "s"(ws) : "memory");
    if (__all(f.x == tag.x && f.y == tag.y)) break;
    __builtin_amdgcn_s_sleep(1);
  }
  asm volatile("" ::: "memory");
}
DI void lds_signal(LAS char* smem, int word, int lane) {
  waitl();
  if (lane == 0) __hip_atomic_fetch_add((LAS u32*)(smem + L_FLAG) + word, 1u, __ATOMIC_RELAXED, __HIP_MEMORY_SCOPE_WORKGROUP);
}
DI void lds_await(LAS char* smem, int word, u32 want) {
  for (u32 spins = 0; spins < (SPIN_CAP << 3); ++spins) {
    const u32 v = __hip_atomic_load((LAS u32*)(smem + L_FLAG) + word, __ATOMIC_RELAXED, __HIP_MEMORY_SCOPE_WORKGROUP);
    if (__builtin_amdgcn_readfirstlane(v) >= want) break;
  }
  asm volatile("" ::: "memory");
}

#define STAMP(i)
#define STAMP_OUT()

struct Params {
  const u16 *q, *k, *v, *sk, *h, *dd, *x, *wqkv, *wo;
  u16* out;
  char* ws;
};

struct Frags { u32x4 x[4]; u32x4 w[6]; };
DI u32x4 tr_frag(LAS char* a, int d) {
  s16x4 lo = __builtin_amdgcn_ds_read_tr16_b64_v4i16((LAS s16x4*)a);
  s16x4 hi = __builtin_amdgcn_ds_read_tr16_b64_v4i16((LAS s16x4*)(a + d));
  return __builtin_bit_cast(u32x4, __builtin_shufflevector(lo, hi, 0, 1, 2, 3, 4, 5, 6, 7));
}
DI void ldfr(Frags& f, LAS char* xa, LAS char* wb0, LAS char* wb1, int ks) {
#pragma unroll
  for (int mt = 0; mt < 4; ++mt) f.x[mt] = *(LAS u32x4*)(xa + mt * 2048);
#pragma unroll
  for (int sg = 0; sg < 3; ++sg)
#pragma unroll
    for (int t = 0; t < 2; ++t) f.w[sg * 2 + t] = tr_frag((t ? wb1 : wb0) + sg * 8192 + ks * 4096, 512);
}
DI void mm(f32x4 (&acc)[4][6], const Frags& f) {
#pragma unroll
  for (int n = 0; n < 6; ++n)
#pragma unroll
    for (int m_ = 0; m_ < 4; ++m_) {
      const int mt = (n & 1) ? 3 - m_ : m_;
      if (n < 2) acc[mt][n] = mfma(f.w[n], f.x[mt], acc[mt][n]);
      else acc[mt][n] = mfma(f.x[mt], f.w[n], acc[mt][n]);
    }
}
DI void interleave_b1() {
#pragma unroll
  for (int i = 0; i < 16; ++i) { __builtin_amdgcn_sched_group_barrier(0x8, 1, 0); __builtin_amdgcn_sched_group_barrier(0x100, 1, 0); }
  __builtin_amdgcn_sched_group_barrier(0x8, 8, 0);
}
constexpr float EPS = 1e-6f;
DI float phi(float t) {
  const float e = __builtin_amdgcn_fmed3f(__builtin_amdgcn_exp2f(__builtin_fmaf(t, 1.44269504088896f, -EPS * 1.44269504088896f)), 0.f, 1.f);
  return e + __builtin_fmaxf(t, EPS);
}
DI u32x4 pack_phi(f32x4 a, f32x4 b) { return u32x4{pk2(phi(a[0]), phi(a[1])), pk2(phi(a[2]), phi(a[3])), pk2(phi(b[0]), phi(b[1])), pk2(phi(b[2]), phi(b[3]))}; }
DI u32x4 pack8(f32x4 a, f32x4 b) { return u32x4{pk2(a[0], a[1]), pk2(a[2], a[3]), pk2(b[0], b[1]), pk2(b[2], b[3])}; }
template <int CTRL> DI float dpp_add(float v) {
  return v + __int_as_float(__builtin_amdgcn_update_dpp(0, __float_as_int(v), CTRL, 0xf, 0xf, false));
}
DI float row_allsum(float v) { v = dpp_add<0x128>(v); v = dpp_add<0x124>(v); v = dpp_add<0x122>(v); v = dpp_add<0x121>(v); return v; }

struct CFr { u32x4 a[8]; u32x4 b[8]; };
DI void ldconv(CFr& f, LAS char* ha, LAS char* xb) {
#pragma unroll
  for (int i = 0; i < 8; ++i) {
    f.b[i] = *(LAS u32x4*)(xb + (7 - i) * 512);
    LAS u32* hp = (LAS u32*)(ha + i * 512);
    f.a[i] = u32x4{hp[0], hp[1], hp[2], hp[3]};
  }
}
DI void mmconv(f32x4 (&acc)[8], const CFr& f) {
#pragma unroll
  for (int n = 0; n < 8; ++n)
#pragma unroll
    for (int q = 0; q <= n; ++q) acc[n] = mfma(f.a[q], f.b[n - q], acc[n]);
}
DI void interleave_conv() {
#pragma unroll
  for (int i = 0; i < 24; ++i) { __builtin_amdgcn_sched_group_barrier(0x8, 1, 0); __builtin_amdgcn_sched_group_barrier(0x100, 1, 0); }
  __builtin_amdgcn_sched_group_barrier(0x8, 12, 0);
}

__global__ __launch_bounds__(512, 2) void fused_h3_linattn(Params p) {
  extern __shared__ __attribute__((aligned(16))) char smem_[];
  LAS char* smem = (LAS char*)smem_;
  const int bid = blockIdx.x, xcd = bid & 7, j = bid >> 3;
  const int rb = (xcd & 1) * 8 + (j & 7), cb = (xcd >> 1) * 4 + (j >> 3);
  const int rb2 = xcd * 2 + (j & 1), cb2 = j >> 1;
  const int hx = (cb2 & 1) * 16;
  const int wave = __builtin_amdgcn_readfirstlane(threadIdx.x >> 6), lane = threadIdx.x & 63;
  const rsrc_t ws = mkrsrc(p.ws, WS_BYTES);
  STAMP(0);

  if (wave >= 4) {
    const int hw = wave - 4;
    const rsrc_t rx = mkrsrc(p.x, 2048 * 1024 * 2), rw = mkrsrc(p.wqkv, 1024 * 3072 * 2);
    const int r8 = lane >> 3, c8 = lane & 7;
    u32 vx[2], vw[2];
#pragma unroll
    for (int e = 0; e < 2; ++e) {
      vx[e] = r8 * 2048 + ((c8 ^ (4 * e + (r8 >> 1))) * 16);
      vw[e] = r8 * 6144 + ((c8 ^ ((((r8 >> 1) & 1) | (e << 1)) << 1)) * 16);
    }
    const u32 sx0 = (rb * 128 + hw * 32) * 2048, sw0 = (hw * 16) * 6144 + cb * 128;
    const int rot = 4 * ((rb + cb) & 3);
    auto issue = [&](int s_) {
      const u32 s = (s_ + rot) & 15, base = ((s_ + 1) % 3) * SLOT;
#pragma unroll
      for (int e = 0; e < 4; ++e) dma16(rx, base + (hw * 4 + e) * 1024, vx[e & 1], sx0 + e * 8 * 2048 + s * 128);
#pragma unroll
      for (int sg = 0; sg < 3; ++sg)
#pragma unroll
        for (int e = 0; e < 2; ++e) dma16(rw, base + 16384 + sg * 8192 + (hw * 2 + e) * 1024, vw[e], sw0 + e * 8 * 6144 + sg * 2048 + s * 64 * 6144);
    };
    issue(0); issue(1);
    waitv<10>(); BAR();
    issue(2);
    const u32x2 tag = *(LAS u32x2*)(smem + L_FLAG + 48);
    waitv<10>(); BAR(); issue(3);
    waitv<10>(); BAR(); issue(4);
    const int cg = lane & 15, rg = lane >> 4;
    const int R0 = rb * 128 + hw * 32 + rg * 8;
    u32x2 kk[11], vv[8]; u32x4 tw[2];
    {
      const rsrc_t rk = mkrsrc(p.k, 2048 * 1024 * 2), rv = mkrsrc(p.v, 2048 * 1024 * 2), rs = mkrsrc(p.sk, 1024 * 4 * 2);
      const u32 vo = R0 * 2048 + cb * 128 + cg * 8;
      const u32 vh = R0 == 0 ? 0x40000000u : vo - 3 * 2048;
#pragma unroll
      for (int i = 0; i < 3; ++i) ald64(kk[i], rk, vh, i * 2048);
#pragma unroll
      for (int i = 0; i < 8; ++i) ald64(kk[3 + i], rk, vo, i * 2048);
#pragma unroll
      for (int i = 0; i < 8; ++i) ald64(vv[i], rv, vo, i * 2048);
      ald128(tw[0], rs, cb * 512 + cg * 32, 0); ald128(tw[1], rs, cb * 512 + cg * 32, 16);
    }
    waitv<31>(); BAR(); issue(5);
    waitv<31>(); BAR(); issue(6);
    const u32 kvt_vo = WS_KVT + cb * 262144 + (cg >> 2) * 65536 + (255 - (R0 >> 3)) * 64 + (cg & 3) * 16;
    auto shortconv = [&](int c) {
      float kf[11], wf[4], o[8];
#pragma unroll
      for (int i = 0; i < 11; ++i) kf[i] = (c & 1) ? bhi(kk[i][c >> 1]) : blo(kk[i][c >> 1]);
#pragma unroll
      for (int t = 0; t < 4; ++t) { const u32 d = tw[c >> 1][(c & 1) * 2 + (t >> 1)]; wf[t] = (t & 1) ? bhi(d) : blo(d); }
#pragma unroll
      for (int i = 0; i < 8; ++i) {
        float a = kf[i] * wf[3];
        a = __builtin_fmaf(kf[i + 1], wf[2], a); a = __builtin_fmaf(kf[i + 2], wf[1], a); a = __builtin_fmaf(kf[i + 3], wf[0], a);
        o[i] = a * ((c & 1) ? bhi(vv[i][c >> 1]) : blo(vv[i][c >> 1]));
      }
      st128_sc(u32x4{pk2(o[7], o[6]), pk2(o[5], o[4]), pk2(o[3], o[2]), pk2(o[1], o[0])}, ws, kvt_vo, c * 16384);
    };
    waitv<10>(); BAR(); issue(7);
#pragma unroll
    for (int i = 0; i < 11; ++i) PIN(kk[i]);
#pragma unroll
    for (int i = 0; i < 8; ++i) PIN(vv[i]);
    PIN(tw[0]); PIN(tw[1]);
    shortconv(0); SCHED();
    waitv<11>(); BAR(); issue(8); shortconv(1); SCHED();
    waitv<12>(); BAR(); issue(9); shortconv(2); SCHED();
    waitv<12>(); BAR(); issue(10); shortconv(3); SCHED();
    const int ch = cb * 64 + (rb >> 2) * 16 + hw * 4 + (rb & 3);
    u32x4 hh4[4]; u32x2 dd2;
    {
      const rsrc_t rh = mkrsrc(p.h, 1024 * 2048 * 2), rd = mkrsrc(p.dd, 1024 * 2);
#pragma unroll
      for (int n = 0; n < 4; ++n) ald128(hh4[n], rh, lane * 16, ch * 4096 + n * 1024);
      ald64(dd2, rd, 0, (ch >> 2) * 8);
    }
    waitv<17>(); BAR(); issue(11);
    waitv<16>(); BAR();
    publish(ws, WS_FA + ((cb * 16 + rb) * 4 + hw) * 8, tag, lane);
    issue(12);
    waitv<10>(); BAR(); issue(13);
    waitv<10>(); BAR(); issue(14);
    waitv<10>(); BAR();
    const u32 fa_off = WS_FA + (cb * 16 + (3 - hw) * 4) * 32;
    u32x2 fl;
    ald64_sc(fl, ws, fa_off + (lane & 15) * 8, 0);
    issue(15);
    waitv<10>(); PIN(fl);
    if (!__all(fl.x == tag.x && fl.y == tag.y)) await(ws, fa_off, 15, tag, lane);
    u32x4 xs[4];
#pragma unroll
    for (int n = 0; n < 4; ++n) ald128_sc(xs[n], ws, lane * 16, WS_KVT + cb * 262144 + rb * 16384 + (hw * 4 + n) * 1024);
    BAR();
    LAS char* cr = smem + L_CONV + hw * CH_STRIDE;
#pragma unroll
    for (int n = 0; n < 4; ++n) PIN(hh4[n]);
    PIN(dd2);
    {
      const u32x4 z4 = {0, 0, 0, 0};
      if (lane < 30) *(LAS u32x4*)(cr + 4096 + lane * 16) = z4;
      if (lane < 4) *(LAS u32x4*)(cr + C_O + (lane >> 1) * (C_E - C_O) + (lane & 1) * 16) = z4;
#pragma unroll
      for (int n = 0; n < 4; ++n) *(LAS u32x4*)(cr + C_O + 32 + n * 1024 + lane * 16) = hh4[n];
      waitl();
#pragma unroll
      for (int n = 0; n < 4; ++n) {
        const u32 dp = *(LAS u32*)(cr + C_O + 28 + n * 1024 + lane * 16);
        const u32x4 d = hh4[n];
        *(LAS u32x4*)(cr + C_E + 32 + n * 1024 + lane * 16) =
            u32x4{__builtin_amdgcn_alignbit(d[0], dp, 16), __builtin_amdgcn_alignbit(d[1], d[0], 16),
                  __builtin_amdgcn_alignbit(d[2], d[1], 16), __builtin_amdgcn_alignbit(d[3], d[2], 16)};
      }
    }
    waitv<4>(); BAR();
    waitv<0>();
#pragma unroll
    for (int n = 0; n < 4; ++n) PIN(xs[n]);
#pragma unroll
    for (int n = 0; n < 4; ++n) *(LAS u32x4*)(smem + L_CONV + (lane & 3) * CH_STRIDE + ((hw * 4 + n) * 16 + (lane >> 2)) * 16) = xs[n];
    waitl(); BAR();
    STAMP(1);
    const int R2 = rb * 128 + ((hw >> 1) * 4 + rg) * 16 + (hw & 1) * 8;
    u32x2 qq[8];
    {
      const rsrc_t rq = mkrsrc(p.q, 2048 * 1024 * 2);
#pragma unroll
      for (int i = 0; i < 8; ++i) ald64(qq[i], rq, R2 * 2048 + cb * 128 + cg * 8, i * 2048);
    }
    const rsrc_t rwo = mkrsrc(p.wo, 1024 * 1024 * 2);
    const u32 vwo = r8 * 2048 + ((c8 ^ (((r8 >> 1) & 3) << 1)) * 16);
    const int b16 = lane & 15, kg = lane >> 4;
    LAS char* ha = cr + ((b16 & 1) ? C_O : C_E) + 4 + 4 * (b16 >> 1) + 16 * kg;
    LAS char* xb = cr + (254 - 224 - 2 * b16 + kg) * 16;
    f32x4 acc[8];
#pragma unroll
    for (int n = 0; n < 8; ++n) acc[n] = f32x4{0, 0, 0, 0};
    CFr C0, C1;
    ldconv(C0, ha, xb);
#pragma unroll 1
    for (int it = 0; it < 3; ++it) {
#pragma unroll
      for (int e = 0; e < 4; ++e) { const int st = it * 8 + hw; dma16(rwo, st * 4096 + e * 1024, vwo, ((st ^ hx) * 32 + e * 8) * 2048 + cb2 * 128); }
      SCHED();
      ldconv(C1, ha + it * 128 + 64, xb + it * 128 + 64);
      mmconv(acc, C0); interleave_conv(); SCHED();
#pragma unroll
      for (int e = 0; e < 4; ++e) { const int st = it * 8 + 4 + hw; dma16(rwo, st * 4096 + e * 1024, vwo, ((st ^ hx) * 32 + e * 8) * 2048 + cb2 * 128); }
      SCHED();
      ldconv(C0, ha + it * 128 + 128, xb + it * 128 + 128);
      mmconv(acc, C1); interleave_conv(); SCHED();
    }
    ldconv(C1, ha + 448, xb + 448);
    mmconv(acc, C0); interleave_conv(); SCHED();
    u32x2 xv[8];
#pragma unroll
    for (int n = 0; n < 8; ++n) xv[n] = *(LAS u32x2*)(cr + (2044 - 256 * n - 16 * b16 - 4 * kg) * 2);
    mmconv(acc, C1);
    SCHED();
    waitv<0>();
    lds_signal(smem, 8, lane);
    STAMP(2);
#pragma unroll
    for (int e = 0; e < 8; ++e) { const int st = 24 + hw * 2 + (e >> 2); dma16(rwo, L_CONV + hw * CH_STRIDE + e * 1024, vwo, ((st ^ hx) * 32 + (e & 3) * 8) * 2048 + cb2 * 128); }
    {
      const u32 dw = (ch & 2) ? dd2.y : dd2.x;
      const float dv = (ch & 1) ? bhi(dw) : blo(dw);
      u32x2 y[8];
#pragma unroll
      for (int n = 0; n < 8; ++n)
        y[n] = u32x2{pk2(__builtin_fmaf(bhi(xv[n].y), dv, acc[n][0]), __builtin_fmaf(blo(xv[n].y), dv, acc[n][1])),
                     pk2(__builtin_fmaf(bhi(xv[n].x), dv, acc[n][2]), __builtin_fmaf(blo(xv[n].x), dv, acc[n][3]))};
      const u32 yvo = WS_YT + cb * 262144 + ((kg & 1) * 2 + (b16 >> 3)) * 16384 + (ch & 63) * 256 + (kg >> 1) * 128 + (b16 & 7) * 16;
#pragma unroll
      for (int n = 0; n < 8; n += 2) {
        auto s0 = __builtin_amdgcn_permlane16_swap(y[n].x, y[n + 1].x, false, false);
        auto s1 = __builtin_amdgcn_permlane16_swap(y[n].y, y[n + 1].y, false, false);
        st128_sc(u32x4{s0[0], s1[0], s0[1], s1[1]}, ws, yvo, n * 32768);
      }
    }
    publish(ws, WS_FY + ((cb * 16 + rb) * 4 + hw) * 8, tag, lane);
    lds_signal(smem, 9, lane);
    STAMP(3);
    await(ws, WS_FY + cb * 512, 63, tag, lane);
    STAMP(4);
    {
      u32x4 yy[4];
      const u32 yo = WS_YT + cb * 262144 + rb * 16384 + (lane >> 2) * 1024 + (hw & 1) * 128 + ((hw >> 1) * 4 + (lane & 3)) * 16;
#pragma unroll
      for (int c = 0; c < 4; ++c) yy[c] = ld128_sc(ws, yo, c * 256);
      LAS char* sc = cr + 8192;
#pragma unroll
      for (int c = 0; c < 4; ++c) *(LAS u32x4*)(sc + c * 1024 + lane * 16) = yy[c];
      asm volatile("" ::: "memory");
#pragma unroll
      for (int c = 0; c < 4; ++c) yy[c] = *(LAS u32x4*)(sc + c * 1024 + (cg * 4 + rg) * 16);
#pragma unroll
      for (int i = 0; i < 8; ++i) PIN(qq[i]);
      const rsrc_t ro = mkrsrc(p.out, 2048 * 2048 * 2);
#pragma unroll
      for (int i = 0; i < 8; ++i) {
        float f[4];
#pragma unroll
        for (int c = 0; c < 4; ++c) {
          const float yv = (i & 1) ? bhi(yy[c][i >> 1]) : blo(yy[c][i >> 1]);
          const float qv = (c & 1) ? bhi(qq[i][c >> 1]) : blo(qq[i][c >> 1]);
          f[c] = yv * qv;
        }
        __builtin_amdgcn_raw_buffer_store_b64(u32x2{pk2(f[0], f[1]), pk2(f[2], f[3])}, ro, R2 * 4096 + cb * 128 + cg * 8, i * 4096, 0);
      }
    }
    STAMP(5);
    STAMP_OUT();
    return;
  }

  __builtin_amdgcn_s_setprio(1);
  u32x2 tag;
  {
    const u32x2 e = __builtin_amdgcn_raw_buffer_load_b64(ws, WS_EPOCH, 0, AUX_SC1);
    const u64 t = (((u64)e.y << 32) | e.x) * 6364136223846793005ull + 1442695040888963407ull;
    tag = u32x2{(u32)t, (u32)(t >> 32)};
    if (wave == 0 && lane < 16) ((LAS u32*)(smem + L_FLAG))[lane] = lane == 12 ? tag.x : lane == 13 ? tag.y : 0u;
    waitl();
  }
  const int mh = wave & 1, hh = wave >> 1;
  const int r16 = lane & 15, g = lane >> 4, q4 = r16 >> 2, p4 = lane & 3;
  f32x4 acc[4][6];
  {
    const int xo = (64 * mh + r16) * 128 + ((g ^ (r16 >> 1)) * 16);
    const int f = ((q4 >> 1) & 1) | ((g & 1) << 1);
    const int wo = 16384 + (8 * g + q4) * 128 + (((4 * hh + (p4 >> 1)) ^ (f << 1)) * 16) + 8 * (p4 & 1);
#pragma unroll
    for (int a = 0; a < 4; ++a)
#pragma unroll
      for (int b = 0; b < 6; ++b) { const float i0 = b < 4 ? EPS : 0.f; acc[a][b] = f32x4{i0, i0, i0, i0}; PIN(acc[a][b]); }
    Frags F0, F1;
    BAR();
    STAMP(1);
    int so = SLOT;
    ldfr(F0, smem + so + xo, smem + so + wo, smem + so + (wo ^ 32), 0);
#pragma unroll 1
    for (int s = 0; s < 15; ++s) {
      ldfr(F1, smem + so + (xo ^ 64), smem + so + wo, smem + so + (wo ^ 32), 1);
      mm(acc, F0); interleave_b1(); SCHED();
      waitl(); BAR(); SCHED();
      so = (so == 2 * SLOT) ? 0 : so + SLOT;
      ldfr(F0, smem + so + xo, smem + so + wo, smem + so + (wo ^ 32), 0);
      mm(acc, F1); interleave_b1(); SCHED();
    }
    ldfr(F1, smem + so + (xo ^ 64), smem + so + wo, smem + so + (wo ^ 32), 1);
    mm(acc, F0); interleave_b1(); SCHED();
    waitl(); BAR(); SCHED();
    mm(acc, F1);
  }
  SCHED();
  STAMP(2);
  const u32 pslot = (cb * 2 + hh) * 2 + mh;
  {
    f32x4 kvp[2][2], zp[2];
    const f32x4 zero = {0, 0, 0, 0};
    const u32x4 ones = {0x3f803f80u, 0x3f803f80u, 0x3f803f80u, 0x3f803f80u};
#pragma unroll
    for (int u = 0; u < 2; ++u) {
      u32x4 pkf[2], vf[2];
#pragma unroll
      for (int t = 0; t < 2; ++t) { pkf[t] = pack_phi(acc[2 * u][2 + t], acc[2 * u + 1][2 + t]); vf[t] = pack8(acc[2 * u][4 + t], acc[2 * u + 1][4 + t]); }
#pragma unroll
      for (int dt = 0; dt < 2; ++dt) {
#pragma unroll
        for (int et = 0; et < 2; ++et) kvp[dt][et] = mfma(pkf[dt], vf[et], u ? kvp[dt][et] : zero);
        zp[dt] = mfma(pkf[dt], ones, u ? zp[dt] : zero);
      }
    }
    LAS char* mine = smem + L_X1 + wave * 2304;
    LAS char* theirs = smem + L_X1 + (wave ^ 1) * 2304;
    f32x4 keep[2];
#pragma unroll
    for (int dt = 0; dt < 2; ++dt) {
      keep[dt] = mh ? kvp[dt][1] : kvp[dt][0];
      *(LAS f32x4*)(mine + dt * 1024 + lane * 16) = mh ? kvp[dt][0] : kvp[dt][1];
      if (r16 == 0) *(LAS f32x4*)(mine + 2048 + dt * 64 + g * 16) = zp[dt];
    }
    lds_signal(smem, wave, lane);
    lds_await(smem, wave ^ 1, 1);
#pragma unroll
    for (int dt = 0; dt < 2; ++dt) {
      keep[dt] += *(LAS f32x4*)(theirs + dt * 1024 + lane * 16);
      zp[dt] += *(LAS f32x4*)(theirs + 2048 + dt * 64 + g * 16);
      st128_sc(__builtin_bit_cast(u32x4, keep[dt]), ws, lane * 16, WS_PART + pslot * 32768 + rb * 2048 + dt * 1024);
    }
    if (r16 < 2) st128_sc(__builtin_bit_cast(u32x4, r16 ? zp[1] : zp[0]), ws, r16 * 64 + g * 16, WS_Z + pslot * 2048 + rb * 128);
  }
  SCHED();
  u32x4 pq[4];
#pragma unroll
  for (int mt = 0; mt < 4; ++mt) { pq[mt] = pack_phi(acc[mt][0], acc[mt][1]); PIN(pq[mt]); }
  SCHED();
  publish(ws, WS_F1 + ((cb * 4 + wave) * 16 + rb) * 8, tag, lane);
  STAMP(3);
  await(ws, WS_F1 + (cb * 4 + wave) * 128, 15, tag, lane);
  STAMP(4);
  {
    u32x4 pr[16][2], zr[2];
#pragma unroll
    for (int dt = 0; dt < 2; ++dt) zr[dt] = ld128_sc(ws, r16 * 128 + dt * 64 + g * 16, WS_Z + pslot * 2048);
#pragma unroll
    for (int r = 0; r < 16; ++r)
#pragma unroll
      for (int dt = 0; dt < 2; ++dt) pr[r][dt] = ld128_sc(ws, lane * 16 + ((r & 1) * 2 + dt) * 1024, WS_PART + pslot * 32768 + (r >> 1) * 4096);
    SCHED();
    f32x4 zs[2], sum[2];
#pragma unroll
    for (int dt = 0; dt < 2; ++dt) {
#pragma unroll
      for (int r = 0; r < 4; ++r) zs[dt][r] = row_allsum(__uint_as_float(zr[dt][r]));
      sum[dt] = __builtin_bit_cast(f32x4, pr[0][dt]);
    }
#pragma unroll
    for (int r = 1; r < 16; ++r)
#pragma unroll
      for (int dt = 0; dt < 2; ++dt) sum[dt] += __builtin_bit_cast(f32x4, pr[r][dt]);
    const u32x4 zf = pack8(zs[0], zs[1]);
    *(LAS u32x4*)(smem + L_X2 + wave * 1024 + lane * 16) = pack8(sum[0], sum[1]);
    lds_signal(smem, 4 + wave, lane);
    lds_await(smem, 4 + (wave ^ 1), 1);
    const u32x4 kv0 = *(LAS u32x4*)(smem + L_X2 + hh * 2048 + lane * 16);
    const u32x4 kv1 = *(LAS u32x4*)(smem + L_X2 + hh * 2048 + 1024 + lane * 16);
    const f32x4 zero = {0, 0, 0, 0}, eps4 = {EPS, EPS, EPS, EPS};
#pragma unroll
    for (int mt = 0; mt < 4; ++mt) {
      const f32x4 den = mfma(zf, pq[mt], eps4);
      const f32x4 n0 = mfma(kv0, pq[mt], zero), n1 = mfma(kv1, pq[mt], zero);
      const float inv = __builtin_amdgcn_rcpf(den[0]);
      st128_sc(pack8(n0 * inv, n1 * inv), ws, lane * 16 + (mt & 1) * 1024, WS_O + rb * 262144 + (mh * 2 + (mt >> 1)) * 65536 + (cb * 2 + hh) * 2048);
    }
  }
  STAMP(5);
  publish(ws, WS_F2 + (((rb * 2 + mh) * 16 + cb) * 2 + hh) * 8, tag, lane);
  STAMP(6);
  await(ws, WS_F2 + (rb2 * 2 + (wave >> 1)) * 256, 31, tag, lane);
  STAMP(7);
  if (bid == 0 && wave == 0 && lane == 0) __builtin_amdgcn_raw_buffer_store_b64(tag, ws, WS_EPOCH, 0, AUX_SC1);
  {
    constexpr int WIN = 16;
    u32x4 of[WIN][2];
    const u32 ob = WS_O + rb2 * 262144 + wave * 65536;
    auto ldo = [&](int st) {
#pragma unroll
      for (int m = 0; m < 2; ++m) of[st % WIN][m] = ld128_sc(ws, lane * 16 + ((st & 1) * 2 + m) * 1024, ob + (((st & ~1) ^ hx) * 2048));
    };
#pragma unroll
    for (int st = 0; st < WIN; ++st) ldo(st);
    SCHED();
    lds_await(smem, 8, 4);
    const int fw = (2 * g + (q4 >> 1)) & 3;
    int wb[4];
#pragma unroll
    for (int t = 0; t < 4; ++t) wb[t] = (4 * g + q4) * 128 + ((t ^ fw) * 32) + p4 * 8;
    auto ldw = [&](u32x4 (&w)[4], int st) {
      const int base = st < 24 ? st * 4096 : L_CONV + ((st - 24) >> 1) * CH_STRIDE + (st & 1) * 4096;
#pragma unroll
      for (int t = 0; t < 4; ++t) w[t] = tr_frag(smem + base + wb[t], 2048);
    };
    f32x4 oc[2][4];
#pragma unroll
    for (int m = 0; m < 2; ++m)
#pragma unroll
      for (int t = 0; t < 4; ++t) oc[m][t] = f32x4{0, 0, 0, 0};
    u32x4 wf[2][4];
    ldw(wf[0], 0);
#pragma unroll
    for (int st = 0; st < 32; ++st) {
      if (st == 23) lds_await(smem, 9, 4);
      if (st < 31) ldw(wf[(st + 1) & 1], st + 1);
#pragma unroll
      for (int t = 0; t < 4; ++t)
#pragma unroll
        for (int m = 0; m < 2; ++m) oc[m][t] = mfma(wf[st & 1][t], of[st % WIN][m], oc[m][t]);
#pragma unroll
      for (int i = 0; i < 8; ++i) { __builtin_amdgcn_sched_group_barrier(0x8, 1, 0); __builtin_amdgcn_sched_group_barrier(0x100, 1, 0); }
      SCHED();
      if (st + WIN < 32) { ldo(st + WIN); SCHED(); }
    }
    STAMP(8);
    const rsrc_t ro = mkrsrc(p.out, 2048 * 2048 * 2);
    LAS char* sc = smem + L_X1 + (wave ^ 1) * 2304;
#pragma unroll
    for (int m = 0; m < 2; ++m) {
#pragma unroll
      for (int t = 0; t < 4; ++t) *(LAS u32x2*)(sc + r16 * 144 + t * 32 + g * 8) = u32x2{pk2(oc[m][t][0], oc[m][t][1]), pk2(oc[m][t][2], oc[m][t][3])};
      asm volatile("" ::: "memory");
#pragma unroll
      for (int e = 0; e < 2; ++e) {
        const u32x4 r = *(LAS u32x4*)(sc + (e * 8 + (lane >> 3)) * 144 + (lane & 7) * 16);
        __builtin_amdgcn_raw_buffer_store_b128(r, ro, (lane >> 3) * 4096 + (lane & 7) * 16, (rb2 * 128 + wave * 32 + m * 16 + e * 8) * 4096 + 2048 + cb2 * 128, 0);
      }
      asm volatile("" ::: "memory");
    }
  }
  STAMP(9);
  STAMP_OUT();
}

extern "C" void kernel_launch(void* const* d_in, const int* in_sizes, int n_in, void* d_out, int out_size, void* d_ws, size_t ws_size,
                              hipStream_t stream) {
  Params p;
  p.q = (const u16*)d_in[0]; p.k = (const u16*)d_in[1]; p.v = (const u16*)d_in[2]; p.sk = (const u16*)d_in[3];
  p.h = (const u16*)d_in[4]; p.dd = (const u16*)d_in[5]; p.x = (const u16*)d_in[6]; p.wqkv = (const u16*)d_in[7];
  p.wo = (const u16*)d_in[8]; p.out = (u16*)d_out; p.ws = (char*)d_ws;
  for(int i=0;i<9;++i) fprintf(stderr,"in%d %p\n",i,d_in[i]); fprintf(stderr,"out %p ws %p\n",d_out,d_ws);
  { hipPointerAttribute_t a; if(hipPointerGetAttributes(&a,d_ws)==hipSuccess) fprintf(stderr,"ws type %d flags %u managed %d\n",(int)a.type,a.allocationFlags,a.isManaged); if(hipPointerGetAttributes(&a,d_in[6])==hipSuccess) fprintf(stderr,"x type %d flags %u\n",(int)a.type,a.allocationFlags);}
  (void)hipFuncSetAttribute((const void*)fused_h3_linattn, hipFuncAttributeMaxDynamicSharedMemorySize, LDS_BYTES);
  fused_h3_linattn<<<256, 512, LDS_BYTES, stream>>>(p);
}
```

```cpp
#define PCOND (wave<8)
#define P0 0
#define P1 0
#include <hip/hip_runtime.h>
#include <hip/hip_bf16.h>

typedef unsigned short u16;
typedef unsigned int u32;
typedef unsigned long long u64;
typedef float f32x2 __attribute__((ext_vector_type(2)));
typedef float f32x4 __attribute__((ext_vector_type(4)));
typedef u32 u32x2 __attribute__((ext_vector_type(2)));
typedef u32 u32x4 __attribute__((ext_vector_type(4)));
typedef short s16x4 __attribute__((ext_vector_type(4)));
typedef short s16x8 __attribute__((ext_vector_type(8)));
typedef __bf16 bf16x2 __attribute__((ext_vector_type(2)));
typedef __bf16 bf16x8 __attribute__((ext_vector_type(8)));
typedef __amdgpu_buffer_rsrc_t rsrc_t;
#define LAS __attribute__((address_space(3)))
#define GAS __attribute__((address_space(1)))
#define DI __device__ __forceinline__

#define USE_MEMSET 0
#define CODE_PF 0
#define STAMPS 0

constexpr u32 WS_EPOCH = 0;
constexpr u32 WS_F1 = 4096;
constexpr u32 WS_F2 = WS_F1 + 8192;
constexpr u32 WS_FA = WS_F2 + 8192;
constexpr u32 WS_FY = WS_FA + 8192;
constexpr u32 WS_CTRL_END = WS_FY + 8192;
constexpr u32 WS_Z = 512u << 10;
constexpr u32 WS_PART = 1u << 20;
constexpr u32 WS_O = 4u << 20;
constexpr u32 WS_KVT = 8u << 20;
constexpr u32 WS_YT = 12u << 20;
constexpr u32 WS_BYTES = 16u << 20;
constexpr u32 WS_DBG = 32u << 20;

constexpr int SLOT = 40960;
constexpr int L_X1 = 98304;
constexpr int L_X2 = L_X1 + 4 * 2304;
constexpr int CH_STRIDE = 12960;
constexpr int L_CONV = 163840 - 4 * CH_STRIDE;
constexpr int C_O = 4608, C_E = C_O + 4160;
constexpr int L_FLAG = 163840 - 64;
constexpr int LDS_BYTES = 163840;
static_assert(L_X2 + 4096 <= L_CONV, "lds map");

constexpr int AUX_SC1 = 16;
constexpr u32 SPIN_CAP = 1u << 20;

DI f32x4 mfma(u32x4 a, u32x4 b, f32x4 c) {
  return __builtin_amdgcn_mfma_f32_16x16x32_bf16(__builtin_bit_cast(bf16x8, a), __builtin_bit_cast(bf16x8, b), c, 0, 0, 0);
}
DI u32 pk2(float lo, float hi) {
  bf16x2 r = __builtin_convertvector(f32x2{lo, hi}, bf16x2);
  return __builtin_bit_cast(u32, r);
}
DI float blo(u32 x) { return __uint_as_float(x << 16); }
DI float bhi(u32 x) { return __uint_as_float(x & 0xffff0000u); }
template <int N> DI void waitv() { asm volatile("s_waitcnt vmcnt(%0)" ::"n"(N) : "memory"); }
DI void waitl() { asm volatile("s_waitcnt lgkmcnt(0)" ::: "memory"); }
#define BAR() do { asm volatile("" ::: "memory"); __builtin_amdgcn_s_barrier(); asm volatile("" ::: "memory"); } while (0)
#define SCHED() __builtin_amdgcn_sched_barrier(0)
DI rsrc_t mkrsrc(const void* p, u32 bytes) { return __builtin_amdgcn_make_buffer_rsrc((void*)p, 0, bytes, 0x20000); }
DI void dma16(rsrc_t r, u32 lds, u32 voff, u32 soff) {
  asm volatile("s_mov_b32 m0, %0\n\ts_nop 0\n\tbuffer_load_dwordx4 %1, %2, %3 offen lds" ::"s"(lds), "v"(voff), "s"(r), "s"(soff) : "memory");
}
DI void ald64(u32x2& d, rsrc_t r, u32 voff, u32 soff) {
  asm volatile("buffer_load_dwordx2 %0, %1, %2, %3 offen" : "=v"(d) : "v"(voff), "s"(r), "s"(soff) : "memory");
}
DI void ald64_sc(u32x2& d, rsrc_t r, u32 voff, u32 soff) {
  asm volatile("buffer_load_dwordx2 %0, %1, %2, %3 offen sc1" : "=v"(d) : "v"(voff), "s"(r), "s"(soff) : "memory");
}
DI void ald128(u32x4& d, rsrc_t r, u32 voff, u32 soff) {
  asm volatile("buffer_load_dwordx4 %0, %1, %2, %3 offen" : "=v"(d) : "v"(voff), "s"(r), "s"(soff) : "memory");
}
DI void ald128_sc(u32x4& d, rsrc_t r, u32 voff, u32 soff) {
  asm volatile("buffer_load_dwordx4 %0, %1, %2, %3 offen sc1" : "=v"(d) : "v"(voff), "s"(r), "s"(soff) : "memory");
}
#define PIN(x) asm volatile("" : "+v"(x))
DI u32x4 ld128_sc(rsrc_t r, u32 voff, u32 soff) { return __builtin_amdgcn_raw_buffer_load_b128(r, voff, soff, AUX_SC1); }
DI void st128_sc(u32x4 v, rsrc_t r, u32 voff, u32 soff) { __builtin_amdgcn_raw_buffer_store_b128(v, r, voff, soff, AUX_SC1); }

DI void publish(rsrc_t ws, u32 off, u32x2 tag, int lane) {
  waitv<0>();
  if (lane == 0) __builtin_amdgcn_raw_buffer_store_b64(tag, ws, off, 0, AUX_SC1);
}
DI void await(rsrc_t ws, u32 off, int mask, u32x2 tag, int lane) {
  const u32 vo = off + 8u * (lane & mask);
#pragma unroll 1
  for (u32 spins = 0; spins < SPIN_CAP; ++spins) {
    u32x2 f;
    asm volatile("buffer_load_dwordx2 %0, %1, %2, 0 offen sc1\n\ts_waitcnt vmcnt(0)" : "=&v"(f) : "v"(vo), "s"(ws) : "memory");
    if (__all(f.x == tag.x && f.y == tag.y)) break;
    __builtin_amdgcn_s_sleep(1);
  }
  asm volatile("" ::: "memory");
}
DI void lds_signal(LAS char* smem, int word, int lane) {
  waitl();
  if (lane == 0) __hip_atomic_fetch_add((LAS u32*)(smem + L_FLAG) + word, 1u, __ATOMIC_RELAXED, __HIP_MEMORY_SCOPE_WORKGROUP);
}
DI void lds_await(LAS char* smem, int word, u32 want) {
  for (u32 spins = 0; spins < (SPIN_CAP << 3); ++spins) {
    const u32 v = __hip_atomic_load((LAS u32*)(smem + L_FLAG) + word, __ATOMIC_RELAXED, __HIP_MEMORY_SCOPE_WORKGROUP);
    if (__builtin_amdgcn_readfirstlane(v) >= want) break;
  }
  asm volatile("" ::: "memory");
}

#define STAMP(i) do { if ((i)==0 || (i)==P0 || (i)==P1) { u64 t_; asm volatile("s_memrealtime %0\n\ts_waitcnt lgkmcnt(0)" : "=s"(t_) :: "memory"); if((i)==0) ts0=t_; if((i)==P0) tsa=t_; if((i)==P1) tsb=t_; } } while(0)
#define STAMP_OUT() do { if (PCOND) { const u64 until = ts0 + 10000 + 10*(tsb-tsa); for(int z=0;z<100000;++z){ u64 t_; asm volatile("s_memrealtime %0\n\ts_waitcnt lgkmcnt(0)" : "=s"(t_) :: "memory"); if(t_>=until) break; __builtin_amdgcn_s_sleep(2);} } } while(0)

struct Params {
  const u16 *q, *k, *v, *sk, *h, *dd, *x, *wqkv, *wo;
  u16* out;
  char* ws;
};

struct Frags { u32x4 x[4]; u32x4 w[6]; };
DI u32x4 tr_frag(LAS char* a, int d) {
  s16x4 lo = __builtin_amdgcn_ds_read_tr16_b64_v4i16((LAS s16x4*)a);
  s16x4 hi = __builtin_amdgcn_ds_read_tr16_b64_v4i16((LAS s16x4*)(a + d));
  return __builtin_bit_cast(u32x4, __builtin_shufflevector(lo, hi, 0, 1, 2, 3, 4, 5, 6, 7));
}
DI void ldfr(Frags& f, LAS char* xa, LAS char* wb0, LAS char* wb1, int ks) {
#pragma unroll
  for (int mt = 0; mt < 4; ++mt) f.x[mt] = *(LAS u32x4*)(xa + mt * 2048);
#pragma unroll
  for (int sg = 0; sg < 3; ++sg)
#pragma unroll
    for (int t = 0; t < 2; ++t) f.w[sg * 2 + t] = tr_frag((t ? wb1 : wb0) + sg * 8192 + ks * 4096, 512);
}
DI void mm(f32x4 (&acc)[4][6], const Frags& f) {
#pragma unroll
  for (int n = 0; n < 6; ++n)
#pragma unroll
    for (int m_ = 0; m_ < 4; ++m_) {
      const int mt = (n & 1) ? 3 - m_ : m_;
      if (n < 2) acc[mt][n] = mfma(f.w[n], f.x[mt], acc[mt][n]);
      else acc[mt][n] = mfma(f.x[mt], f.w[n], acc[mt][n]);
    }
}
DI void interleave_b1() {
#pragma unroll
  for (int i = 0; i < 16; ++i) { __builtin_amdgcn_sched_group_barrier(0x8, 1, 0); __builtin_amdgcn_sched_group_barrier(0x100, 1, 0); }
  __builtin_amdgcn_sched_group_barrier(0x8, 8, 0);
}
constexpr float EPS = 1e-6f;
DI float phi(float t) {
  const float e = __builtin_amdgcn_fmed3f(__builtin_amdgcn_exp2f(__builtin_fmaf(t, 1.44269504088896f, -EPS * 1.44269504088896f)), 0.f, 1.f);
  return e + __builtin_fmaxf(t, EPS);
}
DI u32x4 pack_phi(f32x4 a, f32x4 b) { return u32x4{pk2(phi(a[0]), phi(a[1])), pk2(phi(a[2]), phi(a[3])), pk2(phi(b[0]), phi(b[1])), pk2(phi(b[2]), phi(b[3]))}; }
DI u32x4 pack8(f32x4 a, f32x4 b) { return u32x4{pk2(a[0], a[1]), pk2(a[2], a[3]), pk2(b[0], b[1]), pk2(b[2], b[3])}; }
template <int CTRL> DI float dpp_add(float v) {
  return v + __int_as_float(__builtin_amdgcn_update_dpp(0, __float_as_int(v), CTRL, 0xf, 0xf, false));
}
DI float row_allsum(float v) { v = dpp_add<0x128>(v); v = dpp_add<0x124>(v); v = dpp_add<0x122>(v); v = dpp_add<0x121>(v); return v; }

struct CFr { u32x4 a[8]; u32x4 b[8]; };
DI void ldconv(CFr& f, LAS char* ha, LAS char* xb) {
#pragma unroll
  for (int i = 0; i < 8; ++i) {
    f.b[i] = *(LAS u32x4*)(xb + (7 - i) * 512);
    LAS u32* hp = (LAS u32*)(ha + i * 512);
    f.a[i] = u32x4{hp[0], hp[1], hp[2], hp[3]};
  }
}
DI void mmconv(f32x4 (&acc)[8], const CFr& f) {
#pragma unroll
  for (int n = 0; n < 8; ++n)
#pragma unroll
    for (int q = 0; q <= n; ++q) acc[n] = mfma(f.a[q], f.b[n - q], acc[n]);
}
DI void interleave_conv() {
#pragma unroll
  for (int i = 0; i < 24; ++i) { __builtin_amdgcn_sched_group_barrier(0x8, 1, 0); __builtin_amdgcn_sched_group_barrier(0x100, 1, 0); }
  __builtin_amdgcn_sched_group_barrier(0x8, 12, 0);
}

__global__ __launch_bounds__(512, 2) void fused_h3_linattn(Params p) {
  extern __shared__ __attribute__((aligned(16))) char smem_[];
  LAS char* smem = (LAS char*)smem_;
  const int bid = blockIdx.x, xcd = bid & 7, j = bid >> 3;
  const int rb = (xcd & 1) * 8 + (j & 7), cb = (xcd >> 1) * 4 + (j >> 3);
  const int rb2 = xcd * 2 + (j & 1), cb2 = j >> 1;
  const int hx = (cb2 & 1) * 16;
  const int wave = __builtin_amdgcn_readfirstlane(threadIdx.x >> 6), lane = threadIdx.x & 63;
  const rsrc_t ws = mkrsrc(p.ws, WS_BYTES);
  u64 ts0=0,tsa=0,tsb=0;
  STAMP(0);

  if (wave >= 4) {
    const int hw = wave - 4;
    const rsrc_t rx = mkrsrc(p.x, 2048 * 1024 * 2), rw = mkrsrc(p.wqkv, 1024 * 3072 * 2);
    const int r8 = lane >> 3, c8 = lane & 7;
    u32 vx[2], vw[2];
#pragma unroll
    for (int e = 0; e < 2; ++e) {
      vx[e] = r8 * 2048 + ((c8 ^ (4 * e + (r8 >> 1))) * 16);
      vw[e] = r8 * 6144 + ((c8 ^ ((((r8 >> 1) & 1) | (e << 1)) << 1)) * 16);
    }
    const u32 sx0 = (rb * 128 + hw * 32) * 2048, sw0 = (hw * 16) * 6144 + cb * 128;
    const int rot = 4 * ((rb + cb) & 3);
    auto issue = [&](int s_) {
      const u32 s = (s_ + rot) & 15, base = ((s_ + 1) % 3) * SLOT;
#pragma unroll
      for (int e = 0; e < 4; ++e) dma16(rx, base + (hw * 4 + e) * 1024, vx[e & 1], sx0 + e * 8 * 2048 + s * 128);
#pragma unroll
      for (int sg = 0; sg < 3; ++sg)
#pragma unroll
        for (int e = 0; e < 2; ++e) dma16(rw, base + 16384 + sg * 8192 + (hw * 2 + e) * 1024, vw[e], sw0 + e * 8 * 6144 + sg * 2048 + s * 64 * 6144);
    };
    issue(0); issue(1);
    waitv<10>(); BAR();
    issue(2);
    const u32x2 tag = *(LAS u32x2*)(smem + L_FLAG + 48);
    waitv<10>(); BAR(); issue(3);
    waitv<10>(); BAR(); issue(4);
    const int cg = lane & 15, rg = lane >> 4;
    const int R0 = rb * 128 + hw * 32 + rg * 8;
    u32x2 kk[11], vv[8]; u32x4 tw[2];
    {
      const rsrc_t rk = mkrsrc(p.k, 2048 * 1024 * 2), rv = mkrsrc(p.v, 2048 * 1024 * 2), rs = mkrsrc(p.sk, 1024 * 4 * 2);
      const u32 vo = R0 * 2048 + cb * 128 + cg * 8;
      const u32 vh = R0 == 0 ? 0x40000000u : vo - 3 * 2048;
#pragma unroll
      for (int i = 0; i < 3; ++i) ald64(kk[i], rk, vh, i * 2048);
#pragma unroll
      for (int i = 0; i < 8; ++i) ald64(kk[3 + i], rk, vo, i * 2048);
#pragma unroll
      for (int i = 0; i < 8; ++i) ald64(vv[i], rv, vo, i * 2048);
      ald128(tw[0], rs, cb * 512 + cg * 32, 0); ald128(tw[1], rs, cb * 512 + cg * 32, 16);
    }
    waitv<31>(); BAR(); issue(5);
    waitv<31>(); BAR(); issue(6);
    const u32 kvt_vo = WS_KVT + cb * 262144 + (cg >> 2) * 65536 + (255 - (R0 >> 3)) * 64 + (cg & 3) * 16;
    auto shortconv = [&](int c) {
      float kf[11], wf[4], o[8];
#pragma unroll
      for (int i = 0; i < 11; ++i) kf[i] = (c & 1) ? bhi(kk[i][c >> 1]) : blo(kk[i][c >> 1]);
#pragma unroll
      for (int t = 0; t < 4; ++t) { const u32 d = tw[c >> 1][(c & 1) * 2 + (t >> 1)]; wf[t] = (t & 1) ? bhi(d) : blo(d); }
#pragma unroll
      for (int i = 0; i < 8; ++i) {
        float a = kf[i] * wf[3];
        a = __builtin_fmaf(kf[i + 1], wf[2], a); a = __builtin_fmaf(kf[i + 2], wf[1], a); a = __builtin_fmaf(kf[i + 3], wf[0], a);
        o[i] = a * ((c & 1) ? bhi(vv[i][c >> 1]) : blo(vv[i][c >> 1]));
      }
      st128_sc(u32x4{pk2(o[7], o[6]), pk2(o[5], o[4]), pk2(o[3], o[2]), pk2(o[1], o[0])}, ws, kvt_vo, c * 16384);
    };
    waitv<10>(); BAR(); issue(7);
#pragma unroll
    for (int i = 0; i < 11; ++i) PIN(kk[i]);
#pragma unroll
    for (int i = 0; i < 8; ++i) PIN(vv[i]);
    PIN(tw[0]); PIN(tw[1]);
    shortconv(0); SCHED();
    waitv<11>(); BAR(); issue(8); shortconv(1); SCHED();
    waitv<12>(); BAR(); issue(9); shortconv(2); SCHED();
    waitv<12>(); BAR(); issue(10); shortconv(3); SCHED();
    const int ch = cb * 64 + (rb >> 2) * 16 + hw * 4 + (rb & 3);
    u32x4 hh4[4]; u32x2 dd2;
    {
      const rsrc_t rh = mkrsrc(p.h, 1024 * 2048 * 2), rd = mkrsrc(p.dd, 1024 * 2);
#pragma unroll
      for (int n = 0; n < 4; ++n) ald128(hh4[n], rh, lane * 16, ch * 4096 + n * 1024);
      ald64(dd2, rd, 0, (ch >> 2) * 8);
    }
    waitv<17>(); BAR(); issue(11);
    waitv<16>(); BAR();
    publish(ws, WS_FA + ((cb * 16 + rb) * 4 + hw) * 8, tag, lane);
    issue(12);
    waitv<10>(); BAR(); issue(13);
    waitv<10>(); BAR(); issue(14);
    waitv<10>(); BAR();
    const u32 fa_off = WS_FA + (cb * 16 + (3 - hw) * 4) * 32;
    u32x2 fl;
    ald64_sc(fl, ws, fa_off + (lane & 15) * 8, 0);
    issue(15);
    waitv<10>(); PIN(fl);
    if (!__all(fl.x == tag.x && fl.y == tag.y)) await(ws, fa_off, 15, tag, lane);
    u32x4 xs[4];
#pragma unroll
    for (int n = 0; n < 4; ++n) ald128_sc(xs[n], ws, lane * 16, WS_KVT + cb * 262144 + rb * 16384 + (hw * 4 + n) * 1024);
    BAR();
    LAS char* cr = smem + L_CONV + hw * CH_STRIDE;
#pragma unroll
    for (int n = 0; n < 4; ++n) PIN(hh4[n]);
    PIN(dd2);
    {
      const u32x4 z4 = {0, 0, 0, 0};
      if (lane < 30) *(LAS u32x4*)(cr + 4096 + lane * 16) = z4;
      if (lane < 4) *(LAS u32x4*)(cr + C_O + (lane >> 1) * (C_E - C_O) + (lane & 1) * 16) = z4;
#pragma unroll
      for (int n = 0; n < 4; ++n) *(LAS u32x4*)(cr + C_O + 32 + n * 1024 + lane * 16) = hh4[n];
      waitl();
#pragma unroll
      for (int n = 0; n < 4; ++n) {
        const u32 dp = *(LAS u32*)(cr + C_O + 28 + n * 1024 + lane * 16);
        const u32x4 d = hh4[n];
        *(LAS u32x4*)(cr + C_E + 32 + n * 1024 + lane * 16) =
            u32x4{__builtin_amdgcn_alignbit(d[0], dp, 16), __builtin_amdgcn_alignbit(d[1], d[0], 16),
                  __builtin_amdgcn_alignbit(d[2], d[1], 16), __builtin_amdgcn_alignbit(d[3], d[2], 16)};
      }
    }
    waitv<4>(); BAR();
    waitv<0>();
#pragma unroll
    for (int n = 0; n < 4; ++n) PIN(xs[n]);
#pragma unroll
    for (int n = 0; n < 4; ++n) *(LAS u32x4*)(smem + L_CONV + (lane & 3) * CH_STRIDE + ((hw * 4 + n) * 16 + (lane >> 2)) * 16) = xs[n];
    waitl(); BAR();
    STAMP(1);
    const int R2 = rb * 128 + ((hw >> 1) * 4 + rg) * 16 + (hw & 1) * 8;
    u32x2 qq[8];
    {
      const rsrc_t rq = mkrsrc(p.q, 2048 * 1024 * 2);
#pragma unroll
      for (int i = 0; i < 8; ++i) ald64(qq[i], rq, R2 * 2048 + cb * 128 + cg * 8, i * 2048);
    }
    const rsrc_t rwo = mkrsrc(p.wo, 1024 * 1024 * 2);
    const u32 vwo = r8 * 2048 + ((c8 ^ (((r8 >> 1) & 3) << 1)) * 16);
    const int b16 = lane & 15, kg = lane >> 4;
    LAS char* ha = cr + ((b16 & 1) ? C_O : C_E) + 4 + 4 * (b16 >> 1) + 16 * kg;
    LAS char* xb = cr + (254 - 224 - 2 * b16 + kg) * 16;
    f32x4 acc[8];
#pragma unroll
    for (int n = 0; n < 8; ++n) acc[n] = f32x4{0, 0, 0, 0};
    CFr C0, C1;
    ldconv(C0, ha, xb);
#pragma unroll 1
    for (int it = 0; it < 3; ++it) {
#pragma unroll
      for (int e = 0; e < 4; ++e) { const int st = it * 8 + hw; dma16(rwo, st * 4096 + e * 1024, vwo, ((st ^ hx) * 32 + e * 8) * 2048 + cb2 * 128); }
      SCHED();
      ldconv(C1, ha + it * 128 + 64, xb + it * 128 + 64);
      mmconv(acc, C0); interleave_conv(); SCHED();
#pragma unroll
      for (int e = 0; e < 4; ++e) { const int st = it * 8 + 4 + hw; dma16(rwo, st * 4096 + e * 1024, vwo, ((st ^ hx) * 32 + e * 8) * 2048 + cb2 * 128); }
      SCHED();
      ldconv(C0, ha + it * 128 + 128, xb + it * 128 + 128);
      mmconv(acc, C1); interleave_conv(); SCHED();
    }
    ldconv(C1, ha + 448, xb + 448);
    mmconv(acc, C0); interleave_conv(); SCHED();
    u32x2 xv[8];
#pragma unroll
    for (int n = 0; n < 8; ++n) xv[n] = *(LAS u32x2*)(cr + (2044 - 256 * n - 16 * b16 - 4 * kg) * 2);
    mmconv(acc, C1);
    SCHED();
    waitv<0>();
    lds_signal(smem, 8, lane);
    STAMP(2);
#pragma unroll
    for (int e = 0; e < 8; ++e) { const int st = 24 + hw * 2 + (e >> 2); dma16(rwo, L_CONV + hw * CH_STRIDE + e * 1024, vwo, ((st ^ hx) * 32 + (e & 3) * 8) * 2048 + cb2 * 128); }
    {
      const u32 dw = (ch & 2) ? dd2.y : dd2.x;
      const float dv = (ch & 1) ? bhi(dw) : blo(dw);
      u32x2 y[8];
#pragma unroll
      for (int n = 0; n < 8; ++n)
        y[n] = u32x2{pk2(__builtin_fmaf(bhi(xv[n].y), dv, acc[n][0]), __builtin_fmaf(blo(xv[n].y), dv, acc[n][1])),
                     pk2(__builtin_fmaf(bhi(xv[n].x), dv, acc[n][2]), __builtin_fmaf(blo(xv[n].x), dv, acc[n][3]))};
      const u32 yvo = WS_YT + cb * 262144 + ((kg & 1) * 2 + (b16 >> 3)) * 16384 + (ch & 63) * 256 + (kg >> 1) * 128 + (b16 & 7) * 16;
#pragma unroll
      for (int n = 0; n < 8; n += 2) {
        auto s0 = __builtin_amdgcn_permlane16_swap(y[n].x, y[n + 1].x, false, false);
        auto s1 = __builtin_amdgcn_permlane16_swap(y[n].y, y[n + 1].y, false, false);
        st128_sc(u32x4{s0[0], s1[0], s0[1], s1[1]}, ws, yvo, n * 32768);
      }
    }
    publish(ws, WS_FY + ((cb * 16 + rb) * 4 + hw) * 8, tag, lane);
    lds_signal(smem, 9, lane);
    STAMP(3);
    await(ws, WS_FY + cb * 512, 63, tag, lane);
    STAMP(4);
    {
      u32x4 yy[4];
      const u32 yo = WS_YT + cb * 262144 + rb * 16384 + (lane >> 2) * 1024 + (hw & 1) * 128 + ((hw >> 1) * 4 + (lane & 3)) * 16;
#pragma unroll
      for (int c = 0; c < 4; ++c) yy[c] = ld128_sc(ws, yo, c * 256);
      LAS char* sc = cr + 8192;
#pragma unroll
      for (int c = 0; c < 4; ++c) *(LAS u32x4*)(sc + c * 1024 + lane * 16) = yy[c];
      asm volatile("" ::: "memory");
#pragma unroll
      for (int c = 0; c < 4; ++c) yy[c] = *(LAS u32x4*)(sc + c * 1024 + (cg * 4 + rg) * 16);
#pragma unroll
      for (int i = 0; i < 8; ++i) PIN(qq[i]);
      const rsrc_t ro = mkrsrc(p.out, 2048 * 2048 * 2);
#pragma unroll
      for (int i = 0; i < 8; ++i) {
        float f[4];
#pragma unroll
        for (int c = 0; c < 4; ++c) {
          const float yv = (i & 1) ? bhi(yy[c][i >> 1]) : blo(yy[c][i >> 1]);
          const float qv = (c & 1) ? bhi(qq[i][c >> 1]) : blo(qq[i][c >> 1]);
          f[c] = yv * qv;
        }
        __builtin_amdgcn_raw_buffer_store_b64(u32x2{pk2(f[0], f[1]), pk2(f[2], f[3])}, ro, R2 * 4096 + cb * 128 + cg * 8, i * 4096, 0);
      }
    }
    STAMP(5);
    STAMP_OUT();
    return;
  }

  __builtin_amdgcn_s_setprio(1);
  u32x2 tag;
  {
    const u32x2 e = __builtin_amdgcn_raw_buffer_load_b64(ws, WS_EPOCH, 0, AUX_SC1);
    const u64 t = (((u64)e.y << 32) | e.x) * 6364136223846793005ull + 1442695040888963407ull;
    tag = u32x2{(u32)t, (u32)(t >> 32)};
    if (wave == 0 && lane < 16) ((LAS u32*)(smem + L_FLAG))[lane] = lane == 12 ? tag.x : lane == 13 ? tag.y : 0u;
    waitl();
  }
  const int mh = wave & 1, hh = wave >> 1;
  const int r16 = lane & 15, g = lane >> 4, q4 = r16 >> 2, p4 = lane & 3;
  f32x4 acc[4][6];
  {
    const int xo = (64 * mh + r16) * 128 + ((g ^ (r16 >> 1)) * 16);
    const int f = ((q4 >> 1) & 1) | ((g & 1) << 1);
    const int wo = 16384 + (8 * g + q4) * 128 + (((4 * hh + (p4 >> 1)) ^ (f << 1)) * 16) + 8 * (p4 & 1);
#pragma unroll
    for (int a = 0; a < 4; ++a)
#pragma unroll
      for (int b = 0; b < 6; ++b) { const float i0 = b < 4 ? EPS : 0.f; acc[a][b] = f32x4{i0, i0, i0, i0}; PIN(acc[a][b]); }
    Frags F0, F1;
    BAR();
    STAMP(1);
    int so = SLOT;
    ldfr(F0, smem + so + xo, smem + so + wo, smem + so + (wo ^ 32), 0);
#pragma unroll 1
    for (int s = 0; s < 15; ++s) {
      ldfr(F1, smem + so + (xo ^ 64), smem + so + wo, smem + so + (wo ^ 32), 1);
      mm(acc, F0); interleave_b1(); SCHED();
      waitl(); BAR(); SCHED();
      so = (so == 2 * SLOT) ? 0 : so + SLOT;
      ldfr(F0, smem + so + xo, smem + so + wo, smem + so + (wo ^ 32), 0);
      mm(acc, F1); interleave_b1(); SCHED();
    }
    ldfr(F1, smem + so + (xo ^ 64), smem + so + wo, smem + so + (wo ^ 32), 1);
    mm(acc, F0); interleave_b1(); SCHED();
    waitl(); BAR(); SCHED();
    mm(acc, F1);
  }
  SCHED();
  STAMP(2);
  const u32 pslot = (cb * 2 + hh) * 2 + mh;
  {
    f32x4 kvp[2][2], zp[2];
    const f32x4 zero = {0, 0, 0, 0};
    const u32x4 ones = {0x3f803f80u, 0x3f803f80u, 0x3f803f80u, 0x3f803f80u};
#pragma unroll
    for (int u = 0; u < 2; ++u) {
      u32x4 pkf[2], vf[2];
#pragma unroll
      for (int t = 0; t < 2; ++t) { pkf[t] = pack_phi(acc[2 * u][2 + t], acc[2 * u + 1][2 + t]); vf[t] = pack8(acc[2 * u][4 + t], acc[2 * u + 1][4 + t]); }
#pragma unroll
      for (int dt = 0; dt < 2; ++dt) {
#pragma unroll
        for (int et = 0; et < 2; ++et) kvp[dt][et] = mfma(pkf[dt], vf[et], u ? kvp[dt][et] : zero);
        zp[dt] = mfma(pkf[dt], ones, u ? zp[dt] : zero);
      }
    }
    LAS char* mine = smem + L_X1 + wave * 2304;
    LAS char* theirs = smem + L_X1 + (wave ^ 1) * 2304;
    f32x4 keep[2];
#pragma unroll
    for (int dt = 0; dt < 2; ++dt) {
      keep[dt] = mh ? kvp[dt][1] : kvp[dt][0];
      *(LAS f32x4*)(mine + dt * 1024 + lane * 16) = mh ? kvp[dt][0] : kvp[dt][1];
      if (r16 == 0) *(LAS f32x4*)(mine + 2048 + dt * 64 + g * 16) = zp[dt];
    }
    lds_signal(smem, wave, lane);
    lds_await(smem, wave ^ 1, 1);
#pragma unroll
    for (int dt = 0; dt < 2; ++dt) {
      keep[dt] += *(LAS f32x4*)(theirs + dt * 1024 + lane * 16);
      zp[dt] += *(LAS f32x4*)(theirs + 2048 + dt * 64 + g * 16);
      st128_sc(__builtin_bit_cast(u32x4, keep[dt]), ws, lane * 16, WS_PART + pslot * 32768 + rb * 2048 + dt * 1024);
    }
    if (r16 < 2) st128_sc(__builtin_bit_cast(u32x4, r16 ? zp[1] : zp[0]), ws, r16 * 64 + g * 16, WS_Z + pslot * 2048 + rb * 128);
  }
  SCHED();
  u32x4 pq[4];
#pragma unroll
  for (int mt = 0; mt < 4; ++mt) { pq[mt] = pack_phi(acc[mt][0], acc[mt][1]); PIN(pq[mt]); }
  SCHED();
  publish(ws, WS_F1 + ((cb * 4 + wave) * 16 + rb) * 8, tag, lane);
  STAMP(3);
  await(ws, WS_F1 + (cb * 4 + wave) * 128, 15, tag, lane);
  STAMP(4);
  {
    u32x4 pr[16][2], zr[2];
#pragma unroll
    for (int dt = 0; dt < 2; ++dt) zr[dt] = ld128_sc(ws, r16 * 128 + dt * 64 + g * 16, WS_Z + pslot * 2048);
#pragma unroll
    for (int r = 0; r < 16; ++r)
#pragma unroll
      for (int dt = 0; dt < 2; ++dt) pr[r][dt] = ld128_sc(ws, lane * 16 + ((r & 1) * 2 + dt) * 1024, WS_PART + pslot * 32768 + (r >> 1) * 4096);
    SCHED();
    f32x4 zs[2], sum[2];
#pragma unroll
    for (int dt = 0; dt < 2; ++dt) {
#pragma unroll
      for (int r = 0; r < 4; ++r) zs[dt][r] = row_allsum(__uint_as_float(zr[dt][r]));
      sum[dt] = __builtin_bit_cast(f32x4, pr[0][dt]);
    }
#pragma unroll
    for (int r = 1; r < 16; ++r)
#pragma unroll
      for (int dt = 0; dt < 2; ++dt) sum[dt] += __builtin_bit_cast(f32x4, pr[r][dt]);
    const u32x4 zf = pack8(zs[0], zs[1]);
    *(LAS u32x4*)(smem + L_X2 + wave * 1024 + lane * 16) = pack8(sum[0], sum[1]);
    lds_signal(smem, 4 + wave, lane);
    lds_await(smem, 4 + (wave ^ 1), 1);
    const u32x4 kv0 = *(LAS u32x4*)(smem + L_X2 + hh * 2048 + lane * 16);
    const u32x4 kv1 = *(LAS u32x4*)(smem + L_X2 + hh * 2048 + 1024 + lane * 16);
    const f32x4 zero = {0, 0, 0, 0}, eps4 = {EPS, EPS, EPS, EPS};
#pragma unroll
    for (int mt = 0; mt < 4; ++mt) {
      const f32x4 den = mfma(zf, pq[mt], eps4);
      const f32x4 n0 = mfma(kv0, pq[mt], zero), n1 = mfma(kv1, pq[mt], zero);
      const float inv = __builtin_amdgcn_rcpf(den[0]);
      st128_sc(pack8(n0 * inv, n1 * inv), ws, lane * 16 + (mt & 1) * 1024, WS_O + rb * 262144 + (mh * 2 + (mt >> 1)) * 65536 + (cb * 2 + hh) * 2048);
    }
  }
  STAMP(5);
  publish(ws, WS_F2 + (((rb * 2 + mh) * 16 + cb) * 2 + hh) * 8, tag, lane);
  STAMP(6);
  await(ws, WS_F2 + (rb2 * 2 + (wave >> 1)) * 256, 31, tag, lane);
  STAMP(7);
  if (bid == 0 && wave == 0 && lane == 0) __builtin_amdgcn_raw_buffer_store_b64(tag, ws, WS_EPOCH, 0, AUX_SC1);
  {
    constexpr int WIN = 16;
    u32x4 of[WIN][2];
    const u32 ob = WS_O + rb2 * 262144 + wave * 65536;
    auto ldo = [&](int st) {
#pragma unroll
      for (int m = 0; m < 2; ++m) of[st % WIN][m] = ld128_sc(ws, lane * 16 + ((st & 1) * 2 + m) * 1024, ob + (((st & ~1) ^ hx) * 2048));
    };
#pragma unroll
    for (int st = 0; st < WIN; ++st) ldo(st);
    SCHED();
    lds_await(smem, 8, 4);
    const int fw = (2 * g + (q4 >> 1)) & 3;
    int wb[4];
#pragma unroll
    for (int t = 0; t < 4; ++t) wb[t] = (4 * g + q4) * 128 + ((t ^ fw) * 32) + p4 * 8;
    auto ldw = [&](u32x4 (&w)[4], int st) {
      const int base = st < 24 ? st * 4096 : L_CONV + ((st - 24) >> 1) * CH_STRIDE + (st & 1) * 4096;
#pragma unroll
      for (int t = 0; t < 4; ++t) w[t] = tr_frag(smem + base + wb[t], 2048);
    };
    f32x4 oc[2][4];
#pragma unroll
    for (int m = 0; m < 2; ++m)
#pragma unroll
      for (int t = 0; t < 4; ++t) oc[m][t] = f32x4{0, 0, 0, 0};
    u32x4 wf[2][4];
    ldw(wf[0], 0);
#pragma unroll
    for (int st = 0; st < 32; ++st) {
      if (st == 23) lds_await(smem, 9, 4);
      if (st < 31) ldw(wf[(st + 1) & 1], st + 1);
#pragma unroll
      for (int t = 0; t < 4; ++t)
#pragma unroll
        for (int m = 0; m < 2; ++m) oc[m][t] = mfma(wf[st & 1][t], of[st % WIN][m], oc[m][t]);
#pragma unroll
      for (int i = 0; i < 8; ++i) { __builtin_amdgcn_sched_group_barrier(0x8, 1, 0); __builtin_amdgcn_sched_group_barrier(0x100, 1, 0); }
      SCHED();
      if (st + WIN < 32) { ldo(st + WIN); SCHED(); }
    }
    STAMP(8);
    const rsrc_t ro = mkrsrc(p.out, 2048 * 2048 * 2);
    LAS char* sc = smem + L_X1 + (wave ^ 1) * 2304;
#pragma unroll
    for (int m = 0; m < 2; ++m) {
#pragma unroll
      for (int t = 0; t < 4; ++t) *(LAS u32x2*)(sc + r16 * 144 + t * 32 + g * 8) = u32x2{pk2(oc[m][t][0], oc[m][t][1]), pk2(oc[m][t][2], oc[m][t][3])};
      asm volatile("" ::: "memory");
#pragma unroll
      for (int e = 0; e < 2; ++e) {
        const u32x4 r = *(LAS u32x4*)(sc + (e * 8 + (lane >> 3)) * 144 + (lane & 7) * 16);
        __builtin_amdgcn_raw_buffer_store_b128(r, ro, (lane >> 3) * 4096 + (lane & 7) * 16, (rb2 * 128 + wave * 32 + m * 16 + e * 8) * 4096 + 2048 + cb2 * 128, 0);
      }
      asm volatile("" ::: "memory");
    }
  }
  STAMP(9);
  STAMP_OUT();
}

extern "C" void kernel_launch(void* const* d_in, const int* in_sizes, int n_in, void* d_out, int out_size, void* d_ws, size_t ws_size,
                              hipStream_t stream) {
  Params p;
  p.q = (const u16*)d_in[0]; p.k = (const u16*)d_in[1]; p.v = (const u16*)d_in[2]; p.sk = (const u16*)d_in[3];
  p.h = (const u16*)d_in[4]; p.dd = (const u16*)d_in[5]; p.x = (const u16*)d_in[6]; p.wqkv = (const u16*)d_in[7];
  p.wo = (const u16*)d_in[8]; p.out = (u16*)d_out; p.ws = (char*)d_ws;
  (void)hipFuncSetAttribute((const void*)fused_h3_linattn, hipFuncAttributeMaxDynamicSharedMemorySize, LDS_BYTES);
  fused_h3_linattn<<<256, 512, LDS_BYTES, stream>>>(p);
}
```

```cpp
#define OAUX 16
#include <hip/hip_runtime.h>
#include <hip/hip_bf16.h>

typedef unsigned short u16;
typedef unsigned int u32;
typedef unsigned long long u64;
typedef float f32x2 __attribute__((ext_vector_type(2)));
typedef float f32x4 __attribute__((ext_vector_type(4)));
typedef u32 u32x2 __attribute__((ext_vector_type(2)));
typedef u32 u32x4 __attribute__((ext_vector_type(4)));
typedef short s16x4 __attribute__((ext_vector_type(4)));
typedef short s16x8 __attribute__((ext_vector_type(8)));
typedef __bf16 bf16x2 __attribute__((ext_vector_type(2)));
typedef __bf16 bf16x8 __attribute__((ext_vector_type(8)));
typedef __amdgpu_buffer_rsrc_t rsrc_t;
#define LAS __attribute__((address_space(3)))
#define GAS __attribute__((address_space(1)))
#define DI __device__ __forceinline__

#define USE_MEMSET 0
#define CODE_PF 0
#define STAMPS 0

constexpr u32 WS_EPOCH = 0;
constexpr u32 WS_F1 = 4096;
constexpr u32 WS_F2 = WS_F1 + 8192;
constexpr u32 WS_FA = WS_F2 + 8192;
constexpr u32 WS_FY = WS_FA + 8192;
constexpr u32 WS_CTRL_END = WS_FY + 8192;
constexpr u32 WS_Z = 512u << 10;
constexpr u32 WS_PART = 1u << 20;
constexpr u32 WS_O = 4u << 20;
constexpr u32 WS_KVT = 8u << 20;
constexpr u32 WS_YT = 12u << 20;
constexpr u32 WS_BYTES = 16u << 20;
constexpr u32 WS_DBG = 32u << 20;

constexpr int SLOT = 40960;
constexpr int L_X1 = 98304;
constexpr int L_X2 = L_X1 + 4 * 2304;
constexpr int CH_STRIDE = 12960;
constexpr int L_CONV = 163840 - 4 * CH_STRIDE;
constexpr int C_O = 4608, C_E = C_O + 4160;
constexpr int L_FLAG = 163840 - 64;
constexpr int LDS_BYTES = 163840;
static_assert(L_X2 + 4096 <= L_CONV, "lds map");

constexpr int AUX_SC1 = 16;
constexpr u32 SPIN_CAP = 1u << 20;

DI f32x4 mfma(u32x4 a, u32x4 b, f32x4 c) {
  return __builtin_amdgcn_mfma_f32_16x16x32_bf16(__builtin_bit_cast(bf16x8, a), __builtin_bit_cast(bf16x8, b), c, 0, 0, 0);
}
DI u32 pk2(float lo, float hi) {
  bf16x2 r = __builtin_convertvector(f32x2{lo, hi}, bf16x2);
  return __builtin_bit_cast(u32, r);
}
DI float blo(u32 x) { return __uint_as_float(x << 16); }
DI float bhi(u32 x) { return __uint_as_float(x & 0xffff0000u); }
template <int N> DI void waitv() { asm volatile("s_waitcnt vmcnt(%0)" ::"n"(N) : "memory"); }
DI void waitl() { asm volatile("s_waitcnt lgkmcnt(0)" ::: "memory"); }
#define BAR() do { asm volatile("" ::: "memory"); __builtin_amdgcn_s_barrier(); asm volatile("" ::: "memory"); } while (0)
#define SCHED() __builtin_amdgcn_sched_barrier(0)
DI rsrc_t mkrsrc(const void* p, u32 bytes) { return __builtin_amdgcn_make_buffer_rsrc((void*)p, 0, bytes, 0x20000); }
DI void dma16(rsrc_t r, u32 lds, u32 voff, u32 soff) {
  asm volatile("s_mov_b32 m0, %0\n\ts_nop 0\n\tbuffer_load_dwordx4 %1, %2, %3 offen lds" ::"s"(lds), "v"(voff), "s"(r), "s"(soff) : "memory");
}
DI void ald64(u32x2& d, rsrc_t r, u32 voff, u32 soff) {
  asm volatile("buffer_load_dwordx2 %0, %1, %2, %3 offen" : "=v"(d) : "v"(voff), "s"(r), "s"(soff) : "memory");
}
DI void ald64_sc(u32x2& d, rsrc_t r, u32 voff, u32 soff) {
  asm volatile("buffer_load_dwordx2 %0, %1, %2, %3 offen sc1" : "=v"(d) : "v"(voff), "s"(r), "s"(soff) : "memory");
}
DI void ald128(u32x4& d, rsrc_t r, u32 voff, u32 soff) {
  asm volatile("buffer_load_dwordx4 %0, %1, %2, %3 offen" : "=v"(d) : "v"(voff), "s"(r), "s"(soff) : "memory");
}
DI void ald128_sc(u32x4& d, rsrc_t r, u32 voff, u32 soff) {
  asm volatile("buffer_load_dwordx4 %0, %1, %2, %3 offen sc1" : "=v"(d) : "v"(voff), "s"(r), "s"(soff) : "memory");
}
#define PIN(x) asm volatile("" : "+v"(x))
DI u32x4 ld128_sc(rsrc_t r, u32 voff, u32 soff) { return __builtin_amdgcn_raw_buffer_load_b128(r, voff, soff, AUX_SC1); }
DI void st128_sc(u32x4 v, rsrc_t r, u32 voff, u32 soff) { __builtin_amdgcn_raw_buffer_store_b128(v, r, voff, soff, AUX_SC1); }

DI void publish(rsrc_t ws, u32 off, u32x2 tag, int lane) {
  waitv<0>();
  if (lane == 0) __builtin_amdgcn_raw_buffer_store_b64(tag, ws, off, 0, AUX_SC1);
}
DI void await(rsrc_t ws, u32 off, int mask, u32x2 tag, int lane) {
  const u32 vo = off + 8u * (lane & mask);
#pragma unroll 1
  for (u32 spins = 0; spins < SPIN_CAP; ++spins) {
    u32x2 f;
    asm volatile("buffer_load_dwordx2 %0, %1, %2, 0 offen sc1\n\ts_waitcnt vmcnt(0)" : "=&v"(f) : "v"(vo), "s"(ws) : "memory");
    if (__all(f.x == tag.x && f.y == tag.y)) break;
    __builtin_amdgcn_s_sleep(1);
  }
  asm volatile("" ::: "memory");
}
DI void lds_signal(LAS char* smem, int word, int lane) {
  waitl();
  if (lane == 0) __hip_atomic_fetch_add((LAS u32*)(smem + L_FLAG) + word, 1u, __ATOMIC_RELAXED, __HIP_MEMORY_SCOPE_WORKGROUP);
}
DI void lds_await(LAS char* smem, int word, u32 want) {
  for (u32 spins = 0; spins < (SPIN_CAP << 3); ++spins) {
    const u32 v = __hip_atomic_load((LAS u32*)(smem + L_FLAG) + word, __ATOMIC_RELAXED, __HIP_MEMORY_SCOPE_WORKGROUP);
    if (__builtin_amdgcn_readfirstlane(v) >= want) break;
  }
  asm volatile("" ::: "memory");
}

#define STAMP(i)
#define STAMP_OUT()

struct Params {
  const u16 *q, *k, *v, *sk, *h, *dd, *x, *wqkv, *wo;
  u16* out;
  char* ws;
};

struct Frags { u32x4 x[4]; u32x4 w[6]; };
DI u32x4 tr_frag(LAS char* a, int d) {
  s16x4 lo = __builtin_amdgcn_ds_read_tr16_b64_v4i16((LAS s16x4*)a);
  s16x4 hi = __builtin_amdgcn_ds_read_tr16_b64_v4i16((LAS s16x4*)(a + d));
  return __builtin_bit_cast(u32x4, __builtin_shufflevector(lo, hi, 0, 1, 2, 3, 4, 5, 6, 7));
}
DI void ldfr(Frags& f, LAS char* xa, LAS char* wb0, LAS char* wb1, int ks) {
#pragma unroll
  for (int mt = 0; mt < 4; ++mt) f.x[mt] = *(LAS u32x4*)(xa + mt * 2048);
#pragma unroll
  for (int sg = 0; sg < 3; ++sg)
#pragma unroll
    for (int t = 0; t < 2; ++t) f.w[sg * 2 + t] = tr_frag((t ? wb1 : wb0) + sg * 8192 + ks * 4096, 512);
}
DI void mm(f32x4 (&acc)[4][6], const Frags& f) {
#pragma unroll
  for (int n = 0; n < 6; ++n)
#pragma unroll
    for (int m_ = 0; m_ < 4; ++m_) {
      const int mt = (n & 1) ? 3 - m_ : m_;
      if (n < 2) acc[mt][n] = mfma(f.w[n], f.x[mt], acc[mt][n]);
      else acc[mt][n] = mfma(f.x[mt], f.w[n], acc[mt][n]);
    }
}
DI void interleave_b1() {
#pragma unroll
  for (int i = 0; i < 16; ++i) { __builtin_amdgcn_sched_group_barrier(0x8, 1, 0); __builtin_amdgcn_sched_group_barrier(0x100, 1, 0); }
  __builtin_amdgcn_sched_group_barrier(0x8, 8, 0);
}
constexpr float EPS = 1e-6f;
DI float phi(float t) {
  const float e = __builtin_amdgcn_fmed3f(__builtin_amdgcn_exp2f(__builtin_fmaf(t, 1.44269504088896f, -EPS * 1.44269504088896f)), 0.f, 1.f);
  return e + __builtin_fmaxf(t, EPS);
}
DI u32x4 pack_phi(f32x4 a, f32x4 b) { return u32x4{pk2(phi(a[0]), phi(a[1])), pk2(phi(a[2]), phi(a[3])), pk2(phi(b[0]), phi(b[1])), pk2(phi(b[2]), phi(b[3]))}; }
DI u32x4 pack8(f32x4 a, f32x4 b) { return u32x4{pk2(a[0], a[1]), pk2(a[2], a[3]), pk2(b[0], b[1]), pk2(b[2], b[3])}; }
template <int CTRL> DI float dpp_add(float v) {
  return v + __int_as_float(__builtin_amdgcn_update_dpp(0, __float_as_int(v), CTRL, 0xf, 0xf, false));
}
DI float row_allsum(float v) { v = dpp_add<0x128>(v); v = dpp_add<0x124>(v); v = dpp_add<0x122>(v); v = dpp_add<0x121>(v); return v; }

struct CFr { u32x4 a[8]; u32x4 b[8]; };
DI void ldconv(CFr& f, LAS char* ha, LAS char* xb) {
#pragma unroll
  for (int i = 0; i < 8; ++i) {
    f.b[i] = *(LAS u32x4*)(xb + (7 - i) * 512);
    LAS u32* hp = (LAS u32*)(ha + i * 512);
    f.a[i] = u32x4{hp[0], hp[1], hp[2], hp[3]};
  }
}
DI void mmconv(f32x4 (&acc)[8], const CFr& f) {
#pragma unroll
  for (int n = 0; n < 8; ++n)
#pragma unroll
    for (int q = 0; q <= n; ++q) acc[n] = mfma(f.a[q], f.b[n - q], acc[n]);
}
DI void interleave_conv() {
#pragma unroll
  for (int i = 0; i < 24; ++i) { __builtin_amdgcn_sched_group_barrier(0x8, 1, 0); __builtin_amdgcn_sched_group_barrier(0x100, 1, 0); }
  __builtin_amdgcn_sched_group_barrier(0x8, 12, 0);
}

__global__ __launch_bounds__(512, 2) void fused_h3_linattn(Params p) {
  extern __shared__ __attribute__((aligned(16))) char smem_[];
  LAS char* smem = (LAS char*)smem_;
  const int bid = blockIdx.x, xcd = bid & 7, j = bid >> 3;
  const int rb = (xcd & 1) * 8 + (j & 7), cb = (xcd >> 1) * 4 + (j >> 3);
  const int rb2 = xcd * 2 + (j & 1), cb2 = j >> 1;
  const int hx = (cb2 & 1) * 16;
  const int wave = __builtin_amdgcn_readfirstlane(threadIdx.x >> 6), lane = threadIdx.x & 63;
  const rsrc_t ws = mkrsrc(p.ws, WS_BYTES);
  STAMP(0);

  if (wave >= 4) {
    const int hw = wave - 4;
    const rsrc_t rx = mkrsrc(p.x, 2048 * 1024 * 2), rw = mkrsrc(p.wqkv, 1024 * 3072 * 2);
    const int r8 = lane >> 3, c8 = lane & 7;
    u32 vx[2], vw[2];
#pragma unroll
    for (int e = 0; e < 2; ++e) {
      vx[e] = r8 * 2048 + ((c8 ^ (4 * e + (r8 >> 1))) * 16);
      vw[e] = r8 * 6144 + ((c8 ^ ((((r8 >> 1) & 1) | (e << 1)) << 1)) * 16);
    }
    const u32 sx0 = (rb * 128 + hw * 32) * 2048, sw0 = (hw * 16) * 6144 + cb * 128;
    const int rot = 4 * ((rb + cb) & 3);
    auto issue = [&](int s_) {
      const u32 s = (s_ + rot) & 15, base = ((s_ + 1) % 3) * SLOT;
#pragma unroll
      for (int e = 0; e < 4; ++e) dma16(rx, base + (hw * 4 + e) * 1024, vx[e & 1], sx0 + e * 8 * 2048 + s * 128);
#pragma unroll
      for (int sg = 0; sg < 3; ++sg)
#pragma unroll
        for (int e = 0; e < 2; ++e) dma16(rw, base + 16384 + sg * 8192 + (hw * 2 + e) * 1024, vw[e], sw0 + e * 8 * 6144 + sg * 2048 + s * 64 * 6144);
    };
    issue(0); issue(1);
    waitv<10>(); BAR();
    issue(2);
    const u32x2 tag = *(LAS u32x2*)(smem + L_FLAG + 48);
    waitv<10>(); BAR(); issue(3);
    waitv<10>(); BAR(); issue(4);
    const int cg = lane & 15, rg = lane >> 4;
    const int R0 = rb * 128 + hw * 32 + rg * 8;
    u32x2 kk[11], vv[8]; u32x4 tw[2];
    {
      const rsrc_t rk = mkrsrc(p.k, 2048 * 1024 * 2), rv = mkrsrc(p.v, 2048 * 1024 * 2), rs = mkrsrc(p.sk, 1024 * 4 * 2);
      const u32 vo = R0 * 2048 + cb * 128 + cg * 8;
      const u32 vh = R0 == 0 ? 0x40000000u : vo - 3 * 2048;
#pragma unroll
      for (int i = 0; i < 3; ++i) ald64(kk[i], rk, vh, i * 2048);
#pragma unroll
      for (int i = 0; i < 8; ++i) ald64(kk[3 + i], rk, vo, i * 2048);
#pragma unroll
      for (int i = 0; i < 8; ++i) ald64(vv[i], rv, vo, i * 2048);
      ald128(tw[0], rs, cb * 512 + cg * 32, 0); ald128(tw[1], rs, cb * 512 + cg * 32, 16);
    }
    waitv<31>(); BAR(); issue(5);
    waitv<31>(); BAR(); issue(6);
    const u32 kvt_vo = WS_KVT + cb * 262144 + (cg >> 2) * 65536 + (255 - (R0 >> 3)) * 64 + (cg & 3) * 16;
    auto shortconv = [&](int c) {
      float kf[11], wf[4], o[8];
#pragma unroll
      for (int i = 0; i < 11; ++i) kf[i] = (c & 1) ? bhi(kk[i][c >> 1]) : blo(kk[i][c >> 1]);
#pragma unroll
      for (int t = 0; t < 4; ++t) { const u32 d = tw[c >> 1][(c & 1) * 2 + (t >> 1)]; wf[t] = (t & 1) ? bhi(d) : blo(d); }
#pragma unroll
      for (int i = 0; i < 8; ++i) {
        float a = kf[i] * wf[3];
        a = __builtin_fmaf(kf[i + 1], wf[2], a); a = __builtin_fmaf(kf[i + 2], wf[1], a); a = __builtin_fmaf(kf[i + 3], wf[0], a);
        o[i] = a * ((c & 1) ? bhi(vv[i][c >> 1]) : blo(vv[i][c >> 1]));
      }
      st128_sc(u32x4{pk2(o[7], o[6]), pk2(o[5], o[4]), pk2(o[3], o[2]), pk2(o[1], o[0])}, ws, kvt_vo, c * 16384);
    };
    waitv<10>(); BAR(); issue(7);
#pragma unroll
    for (int i = 0; i < 11; ++i) PIN(kk[i]);
#pragma unroll
    for (int i = 0; i < 8; ++i) PIN(vv[i]);
    PIN(tw[0]); PIN(tw[1]);
    shortconv(0); SCHED();
    waitv<11>(); BAR(); issue(8); shortconv(1); SCHED();
    waitv<12>(); BAR(); issue(9); shortconv(2); SCHED();
    waitv<12>(); BAR(); issue(10); shortconv(3); SCHED();
    const int ch = cb * 64 + (rb >> 2) * 16 + hw * 4 + (rb & 3);
    u32x4 hh4[4]; u32x2 dd2;
    {
      const rsrc_t rh = mkrsrc(p.h, 1024 * 2048 * 2), rd = mkrsrc(p.dd, 1024 * 2);
#pragma unroll
      for (int n = 0; n < 4; ++n) ald128(hh4[n], rh, lane * 16, ch * 4096 + n * 1024);
      ald64(dd2, rd, 0, (ch >> 2) * 8);
    }
    waitv<17>(); BAR(); issue(11);
    waitv<16>(); BAR();
    publish(ws, WS_FA + ((cb * 16 + rb) * 4 + hw) * 8, tag, lane);
    issue(12);
    waitv<10>(); BAR(); issue(13);
    waitv<10>(); BAR(); issue(14);
    waitv<10>(); BAR();
    const u32 fa_off = WS_FA + (cb * 16 + (3 - hw) * 4) * 32;
    u32x2 fl;
    ald64_sc(fl, ws, fa_off + (lane & 15) * 8, 0);
    issue(15);
    waitv<10>(); PIN(fl);
    if (!__all(fl.x == tag.x && fl.y == tag.y)) await(ws, fa_off, 15, tag, lane);
    u32x4 xs[4];
#pragma unroll
    for (int n = 0; n < 4; ++n) ald128_sc(xs[n], ws, lane * 16, WS_KVT + cb * 262144 + rb * 16384 + (hw * 4 + n) * 1024);
    BAR();
    LAS char* cr = smem + L_CONV + hw * CH_STRIDE;
#pragma unroll
    for (int n = 0; n < 4; ++n) PIN(hh4[n]);
    PIN(dd2);
    {
      const u32x4 z4 = {0, 0, 0, 0};
      if (lane < 30) *(LAS u32x4*)(cr + 4096 + lane * 16) = z4;
      if (lane < 4) *(LAS u32x4*)(cr + C_O + (lane >> 1) * (C_E - C_O) + (lane & 1) * 16) = z4;
#pragma unroll
      for (int n = 0; n < 4; ++n) *(LAS u32x4*)(cr + C_O + 32 + n * 1024 + lane * 16) = hh4[n];
      waitl();
#pragma unroll
      for (int n = 0; n < 4; ++n) {
        const u32 dp = *(LAS u32*)(cr + C_O + 28 + n * 1024 + lane * 16);
        const u32x4 d = hh4[n];
        *(LAS u32x4*)(cr + C_E + 32 + n * 1024 + lane * 16) =
            u32x4{__builtin_amdgcn_alignbit(d[0], dp, 16), __builtin_amdgcn_alignbit(d[1], d[0], 16),
                  __builtin_amdgcn_alignbit(d[2], d[1], 16), __builtin_amdgcn_alignbit(d[3], d[2], 16)};
      }
    }
    waitv<4>(); BAR();
    waitv<0>();
#pragma unroll
    for (int n = 0; n < 4; ++n) PIN(xs[n]);
#pragma unroll
    for (int n = 0; n < 4; ++n) *(LAS u32x4*)(smem + L_CONV + (lane & 3) * CH_STRIDE + ((hw * 4 + n) * 16 + (lane >> 2)) * 16) = xs[n];
    waitl(); BAR();
    STAMP(1);
    const int R2 = rb * 128 + ((hw >> 1) * 4 + rg) * 16 + (hw & 1) * 8;
    u32x2 qq[8];
    {
      const rsrc_t rq = mkrsrc(p.q, 2048 * 1024 * 2);
#pragma unroll
      for (int i = 0; i < 8; ++i) ald64(qq[i], rq, R2 * 2048 + cb * 128 + cg * 8, i * 2048);
    }
    const rsrc_t rwo = mkrsrc(p.wo, 1024 * 1024 * 2);
    const u32 vwo = r8 * 2048 + ((c8 ^ (((r8 >> 1) & 3) << 1)) * 16);
    const int b16 = lane & 15, kg = lane >> 4;
    LAS char* ha = cr + ((b16 & 1) ? C_O : C_E) + 4 + 4 * (b16 >> 1) + 16 * kg;
    LAS char* xb = cr + (254 - 224 - 2 * b16 + kg) * 16;
    f32x4 acc[8];
#pragma unroll
    for (int n = 0; n < 8; ++n) acc[n] = f32x4{0, 0, 0, 0};
    CFr C0, C1;
    ldconv(C0, ha, xb);
#pragma unroll 1
    for (int it = 0; it < 3; ++it) {
#pragma unroll
      for (int e = 0; e < 4; ++e) { const int st = it * 8 + hw; dma16(rwo, st * 4096 + e * 1024, vwo, ((st ^ hx) * 32 + e * 8) * 2048 + cb2 * 128); }
      SCHED();
      ldconv(C1, ha + it * 128 + 64, xb + it * 128 + 64);
      mmconv(acc, C0); interleave_conv(); SCHED();
#pragma unroll
      for (int e = 0; e < 4; ++e) { const int st = it * 8 + 4 + hw; dma16(rwo, st * 4096 + e * 1024, vwo, ((st ^ hx) * 32 + e * 8) * 2048 + cb2 * 128); }
      SCHED();
      ldconv(C0, ha + it * 128 + 128, xb + it * 128 + 128);
      mmconv(acc, C1); interleave_conv(); SCHED();
    }
    ldconv(C1, ha + 448, xb + 448);
    mmconv(acc, C0); interleave_conv(); SCHED();
    u32x2 xv[8];
#pragma unroll
    for (int n = 0; n < 8; ++n) xv[n] = *(LAS u32x2*)(cr + (2044 - 256 * n - 16 * b16 - 4 * kg) * 2);
    mmconv(acc, C1);
    SCHED();
    waitv<0>();
    lds_signal(smem, 8, lane);
    STAMP(2);
#pragma unroll
    for (int e = 0; e < 8; ++e) { const int st = 24 + hw * 2 + (e >> 2); dma16(rwo, L_CONV + hw * CH_STRIDE + e * 1024, vwo, ((st ^ hx) * 32 + (e & 3) * 8) * 2048 + cb2 * 128); }
    {
      const u32 dw = (ch & 2) ? dd2.y : dd2.x;
      const float dv = (ch & 1) ? bhi(dw) : blo(dw);
      u32x2 y[8];
#pragma unroll
      for (int n = 0; n < 8; ++n)
        y[n] = u32x2{pk2(__builtin_fmaf(bhi(xv[n].y), dv, acc[n][0]), __builtin_fmaf(blo(xv[n].y), dv, acc[n][1])),
                     pk2(__builtin_fmaf(bhi(xv[n].x), dv, acc[n][2]), __builtin_fmaf(blo(xv[n].x), dv, acc[n][3]))};
      const u32 yvo = WS_YT + cb * 262144 + ((kg & 1) * 2 + (b16 >> 3)) * 16384 + (ch & 63) * 256 + (kg >> 1) * 128 + (b16 & 7) * 16;
#pragma unroll
      for (int n = 0; n < 8; n += 2) {
        auto s0 = __builtin_amdgcn_permlane16_swap(y[n].x, y[n + 1].x, false, false);
        auto s1 = __builtin_amdgcn_permlane16_swap(y[n].y, y[n + 1].y, false, false);
        st128_sc(u32x4{s0[0], s1[0], s0[1], s1[1]}, ws, yvo, n * 32768);
      }
    }
    publish(ws, WS_FY + ((cb * 16 + rb) * 4 + hw) * 8, tag, lane);
    lds_signal(smem, 9, lane);
    STAMP(3);
    await(ws, WS_FY + cb * 512, 63, tag, lane);
    STAMP(4);
    {
      u32x4 yy[4];
      const u32 yo = WS_YT + cb * 262144 + rb * 16384 + (lane >> 2) * 1024 + (hw & 1) * 128 + ((hw >> 1) * 4 + (lane & 3)) * 16;
#pragma unroll
      for (int c = 0; c < 4; ++c) yy[c] = ld128_sc(ws, yo, c * 256);
      LAS char* sc = cr + 8192;
#pragma unroll
      for (int c = 0; c < 4; ++c) *(LAS u32x4*)(sc + c * 1024 + lane * 16) = yy[c];
      asm volatile("" ::: "memory");
#pragma unroll
      for (int c = 0; c < 4; ++c) yy[c] = *(LAS u32x4*)(sc + c * 1024 + (cg * 4 + rg) * 16);
#pragma unroll
      for (int i = 0; i < 8; ++i) PIN(qq[i]);
      const rsrc_t ro = mkrsrc(p.out, 2048 * 2048 * 2);
#pragma unroll
      for (int i = 0; i < 8; ++i) {
        float f[4];
#pragma unroll
        for (int c = 0; c < 4; ++c) {
          const float yv = (i & 1) ? bhi(yy[c][i >> 1]) : blo(yy[c][i >> 1]);
          const float qv = (c & 1) ? bhi(qq[i][c >> 1]) : blo(qq[i][c >> 1]);
          f[c] = yv * qv;
        }
        __builtin_amdgcn_raw_buffer_store_b64(u32x2{pk2(f[0], f[1]), pk2(f[2], f[3])}, ro, R2 * 4096 + cb * 128 + cg * 8, i * 4096, OAUX);
      }
    }
    STAMP(5);
    STAMP_OUT();
    return;
  }

  __builtin_amdgcn_s_setprio(1);
  u32x2 tag;
  {
    const u32x2 e = __builtin_amdgcn_raw_buffer_load_b64(ws, WS_EPOCH, 0, AUX_SC1);
    const u64 t = (((u64)e.y << 32) | e.x) * 6364136223846793005ull + 1442695040888963407ull;
    tag = u32x2{(u32)t, (u32)(t >> 32)};
    if (wave == 0 && lane < 16) ((LAS u32*)(smem + L_FLAG))[lane] = lane == 12 ? tag.x : lane == 13 ? tag.y : 0u;
    waitl();
  }
  const int mh = wave & 1, hh = wave >> 1;
  const int r16 = lane & 15, g = lane >> 4, q4 = r16 >> 2, p4 = lane & 3;
  f32x4 acc[4][6];
  {
    const int xo = (64 * mh + r16) * 128 + ((g ^ (r16 >> 1)) * 16);
    const int f = ((q4 >> 1) & 1) | ((g & 1) << 1);
    const int wo = 16384 + (8 * g + q4) * 128 + (((4 * hh + (p4 >> 1)) ^ (f << 1)) * 16) + 8 * (p4 & 1);
#pragma unroll
    for (int a = 0; a < 4; ++a)
#pragma unroll
      for (int b = 0; b < 6; ++b) { const float i0 = b < 4 ? EPS : 0.f; acc[a][b] = f32x4{i0, i0, i0, i0}; PIN(acc[a][b]); }
    Frags F0, F1;
    BAR();
    STAMP(1);
    int so = SLOT;
    ldfr(F0, smem + so + xo, smem + so + wo, smem + so + (wo ^ 32), 0);
#pragma unroll 1
    for (int s = 0; s < 15; ++s) {
      ldfr(F1, smem + so + (xo ^ 64), smem + so + wo, smem + so + (wo ^ 32), 1);
      mm(acc, F0); interleave_b1(); SCHED();
      waitl(); BAR(); SCHED();
      so = (so == 2 * SLOT) ? 0 : so + SLOT;
      ldfr(F0, smem + so + xo, smem + so + wo, smem + so + (wo ^ 32), 0);
      mm(acc, F1); interleave_b1(); SCHED();
    }
    ldfr(F1, smem + so + (xo ^ 64), smem + so + wo, smem + so + (wo ^ 32), 1);
    mm(acc, F0); interleave_b1(); SCHED();
    waitl(); BAR(); SCHED();
    mm(acc, F1);
  }
  SCHED();
  STAMP(2);
  const u32 pslot = (cb * 2 + hh) * 2 + mh;
  {
    f32x4 kvp[2][2], zp[2];
    const f32x4 zero = {0, 0, 0, 0};
    const u32x4 ones = {0x3f803f80u, 0x3f803f80u, 0x3f803f80u, 0x3f803f80u};
#pragma unroll
    for (int u = 0; u < 2; ++u) {
      u32x4 pkf[2], vf[2];
#pragma unroll
      for (int t = 0; t < 2; ++t) { pkf[t] = pack_phi(acc[2 * u][2 + t], acc[2 * u + 1][2 + t]); vf[t] = pack8(acc[2 * u][4 + t], acc[2 * u + 1][4 + t]); }
#pragma unroll
      for (int dt = 0; dt < 2; ++dt) {
#pragma unroll
        for (int et = 0; et < 2; ++et) kvp[dt][et] = mfma(pkf[dt], vf[et], u ? kvp[dt][et] : zero);
        zp[dt] = mfma(pkf[dt], ones, u ? zp[dt] : zero);
      }
    }
    LAS char* mine = smem + L_X1 + wave * 2304;
    LAS char* theirs = smem + L_X1 + (wave ^ 1) * 2304;
    f32x4 keep[2];
#pragma unroll
    for (int dt = 0; dt < 2; ++dt) {
      keep[dt] = mh ? kvp[dt][1] : kvp[dt][0];
      *(LAS f32x4*)(mine + dt * 1024 + lane * 16) = mh ? kvp[dt][0] : kvp[dt][1];
      if (r16 == 0) *(LAS f32x4*)(mine + 2048 + dt * 64 + g * 16) = zp[dt];
    }
    lds_signal(smem, wave, lane);
    lds_await(smem, wave ^ 1, 1);
#pragma unroll
    for (int dt = 0; dt < 2; ++dt) {
      keep[dt] += *(LAS f32x4*)(theirs + dt * 1024 + lane * 16);
      zp[dt] += *(LAS f32x4*)(theirs + 2048 + dt * 64 + g * 16);
      st128_sc(__builtin_bit_cast(u32x4, keep[dt]), ws, lane * 16, WS_PART + pslot * 32768 + rb * 2048 + dt * 1024);
    }
    if (r16 < 2) st128_sc(__builtin_bit_cast(u32x4, r16 ? zp[1] : zp[0]), ws, r16 * 64 + g * 16, WS_Z + pslot * 2048 + rb * 128);
  }
  SCHED();
  u32x4 pq[4];
#pragma unroll
  for (int mt = 0; mt < 4; ++mt) { pq[mt] = pack_phi(acc[mt][0], acc[mt][1]); PIN(pq[mt]); }
  SCHED();
  publish(ws, WS_F1 + ((cb * 4 + wave) * 16 + rb) * 8, tag, lane);
  STAMP(3);
  await(ws, WS_F1 + (cb * 4 + wave) * 128, 15, tag, lane);
  STAMP(4);
  {
    u32x4 pr[16][2], zr[2];
#pragma unroll
    for (int dt = 0; dt < 2; ++dt) zr[dt] = ld128_sc(ws, r16 * 128 + dt * 64 + g * 16, WS_Z + pslot * 2048);
#pragma unroll
    for (int r = 0; r < 16; ++r)
#pragma unroll
      for (int dt = 0; dt < 2; ++dt) pr[r][dt] = ld128_sc(ws, lane * 16 + ((r & 1) * 2 + dt) * 1024, WS_PART + pslot * 32768 + (r >> 1) * 4096);
    SCHED();
    f32x4 zs[2], sum[2];
#pragma unroll
    for (int dt = 0; dt < 2; ++dt) {
#pragma unroll
      for (int r = 0; r < 4; ++r) zs[dt][r] = row_allsum(__uint_as_float(zr[dt][r]));
      sum[dt] = __builtin_bit_cast(f32x4, pr[0][dt]);
    }
#pragma unroll
    for (int r = 1; r < 16; ++r)
#pragma unroll
      for (int dt = 0; dt < 2; ++dt) sum[dt] += __builtin_bit_cast(f32x4, pr[r][dt]);
    const u32x4 zf = pack8(zs[0], zs[1]);
    *(LAS u32x4*)(smem + L_X2 + wave * 1024 + lane * 16) = pack8(sum[0], sum[1]);
    lds_signal(smem, 4 + wave, lane);
    lds_await(smem, 4 + (wave ^ 1), 1);
    const u32x4 kv0 = *(LAS u32x4*)(smem + L_X2 + hh * 2048 + lane * 16);
    const u32x4 kv1 = *(LAS u32x4*)(smem + L_X2 + hh * 2048 + 1024 + lane * 16);
    const f32x4 zero = {0, 0, 0, 0}, eps4 = {EPS, EPS, EPS, EPS};
#pragma unroll
    for (int mt = 0; mt < 4; ++mt) {
      const f32x4 den = mfma(zf, pq[mt], eps4);
      const f32x4 n0 = mfma(kv0, pq[mt], zero), n1 = mfma(kv1, pq[mt], zero);
      const float inv = __builtin_amdgcn_rcpf(den[0]);
      st128_sc(pack8(n0 * inv, n1 * inv), ws, lane * 16 + (mt & 1) * 1024, WS_O + rb * 262144 + (mh * 2 + (mt >> 1)) * 65536 + (cb * 2 + hh) * 2048);
    }
  }
  STAMP(5);
  publish(ws, WS_F2 + (((rb * 2 + mh) * 16 + cb) * 2 + hh) * 8, tag, lane);
  STAMP(6);
  await(ws, WS_F2 + (rb2 * 2 + (wave >> 1)) * 256, 31, tag, lane);
  STAMP(7);
  if (bid == 0 && wave == 0 && lane == 0) __builtin_amdgcn_raw_buffer_store_b64(tag, ws, WS_EPOCH, 0, AUX_SC1);
  {
    constexpr int WIN = 16;
    u32x4 of[WIN][2];
    const u32 ob = WS_O + rb2 * 262144 + wave * 65536;
    auto ldo = [&](int st) {
#pragma unroll
      for (int m = 0; m < 2; ++m) of[st % WIN][m] = ld128_sc(ws, lane * 16 + ((st & 1) * 2 + m) * 1024, ob + (((st & ~1) ^ hx) * 2048));
    };
#pragma unroll
    for (int st = 0; st < WIN; ++st) ldo(st);
    SCHED();
    lds_await(smem, 8, 4);
    const int fw = (2 * g + (q4 >> 1)) & 3;
    int wb[4];
#pragma unroll
    for (int t = 0; t < 4; ++t) wb[t] = (4 * g + q4) * 128 + ((t ^ fw) * 32) + p4 * 8;
    auto ldw = [&](u32x4 (&w)[4], int st) {
      const int base = st < 24 ? st * 4096 : L_CONV + ((st - 24) >> 1) * CH_STRIDE + (st & 1) * 4096;
#pragma unroll
      for (int t = 0; t < 4; ++t) w[t] = tr_frag(smem + base + wb[t], 2048);
    };
    f32x4 oc[2][4];
#pragma unroll
    for (int m = 0; m < 2; ++m)
#pragma unroll
      for (int t = 0; t < 4; ++t) oc[m][t] = f32x4{0, 0, 0, 0};
    u32x4 wf[2][4];
    ldw(wf[0], 0);
#pragma unroll
    for (int st = 0; st < 32; ++st) {
      if (st == 23) lds_await(smem, 9, 4);
      if (st < 31) ldw(wf[(st + 1) & 1], st + 1);
#pragma unroll
      for (int t = 0; t < 4; ++t)
#pragma unroll
        for (int m = 0; m < 2; ++m) oc[m][t] = mfma(wf[st & 1][t], of[st % WIN][m], oc[m][t]);
#pragma unroll
      for (int i = 0; i < 8; ++i) { __builtin_amdgcn_sched_group_barrier(0x8, 1, 0); __builtin_amdgcn_sched_group_barrier(0x100, 1, 0); }
      SCHED();
      if (st + WIN < 32) { ldo(st + WIN); SCHED(); }
    }
    STAMP(8);
    const rsrc_t ro = mkrsrc(p.out, 2048 * 2048 * 2);
    LAS char* sc = smem + L_X1 + (wave ^ 1) * 2304;
#pragma unroll
    for (int m = 0; m < 2; ++m) {
#pragma unroll
      for (int t = 0; t < 4; ++t) *(LAS u32x2*)(sc + r16 * 144 + t * 32 + g * 8) = u32x2{pk2(oc[m][t][0], oc[m][t][1]), pk2(oc[m][t][2], oc[m][t][3])};
      asm volatile("" ::: "memory");
#pragma unroll
      for (int e = 0; e < 2; ++e) {
        const u32x4 r = *(LAS u32x4*)(sc + (e * 8 + (lane >> 3)) * 144 + (lane & 7) * 16);
        __builtin_amdgcn_raw_buffer_store_b128(r, ro, (lane >> 3) * 4096 + (lane & 7) * 16, (rb2 * 128 + wave * 32 + m * 16 + e * 8) * 4096 + 2048 + cb2 * 128, OAUX);
      }
      asm volatile("" ::: "memory");
    }
  }
  STAMP(9);
  STAMP_OUT();
}

extern "C" void kernel_launch(void* const* d_in, const int* in_sizes, int n_in, void* d_out, int out_size, void* d_ws, size_t ws_size,
                              hipStream_t stream) {
  Params p;
  p.q = (const u16*)d_in[0]; p.k = (const u16*)d_in[1]; p.v = (const u16*)d_in[2]; p.sk = (const u16*)d_in[3];
  p.h = (const u16*)d_in[4]; p.dd = (const u16*)d_in[5]; p.x = (const u16*)d_in[6]; p.wqkv = (const u16*)d_in[7];
  p.wo = (const u16*)d_in[8]; p.out = (u16*)d_out; p.ws = (char*)d_ws;
  (void)hipFuncSetAttribute((const void*)fused_h3_linattn, hipFuncAttributeMaxDynamicSharedMemorySize, LDS_BYTES);
  fused_h3_linattn<<<256, 512, LDS_BYTES, stream>>>(p);
}
```

```cpp
#define OAUX 16
#define PCOND (wave<8)
#define P0 77
#define P1 77
#include <hip/hip_runtime.h>
#include <hip/hip_bf16.h>

typedef unsigned short u16;
typedef unsigned int u32;
typedef unsigned long long u64;
typedef float f32x2 __attribute__((ext_vector_type(2)));
typedef float f32x4 __attribute__((ext_vector_type(4)));
typedef u32 u32x2 __attribute__((ext_vector_type(2)));
typedef u32 u32x4 __attribute__((ext_vector_type(4)));
typedef short s16x4 __attribute__((ext_vector_type(4)));
typedef short s16x8 __attribute__((ext_vector_type(8)));
typedef __bf16 bf16x2 __attribute__((ext_vector_type(2)));
typedef __bf16 bf16x8 __attribute__((ext_vector_type(8)));
typedef __amdgpu_buffer_rsrc_t rsrc_t;
#define LAS __attribute__((address_space(3)))
#define GAS __attribute__((address_space(1)))
#define DI __device__ __forceinline__

#define USE_MEMSET 0
#define CODE_PF 0
#define STAMPS 0

constexpr u32 WS_EPOCH = 0;
constexpr u32 WS_F1 = 4096;
constexpr u32 WS_F2 = WS_F1 + 8192;
constexpr u32 WS_FA = WS_F2 + 8192;
constexpr u32 WS_FY = WS_FA + 8192;
constexpr u32 WS_CTRL_END = WS_FY + 8192;
constexpr u32 WS_Z = 512u << 10;
constexpr u32 WS_PART = 1u << 20;
constexpr u32 WS_O = 4u << 20;
constexpr u32 WS_KVT = 8u << 20;
constexpr u32 WS_YT = 12u << 20;
constexpr u32 WS_BYTES = 16u << 20;
constexpr u32 WS_DBG = 32u << 20;

constexpr int SLOT = 40960;
constexpr int L_X1 = 98304;
constexpr int L_X2 = L_X1 + 4 * 2304;
constexpr int CH_STRIDE = 12960;
constexpr int L_CONV = 163840 - 4 * CH_STRIDE;
constexpr int C_O = 4608, C_E = C_O + 4160;
constexpr int L_FLAG = 163840 - 64;
constexpr int LDS_BYTES = 163840;
static_assert(L_X2 + 4096 <= L_CONV, "lds map");

constexpr int AUX_SC1 = 16;
constexpr u32 SPIN_CAP = 1u << 20;

DI f32x4 mfma(u32x4 a, u32x4 b, f32x4 c) {
  return __builtin_amdgcn_mfma_f32_16x16x32_bf16(__builtin_bit_cast(bf16x8, a), __builtin_bit_cast(bf16x8, b), c, 0, 0, 0);
}
DI u32 pk2(float lo, float hi) {
  bf16x2 r = __builtin_convertvector(f32x2{lo, hi}, bf16x2);
  return __builtin_bit_cast(u32, r);
}
DI float blo(u32 x) { return __uint_as_float(x << 16); }
DI float bhi(u32 x) { return __uint_as_float(x & 0xffff0000u); }
template <int N> DI void waitv() { asm volatile("s_waitcnt vmcnt(%0)" ::"n"(N) : "memory"); }
DI void waitl() { asm volatile("s_waitcnt lgkmcnt(0)" ::: "memory"); }
#define BAR() do { asm volatile("" ::: "memory"); __builtin_amdgcn_s_barrier(); asm volatile("" ::: "memory"); } while (0)
#define SCHED() __builtin_amdgcn_sched_barrier(0)
DI rsrc_t mkrsrc(const void* p, u32 bytes) { return __builtin_amdgcn_make_buffer_rsrc((void*)p, 0, bytes, 0x20000); }
DI void dma16(rsrc_t r, u32 lds, u32 voff, u32 soff) {
  asm volatile("s_mov_b32 m0, %0\n\ts_nop 0\n\tbuffer_load_dwordx4 %1, %2, %3 offen lds" ::"s"(lds), "v"(voff), "s"(r), "s"(soff) : "memory");
}
DI void ald64(u32x2& d, rsrc_t r, u32 voff, u32 soff) {
  asm volatile("buffer_load_dwordx2 %0, %1, %2, %3 offen" : "=v"(d) : "v"(voff), "s"(r), "s"(soff) : "memory");
}
DI void ald64_sc(u32x2& d, rsrc_t r, u32 voff, u32 soff) {
  asm volatile("buffer_load_dwordx2 %0, %1, %2, %3 offen sc1" : "=v"(d) : "v"(voff), "s"(r), "s"(soff) : "memory");
}
DI void ald128(u32x4& d, rsrc_t r, u32 voff, u32 soff) {
  asm volatile("buffer_load_dwordx4 %0, %1, %2, %3 offen" : "=v"(d) : "v"(voff), "s"(r), "s"(soff) : "memory");
}
DI void ald128_sc(u32x4& d, rsrc_t r, u32 voff, u32 soff) {
  asm volatile("buffer_load_dwordx4 %0, %1, %2, %3 offen sc1" : "=v"(d) : "v"(voff), "s"(r), "s"(soff) : "memory");
}
#define PIN(x) asm volatile("" : "+v"(x))
DI u32x4 ld128_sc(rsrc_t r, u32 voff, u32 soff) { return __builtin_amdgcn_raw_buffer_load_b128(r, voff, soff, AUX_SC1); }
DI void st128_sc(u32x4 v, rsrc_t r, u32 voff, u32 soff) { __builtin_amdgcn_raw_buffer_store_b128(v, r, voff, soff, AUX_SC1); }

DI void publish(rsrc_t ws, u32 off, u32x2 tag, int lane) {
  waitv<0>();
  if (lane == 0) __builtin_amdgcn_raw_buffer_store_b64(tag, ws, off, 0, AUX_SC1);
}
DI void await(rsrc_t ws, u32 off, int mask, u32x2 tag, int lane) {
  const u32 vo = off + 8u * (lane & mask);
#pragma unroll 1
  for (u32 spins = 0; spins < SPIN_CAP; ++spins) {
    u32x2 f;
    asm volatile("buffer_load_dwordx2 %0, %1, %2, 0 offen sc1\n\ts_waitcnt vmcnt(0)" : "=&v"(f) : "v"(vo), "s"(ws) : "memory");
    if (__all(f.x == tag.x && f.y == tag.y)) break;
    __builtin_amdgcn_s_sleep(1);
  }
  asm volatile("" ::: "memory");
}
DI void lds_signal(LAS char* smem, int word, int lane) {
  waitl();
  if (lane == 0) __hip_atomic_fetch_add((LAS u32*)(smem + L_FLAG) + word, 1u, __ATOMIC_RELAXED, __HIP_MEMORY_SCOPE_WORKGROUP);
}
DI void lds_await(LAS char* smem, int word, u32 want) {
  for (u32 spins = 0; spins < (SPIN_CAP << 3); ++spins) {
    const u32 v = __hip_atomic_load((LAS u32*)(smem + L_FLAG) + word, __ATOMIC_RELAXED, __HIP_MEMORY_SCOPE_WORKGROUP);
    if (__builtin_amdgcn_readfirstlane(v) >= want) break;
  }
  asm volatile("" ::: "memory");
}

#define STAMP(i) do { if ((i)==0 || (i)==P0 || (i)==P1) { u64 t_; asm volatile("s_memrealtime %0\n\ts_waitcnt lgkmcnt(0)" : "=s"(t_) :: "memory"); if((i)==0) ts0=t_; if((i)==P0) tsa=t_; if((i)==P1) tsb=t_; } } while(0)
#define STAMP_OUT() do { if (PCOND) { const u64 until = ts0 + 10000 + 10*(tsb-tsa); for(int z=0;z<100000;++z){ u64 t_; asm volatile("s_memrealtime %0\n\ts_waitcnt lgkmcnt(0)" : "=s"(t_) :: "memory"); if(t_>=until) break; __builtin_amdgcn_s_sleep(2);} } } while(0)

struct Params {
  const u16 *q, *k, *v, *sk, *h, *dd, *x, *wqkv, *wo;
  u16* out;
  char* ws;
};

struct Frags { u32x4 x[4]; u32x4 w[6]; };
DI u32x4 tr_frag(LAS char* a, int d) {
  s16x4 lo = __builtin_amdgcn_ds_read_tr16_b64_v4i16((LAS s16x4*)a);
  s16x4 hi = __builtin_amdgcn_ds_read_tr16_b64_v4i16((LAS s16x4*)(a + d));
  return __builtin_bit_cast(u32x4, __builtin_shufflevector(lo, hi, 0, 1, 2, 3, 4, 5, 6, 7));
}
DI void ldfr(Frags& f, LAS char* xa, LAS char* wb0, LAS char* wb1, int ks) {
#pragma unroll
  for (int mt = 0; mt < 4; ++mt) f.x[mt] = *(LAS u32x4*)(xa + mt * 2048);
#pragma unroll
  for (int sg = 0; sg < 3; ++sg)
#pragma unroll
    for (int t = 0; t < 2; ++t) f.w[sg * 2 + t] = tr_frag((t ? wb1 : wb0) + sg * 8192 + ks * 4096, 512);
}
DI void mm(f32x4 (&acc)[4][6], const Frags& f) {
#pragma unroll
  for (int n = 0; n < 6; ++n)
#pragma unroll
    for (int m_ = 0; m_ < 4; ++m_) {
      const int mt = (n & 1) ? 3 - m_ : m_;
      if (n < 2) acc[mt][n] = mfma(f.w[n], f.x[mt], acc[mt][n]);
      else acc[mt][n] = mfma(f.x[mt], f.w[n], acc[mt][n]);
    }
}
DI void interleave_b1() {
#pragma unroll
  for (int i = 0; i < 16; ++i) { __builtin_amdgcn_sched_group_barrier(0x8, 1, 0); __builtin_amdgcn_sched_group_barrier(0x100, 1, 0); }
  __builtin_amdgcn_sched_group_barrier(0x8, 8, 0);
}
constexpr float EPS = 1e-6f;
DI float phi(float t) {
  const float e = __builtin_amdgcn_fmed3f(__builtin_amdgcn_exp2f(__builtin_fmaf(t, 1.44269504088896f, -EPS * 1.44269504088896f)), 0.f, 1.f);
  return e + __builtin_fmaxf(t, EPS);
}
DI u32x4 pack_phi(f32x4 a, f32x4 b) { return u32x4{pk2(phi(a[0]), phi(a[1])), pk2(phi(a[2]), phi(a[3])), pk2(phi(b[0]), phi(b[1])), pk2(phi(b[2]), phi(b[3]))}; }
DI u32x4 pack8(f32x4 a, f32x4 b) { return u32x4{pk2(a[0], a[1]), pk2(a[2], a[3]), pk2(b[0], b[1]), pk2(b[2], b[3])}; }
template <int CTRL> DI float dpp_add(float v) {
  return v + __int_as_float(__builtin_amdgcn_update_dpp(0, __float_as_int(v), CTRL, 0xf, 0xf, false));
}
DI float row_allsum(float v) { v = dpp_add<0x128>(v); v = dpp_add<0x124>(v); v = dpp_add<0x122>(v); v = dpp_add<0x121>(v); return v; }

struct CFr { u32x4 a[8]; u32x4 b[8]; };
DI void ldconv(CFr& f, LAS char* ha, LAS char* xb) {
#pragma unroll
  for (int i = 0; i < 8; ++i) {
    f.b[i] = *(LAS u32x4*)(xb + (7 - i) * 512);
    LAS u32* hp = (LAS u32*)(ha + i * 512);
    f.a[i] = u32x4{hp[0], hp[1], hp[2], hp[3]};
  }
}
DI void mmconv(f32x4 (&acc)[8], const CFr& f) {
#pragma unroll
  for (int n = 0; n < 8; ++n)
#pragma unroll
    for (int q = 0; q <= n; ++q) acc[n] = mfma(f.a[q], f.b[n - q], acc[n]);
}
DI void interleave_conv() {
#pragma unroll
  for (int i = 0; i < 24; ++i) { __builtin_amdgcn_sched_group_barrier(0x8, 1, 0); __builtin_amdgcn_sched_group_barrier(0x100, 1, 0); }
  __builtin_amdgcn_sched_group_barrier(0x8, 12, 0);
}

__global__ __launch_bounds__(512, 2) void fused_h3_linattn(Params p) {
  extern __shared__ __attribute__((aligned(16))) char smem_[];
  LAS char* smem = (LAS char*)smem_;
  const int bid = blockIdx.x, xcd = bid & 7, j = bid >> 3;
  const int rb = (xcd & 1) * 8 + (j & 7), cb = (xcd >> 1) * 4 + (j >> 3);
  const int rb2 = xcd * 2 + (j & 1), cb2 = j >> 1;
  const int hx = (cb2 & 1) * 16;
  const int wave = __builtin_amdgcn_readfirstlane(threadIdx.x >> 6), lane = threadIdx.x & 63;
  const rsrc_t ws = mkrsrc(p.ws, WS_BYTES);
  u64 ts0=0,tsa=0,tsb=0;
  STAMP(0);

  {
    u64 ta_, tb_;
    asm volatile("s_memrealtime %0\n\ts_waitcnt lgkmcnt(0)" : "=s"(ta_) :: "memory");
    { u32 a_=0; { const rsrc_t r_=mkrsrc(p.x,4u<<20); u32x4 v0_=__builtin_amdgcn_raw_buffer_load_b128(r_, bid*16384+threadIdx.x*16,0,0); u32x4 v1_=__builtin_amdgcn_raw_buffer_load_b128(r_, bid*16384+8192+threadIdx.x*16,0,0); a_^=v0_.x^v1_.y; } { const rsrc_t r_=mkrsrc(p.wqkv,4u<<20); u32x4 v0_=__builtin_amdgcn_raw_buffer_load_b128(r_, bid*16384+threadIdx.x*16,0,0); u32x4 v1_=__builtin_amdgcn_raw_buffer_load_b128(r_, bid*16384+8192+threadIdx.x*16,0,0); a_^=v0_.x^v1_.y; } { const rsrc_t r_=mkrsrc((p.wqkv+(1<<20)),4u<<20); u32x4 v0_=__builtin_amdgcn_raw_buffer_load_b128(r_, bid*16384+threadIdx.x*16,0,0); u32x4 v1_=__builtin_amdgcn_raw_buffer_load_b128(r_, bid*16384+8192+threadIdx.x*16,0,0); a_^=v0_.x^v1_.y; } { const rsrc_t r_=mkrsrc(p.x,4u<<20); u32x4 v0_=__builtin_amdgcn_raw_buffer_load_b128(r_, bid*16384+threadIdx.x*16,0,0); u32x4 v1_=__builtin_amdgcn_raw_buffer_load_b128(r_, bid*16384+8192+threadIdx.x*16,0,0); a_^=v0_.x^v1_.y; } asm volatile(""::"v"(a_)); }
    asm volatile("s_waitcnt vmcnt(0)\n\ts_memrealtime %0\n\ts_waitcnt lgkmcnt(0)" : "=s"(tb_) :: "memory");
    tsa=ta_; tsb=tb_;
  }

  if (wave >= 4) {
    const int hw = wave - 4;
    const rsrc_t rx = mkrsrc(p.x, 2048 * 1024 * 2), rw = mkrsrc(p.wqkv, 1024 * 3072 * 2);
    const int r8 = lane >> 3, c8 = lane & 7;
    u32 vx[2], vw[2];
#pragma unroll
    for (int e = 0; e < 2; ++e) {
      vx[e] = r8 * 2048 + ((c8 ^ (4 * e + (r8 >> 1))) * 16);
      vw[e] = r8 * 6144 + ((c8 ^ ((((r8 >> 1) & 1) | (e << 1)) << 1)) * 16);
    }
    const u32 sx0 = (rb * 128 + hw * 32) * 2048, sw0 = (hw * 16) * 6144 + cb * 128;
    const int rot = 4 * ((rb + cb) & 3);
    auto issue = [&](int s_) {
      const u32 s = (s_ + rot) & 15, base = ((s_ + 1) % 3) * SLOT;
#pragma unroll
      for (int e = 0; e < 4; ++e) dma16(rx, base + (hw * 4 + e) * 1024, vx[e & 1], sx0 + e * 8 * 2048 + s * 128);
#pragma unroll
      for (int sg = 0; sg < 3; ++sg)
#pragma unroll
        for (int e = 0; e < 2; ++e) dma16(rw, base + 16384 + sg * 8192 + (hw * 2 + e) * 1024, vw[e], sw0 + e * 8 * 6144 + sg * 2048 + s * 64 * 6144);
    };
    issue(0); issue(1);
    waitv<10>(); BAR();
    issue(2);
    const u32x2 tag = *(LAS u32x2*)(smem + L_FLAG + 48);
    waitv<10>(); BAR(); issue(3);
    waitv<10>(); BAR(); issue(4);
    const int cg = lane & 15, rg = lane >> 4;
    const int R0 = rb * 128 + hw * 32 + rg * 8;
    u32x2 kk[11], vv[8]; u32x4 tw[2];
    {
      const rsrc_t rk = mkrsrc(p.k, 2048 * 1024 * 2), rv = mkrsrc(p.v, 2048 * 1024 * 2), rs = mkrsrc(p.sk, 1024 * 4 * 2);
      const u32 vo = R0 * 2048 + cb * 128 + cg * 8;
      const u32 vh = R0 == 0 ? 0x40000000u : vo - 3 * 2048;
#pragma unroll
      for (int i = 0; i < 3; ++i) ald64(kk[i], rk, vh, i * 2048);
#pragma unroll
      for (int i = 0; i < 8; ++i) ald64(kk[3 + i], rk, vo, i * 2048);
#pragma unroll
      for (int i = 0; i < 8; ++i) ald64(vv[i], rv, vo, i * 2048);
      ald128(tw[0], rs, cb * 512 + cg * 32, 0); ald128(tw[1], rs, cb * 512 + cg * 32, 16);
    }
    waitv<31>(); BAR(); issue(5);
    waitv<31>(); BAR(); issue(6);
    const u32 kvt_vo = WS_KVT + cb * 262144 + (cg >> 2) * 65536 + (255 - (R0 >> 3)) * 64 + (cg & 3) * 16;
    auto shortconv = [&](int c) {
      float kf[11], wf[4], o[8];
#pragma unroll
      for (int i = 0; i < 11; ++i) kf[i] = (c & 1) ? bhi(kk[i][c >> 1]) : blo(kk[i][c >> 1]);
#pragma unroll
      for (int t = 0; t < 4; ++t) { const u32 d = tw[c >> 1][(c & 1) * 2 + (t >> 1)]; wf[t] = (t & 1) ? bhi(d) : blo(d); }
#pragma unroll
      for (int i = 0; i < 8; ++i) {
        float a = kf[i] * wf[3];
        a = __builtin_fmaf(kf[i + 1], wf[2], a); a = __builtin_fmaf(kf[i + 2], wf[1], a); a = __builtin_fmaf(kf[i + 3], wf[0], a);
        o[i] = a * ((c & 1) ? bhi(vv[i][c >> 1]) : blo(vv[i][c >> 1]));
      }
      st128_sc(u32x4{pk2(o[7], o[6]), pk2(o[5], o[4]), pk2(o[3], o[2]), pk2(o[1], o[0])}, ws, kvt_vo, c * 16384);
    };
    waitv<10>(); BAR(); issue(7);
#pragma unroll
    for (int i = 0; i < 11; ++i) PIN(kk[i]);
#pragma unroll
    for (int i = 0; i < 8; ++i) PIN(vv[i]);
    PIN(tw[0]); PIN(tw[1]);
    shortconv(0); SCHED();
    waitv<11>(); BAR(); issue(8); shortconv(1); SCHED();
    waitv<12>(); BAR(); issue(9); shortconv(2); SCHED();
    waitv<12>(); BAR(); issue(10); shortconv(3); SCHED();
    const int ch = cb * 64 + (rb >> 2) * 16 + hw * 4 + (rb & 3);
    u32x4 hh4[4]; u32x2 dd2;
    {
      const rsrc_t rh = mkrsrc(p.h, 1024 * 2048 * 2), rd = mkrsrc(p.dd, 1024 * 2);
#pragma unroll
      for (int n = 0; n < 4; ++n) ald128(hh4[n], rh, lane * 16, ch * 4096 + n * 1024);
      ald64(dd2, rd, 0, (ch >> 2) * 8);
    }
    waitv<17>(); BAR(); issue(11);
    waitv<16>(); BAR();
    publish(ws, WS_FA + ((cb * 16 + rb) * 4 + hw) * 8, tag, lane);
    issue(12);
    waitv<10>(); BAR(); issue(13);
    waitv<10>(); BAR(); issue(14);
    waitv<10>(); BAR();
    const u32 fa_off = WS_FA + (cb * 16 + (3 - hw) * 4) * 32;
    u32x2 fl;
    ald64_sc(fl, ws, fa_off + (lane & 15) * 8, 0);
    issue(15);
    waitv<10>(); PIN(fl);
    if (!__all(fl.x == tag.x && fl.y == tag.y)) await(ws, fa_off, 15, tag, lane);
    u32x4 xs[4];
#pragma unroll
    for (int n = 0; n < 4; ++n) ald128_sc(xs[n], ws, lane * 16, WS_KVT + cb * 262144 + rb * 16384 + (hw * 4 + n) * 1024);
    BAR();
    LAS char* cr = smem + L_CONV + hw * CH_STRIDE;
#pragma unroll
    for (int n = 0; n < 4; ++n) PIN(hh4[n]);
    PIN(dd2);
    {
      const u32x4 z4 = {0, 0, 0, 0};
      if (lane < 30) *(LAS u32x4*)(cr + 4096 + lane * 16) = z4;
      if (lane < 4) *(LAS u32x4*)(cr + C_O + (lane >> 1) * (C_E - C_O) + (lane & 1) * 16) = z4;
#pragma unroll
      for (int n = 0; n < 4; ++n) *(LAS u32x4*)(cr + C_O + 32 + n * 1024 + lane * 16) = hh4[n];
      waitl();
#pragma unroll
      for (int n = 0; n < 4; ++n) {
        const u32 dp = *(LAS u32*)(cr + C_O + 28 + n * 1024 + lane * 16);
        const u32x4 d = hh4[n];
        *(LAS u32x4*)(cr + C_E + 32 + n * 1024 + lane * 16) =
            u32x4{__builtin_amdgcn_alignbit(d[0], dp, 16), __builtin_amdgcn_alignbit(d[1], d[0], 16),
                  __builtin_amdgcn_alignbit(d[2], d[1], 16), __builtin_amdgcn_alignbit(d[3], d[2], 16)};
      }
    }
    waitv<4>(); BAR();
    waitv<0>();
#pragma unroll
    for (int n = 0; n < 4; ++n) PIN(xs[n]);
#pragma unroll
    for (int n = 0; n < 4; ++n) *(LAS u32x4*)(smem + L_CONV + (lane & 3) * CH_STRIDE + ((hw * 4 + n) * 16 + (lane >> 2)) * 16) = xs[n];
    waitl(); BAR();
    STAMP(1);
    const int R2 = rb * 128 + ((hw >> 1) * 4 + rg) * 16 + (hw & 1) * 8;
    u32x2 qq[8];
    {
      const rsrc_t rq = mkrsrc(p.q, 2048 * 1024 * 2);
#pragma unroll
      for (int i = 0; i < 8; ++i) ald64(qq[i], rq, R2 * 2048 + cb * 128 + cg * 8, i * 2048);
    }
    const rsrc_t rwo = mkrsrc(p.wo, 1024 * 1024 * 2);
    const u32 vwo = r8 * 2048 + ((c8 ^ (((r8 >> 1) & 3) << 1)) * 16);
    const int b16 = lane & 15, kg = lane >> 4;
    LAS char* ha = cr + ((b16 & 1) ? C_O : C_E) + 4 + 4 * (b16 >> 1) + 16 * kg;
    LAS char* xb = cr + (254 - 224 - 2 * b16 + kg) * 16;
    f32x4 acc[8];
#pragma unroll
    for (int n = 0; n < 8; ++n) acc[n] = f32x4{0, 0, 0, 0};
    CFr C0, C1;
    ldconv(C0, ha, xb);
#pragma unroll 1
    for (int it = 0; it < 3; ++it) {
#pragma unroll
      for (int e = 0; e < 4; ++e) { const int st = it * 8 + hw; dma16(rwo, st * 4096 + e * 1024, vwo, ((st ^ hx) * 32 + e * 8) * 2048 + cb2 * 128); }
      SCHED();
      ldconv(C1, ha + it * 128 + 64, xb + it * 128 + 64);
      mmconv(acc, C0); interleave_conv(); SCHED();
#pragma unroll
      for (int e = 0; e < 4; ++e) { const int st = it * 8 + 4 + hw; dma16(rwo, st * 4096 + e * 1024, vwo, ((st ^ hx) * 32 + e * 8) * 2048 + cb2 * 128); }
      SCHED();
      ldconv(C0, ha + it * 128 + 128, xb + it * 128 + 128);
      mmconv(acc, C1); interleave_conv(); SCHED();
    }
    ldconv(C1, ha + 448, xb + 448);
    mmconv(acc, C0); interleave_conv(); SCHED();
    u32x2 xv[8];
#pragma unroll
    for (int n = 0; n < 8; ++n) xv[n] = *(LAS u32x2*)(cr + (2044 - 256 * n - 16 * b16 - 4 * kg) * 2);
    mmconv(acc, C1);
    SCHED();
    waitv<0>();
    lds_signal(smem, 8, lane);
    STAMP(2);
#pragma unroll
    for (int e = 0; e < 8; ++e) { const int st = 24 + hw * 2 + (e >> 2); dma16(rwo, L_CONV + hw * CH_STRIDE + e * 1024, vwo, ((st ^ hx) * 32 + (e & 3) * 8) * 2048 + cb2 * 128); }
    {
      const u32 dw = (ch & 2) ? dd2.y : dd2.x;
      const float dv = (ch & 1) ? bhi(dw) : blo(dw);
      u32x2 y[8];
#pragma unroll
      for (int n = 0; n < 8; ++n)
        y[n] = u32x2{pk2(__builtin_fmaf(bhi(xv[n].y), dv, acc[n][0]), __builtin_fmaf(blo(xv[n].y), dv, acc[n][1])),
                     pk2(__builtin_fmaf(bhi(xv[n].x), dv, acc[n][2]), __builtin_fmaf(blo(xv[n].x), dv, acc[n][3]))};
      const u32 yvo = WS_YT + cb * 262144 + ((kg & 1) * 2 + (b16 >> 3)) * 16384 + (ch & 63) * 256 + (kg >> 1) * 128 + (b16 & 7) * 16;
#pragma unroll
      for (int n = 0; n < 8; n += 2) {
        auto s0 = __builtin_amdgcn_permlane16_swap(y[n].x, y[n + 1].x, false, false);
        auto s1 = __builtin_amdgcn_permlane16_swap(y[n].y, y[n + 1].y, false, false);
        st128_sc(u32x4{s0[0], s1[0], s0[1], s1[1]}, ws, yvo, n * 32768);
      }
    }
    publish(ws, WS_FY + ((cb * 16 + rb) * 4 + hw) * 8, tag, lane);
    lds_signal(smem, 9, lane);
    STAMP(3);
    await(ws, WS_FY + cb * 512, 63, tag, lane);
    STAMP(4);
    {
      u32x4 yy[4];
      const u32 yo = WS_YT + cb * 262144 + rb * 16384 + (lane >> 2) * 1024 + (hw & 1) * 128 + ((hw >> 1) * 4 + (lane & 3)) * 16;
#pragma unroll
      for (int c = 0; c < 4; ++c) yy[c] = ld128_sc(ws, yo, c * 256);
      LAS char* sc = cr + 8192;
#pragma unroll
      for (int c = 0; c < 4; ++c) *(LAS u32x4*)(sc + c * 1024 + lane * 16) = yy[c];
      asm volatile("" ::: "memory");
#pragma unroll
      for (int c = 0; c < 4; ++c) yy[c] = *(LAS u32x4*)(sc + c * 1024 + (cg * 4 + rg) * 16);
#pragma unroll
      for (int i = 0; i < 8; ++i) PIN(qq[i]);
      const rsrc_t ro = mkrsrc(p.out, 2048 * 2048 * 2);
#pragma unroll
      for (int i = 0; i < 8; ++i) {
        float f[4];
#pragma unroll
        for (int c = 0; c < 4; ++c) {
          const float yv = (i & 1) ? bhi(yy[c][i >> 1]) : blo(yy[c][i >> 1]);
          const float qv = (c & 1) ? bhi(qq[i][c >> 1]) : blo(qq[i][c >> 1]);
          f[c] = yv * qv;
        }
        __builtin_amdgcn_raw_buffer_store_b64(u32x2{pk2(f[0], f[1]), pk2(f[2], f[3])}, ro, R2 * 4096 + cb * 128 + cg * 8, i * 4096, OAUX);
      }
    }
    STAMP(5);
    STAMP_OUT();
    return;
  }

  __builtin_amdgcn_s_setprio(1);
  u32x2 tag;
  {
    const u32x2 e = __builtin_amdgcn_raw_buffer_load_b64(ws, WS_EPOCH, 0, AUX_SC1);
    const u64 t = (((u64)e.y << 32) | e.x) * 6364136223846793005ull + 1442695040888963407ull;
    tag = u32x2{(u32)t, (u32)(t >> 32)};
    if (wave == 0 && lane < 16) ((LAS u32*)(smem + L_FLAG))[lane] = lane == 12 ? tag.x : lane == 13 ? tag.y : 0u;
    waitl();
  }
  const int mh = wave & 1, hh = wave >> 1;
  const int r16 = lane & 15, g = lane >> 4, q4 = r16 >> 2, p4 = lane & 3;
  f32x4 acc[4][6];
  {
    const int xo = (64 * mh + r16) * 128 + ((g ^ (r16 >> 1)) * 16);
    const int f = ((q4 >> 1) & 1) | ((g & 1) << 1);
    const int wo = 16384 + (8 * g + q4) * 128 + (((4 * hh + (p4 >> 1)) ^ (f << 1)) * 16) + 8 * (p4 & 1);
#pragma unroll
    for (int a = 0; a < 4; ++a)
#pragma unroll
      for (int b = 0; b < 6; ++b) { const float i0 = b < 4 ? EPS : 0.f; acc[a][b] = f32x4{i0, i0, i0, i0}; PIN(acc[a][b]); }
    Frags F0, F1;
    BAR();
    STAMP(1);
    int so = SLOT;
    ldfr(F0, smem + so + xo, smem + so + wo, smem + so + (wo ^ 32), 0);
#pragma unroll 1
    for (int s = 0; s < 15; ++s) {
      ldfr(F1, smem + so + (xo ^ 64), smem + so + wo, smem + so + (wo ^ 32), 1);
      mm(acc, F0); interleave_b1(); SCHED();
      waitl(); BAR(); SCHED();
      so = (so == 2 * SLOT) ? 0 : so + SLOT;
      ldfr(F0, smem + so + xo, smem + so + wo, smem + so + (wo ^ 32), 0);
      mm(acc, F1); interleave_b1(); SCHED();
    }
    ldfr(F1, smem + so + (xo ^ 64), smem + so + wo, smem + so + (wo ^ 32), 1);
    mm(acc, F0); interleave_b1(); SCHED();
    waitl(); BAR(); SCHED();
    mm(acc, F1);
  }
  SCHED();
  STAMP(2);
  const u32 pslot = (cb * 2 + hh) * 2 + mh;
  {
    f32x4 kvp[2][2], zp[2];
    const f32x4 zero = {0, 0, 0, 0};
    const u32x4 ones = {0x3f803f80u, 0x3f803f80u, 0x3f803f80u, 0x3f803f80u};
#pragma unroll
    for (int u = 0; u < 2; ++u) {
      u32x4 pkf[2], vf[2];
#pragma unroll
      for (int t = 0; t < 2; ++t) { pkf[t] = pack_phi(acc[2 * u][2 + t], acc[2 * u + 1][2 + t]); vf[t] = pack8(acc[2 * u][4 + t], acc[2 * u + 1][4 + t]); }
#pragma unroll
      for (int dt = 0; dt < 2; ++dt) {
#pragma unroll
        for (int et = 0; et < 2; ++et) kvp[dt][et] = mfma(pkf[dt], vf[et], u ? kvp[dt][et] : zero);
        zp[dt] = mfma(pkf[dt], ones, u ? zp[dt] : zero);
      }
    }
    LAS char* mine = smem + L_X1 + wave * 2304;
    LAS char* theirs = smem + L_X1 + (wave ^ 1) * 2304;
    f32x4 keep[2];
#pragma unroll
    for (int dt = 0; dt < 2; ++dt) {
      keep[dt] = mh ? kvp[dt][1] : kvp[dt][0];
      *(LAS f32x4*)(mine + dt * 1024 + lane * 16) = mh ? kvp[dt][0] : kvp[dt][1];
      if (r16 == 0) *(LAS f32x4*)(mine + 2048 + dt * 64 + g * 16) = zp[dt];
    }
    lds_signal(smem, wave, lane);
    lds_await(smem, wave ^ 1, 1);
#pragma unroll
    for (int dt = 0; dt < 2; ++dt) {
      keep[dt] += *(LAS f32x4*)(theirs + dt * 1024 + lane * 16);
      zp[dt] += *(LAS f32x4*)(theirs + 2048 + dt * 64 + g * 16);
      st128_sc(__builtin_bit_cast(u32x4, keep[dt]), ws, lane * 16, WS_PART + pslot * 32768 + rb * 2048 + dt * 1024);
    }
    if (r16 < 2) st128_sc(__builtin_bit_cast(u32x4, r16 ? zp[1] : zp[0]), ws, r16 * 64 + g * 16, WS_Z + pslot * 2048 + rb * 128);
  }
  SCHED();
  u32x4 pq[4];
#pragma unroll
  for (int mt = 0; mt < 4; ++mt) { pq[mt] = pack_phi(acc[mt][0], acc[mt][1]); PIN(pq[mt]); }
  SCHED();
  publish(ws, WS_F1 + ((cb * 4 + wave) * 16 + rb) * 8, tag, lane);
  STAMP(3);
  await(ws, WS_F1 + (cb * 4 + wave) * 128, 15, tag, lane);
  STAMP(4);
  {
    u32x4 pr[16][2], zr[2];
#pragma unroll
    for (int dt = 0; dt < 2; ++dt) zr[dt] = ld128_sc(ws, r16 * 128 + dt * 64 + g * 16, WS_Z + pslot * 2048);
#pragma unroll
    for (int r = 0; r < 16; ++r)
#pragma unroll
      for (int dt = 0; dt < 2; ++dt) pr[r][dt] = ld128_sc(ws, lane * 16 + ((r & 1) * 2 + dt) * 1024, WS_PART + pslot * 32768 + (r >> 1) * 4096);
    SCHED();
    f32x4 zs[2], sum[2];
#pragma unroll
    for (int dt = 0; dt < 2; ++dt) {
#pragma unroll
      for (int r = 0; r < 4; ++r) zs[dt][r] = row_allsum(__uint_as_float(zr[dt][r]));
      sum[dt] = __builtin_bit_cast(f32x4, pr[0][dt]);
    }
#pragma unroll
    for (int r = 1; r < 16; ++r)
#pragma unroll
      for (int dt = 0; dt < 2; ++dt) sum[dt] += __builtin_bit_cast(f32x4, pr[r][dt]);
    const u32x4 zf = pack8(zs[0], zs[1]);
    *(LAS u32x4*)(smem + L_X2 + wave * 1024 + lane * 16) = pack8(sum[0], sum[1]);
    lds_signal(smem, 4 + wave, lane);
    lds_await(smem, 4 + (wave ^ 1), 1);
    const u32x4 kv0 = *(LAS u32x4*)(smem + L_X2 + hh * 2048 + lane * 16);
    const u32x4 kv1 = *(LAS u32x4*)(smem + L_X2 + hh * 2048 + 1024 + lane * 16);
    const f32x4 zero = {0, 0, 0, 0}, eps4 = {EPS, EPS, EPS, EPS};
#pragma unroll
    for (int mt = 0; mt < 4; ++mt) {
      const f32x4 den = mfma(zf, pq[mt], eps4);
      const f32x4 n0 = mfma(kv0, pq[mt], zero), n1 = mfma(kv1, pq[mt], zero);
      const float inv = __builtin_amdgcn_rcpf(den[0]);
      st128_sc(pack8(n0 * inv, n1 * inv), ws, lane * 16 + (mt & 1) * 1024, WS_O + rb * 262144 + (mh * 2 + (mt >> 1)) * 65536 + (cb * 2 + hh) * 2048);
    }
  }
  STAMP(5);
  publish(ws, WS_F2 + (((rb * 2 + mh) * 16 + cb) * 2 + hh) * 8, tag, lane);
  STAMP(6);
  await(ws, WS_F2 + (rb2 * 2 + (wave >> 1)) * 256, 31, tag, lane);
  STAMP(7);
  if (bid == 0 && wave == 0 && lane == 0) __builtin_amdgcn_raw_buffer_store_b64(tag, ws, WS_EPOCH, 0, AUX_SC1);
  {
    constexpr int WIN = 16;
    u32x4 of[WIN][2];
    const u32 ob = WS_O + rb2 * 262144 + wave * 65536;
    auto ldo = [&](int st) {
#pragma unroll
      for (int m = 0; m < 2; ++m) of[st % WIN][m] = ld128_sc(ws, lane * 16 + ((st & 1) * 2 + m) * 1024, ob + (((st & ~1) ^ hx) * 2048));
    };
#pragma unroll
    for (int st = 0; st < WIN; ++st) ldo(st);
    SCHED();
    lds_await(smem, 8, 4);
    const int fw = (2 * g + (q4 >> 1)) & 3;
    int wb[4];
#pragma unroll
    for (int t = 0; t < 4; ++t) wb[t] = (4 * g + q4) * 128 + ((t ^ fw) * 32) + p4 * 8;
    auto ldw = [&](u32x4 (&w)[4], int st) {
      const int base = st < 24 ? st * 4096 : L_CONV + ((st - 24) >> 1) * CH_STRIDE + (st & 1) * 4096;
#pragma unroll
      for (int t = 0; t < 4; ++t) w[t] = tr_frag(smem + base + wb[t], 2048);
    };
    f32x4 oc[2][4];
#pragma unroll
    for (int m = 0; m < 2; ++m)
#pragma unroll
      for (int t = 0; t < 4; ++t) oc[m][t] = f32x4{0, 0, 0, 0};
    u32x4 wf[2][4];
    ldw(wf[0], 0);
#pragma unroll
    for (int st = 0; st < 32; ++st) {
      if (st == 23) lds_await(smem, 9, 4);
      if (st < 31) ldw(wf[(st + 1) & 1], st + 1);
#pragma unroll
      for (int t = 0; t < 4; ++t)
#pragma unroll
        for (int m = 0; m < 2; ++m) oc[m][t] = mfma(wf[st & 1][t], of[st % WIN][m], oc[m][t]);
#pragma unroll
      for (int i = 0; i < 8; ++i) { __builtin_amdgcn_sched_group_barrier(0x8, 1, 0); __builtin_amdgcn_sched_group_barrier(0x100, 1, 0); }
      SCHED();
      if (st + WIN < 32) { ldo(st + WIN); SCHED(); }
    }
    STAMP(8);
    const rsrc_t ro = mkrsrc(p.out, 2048 * 2048 * 2);
    LAS char* sc = smem + L_X1 + (wave ^ 1) * 2304;
#pragma unroll
    for (int m = 0; m < 2; ++m) {
#pragma unroll
      for (int t = 0; t < 4; ++t) *(LAS u32x2*)(sc + r16 * 144 + t * 32 + g * 8) = u32x2{pk2(oc[m][t][0], oc[m][t][1]), pk2(oc[m][t][2], oc[m][t][3])};
      asm volatile("" ::: "memory");
#pragma unroll
      for (int e = 0; e < 2; ++e) {
        const u32x4 r = *(LAS u32x4*)(sc + (e * 8 + (lane >> 3)) * 144 + (lane & 7) * 16);
        __builtin_amdgcn_raw_buffer_store_b128(r, ro, (lane >> 3) * 4096 + (lane & 7) * 16, (rb2 * 128 + wave * 32 + m * 16 + e * 8) * 4096 + 2048 + cb2 * 128, OAUX);
      }
      asm volatile("" ::: "memory");
    }
  }
  STAMP(9);
  STAMP_OUT();
}

extern "C" void kernel_launch(void* const* d_in, const int* in_sizes, int n_in, void* d_out, int out_size, void* d_ws, size_t ws_size,
                              hipStream_t stream) {
  Params p;
  p.q = (const u16*)d_in[0]; p.k = (const u16*)d_in[1]; p.v = (const u16*)d_in[2]; p.sk = (const u16*)d_in[3];
  p.h = (const u16*)d_in[4]; p.dd = (const u16*)d_in[5]; p.x = (const u16*)d_in[6]; p.wqkv = (const u16*)d_in[7];
  p.wo = (const u16*)d_in[8]; p.out = (u16*)d_out; p.ws = (char*)d_ws;
  (void)hipFuncSetAttribute((const void*)fused_h3_linattn, hipFuncAttributeMaxDynamicSharedMemorySize, LDS_BYTES);
  fused_h3_linattn<<<256, 512, LDS_BYTES, stream>>>(p);
}
```

```cpp
#define OAUX 16
#define PCOND (wave<8)
#define P0 77
#define P1 77
#include <hip/hip_runtime.h>
#include <hip/hip_bf16.h>

typedef unsigned short u16;
typedef unsigned int u32;
typedef unsigned long long u64;
typedef float f32x2 __attribute__((ext_vector_type(2)));
typedef float f32x4 __attribute__((ext_vector_type(4)));
typedef u32 u32x2 __attribute__((ext_vector_type(2)));
typedef u32 u32x4 __attribute__((ext_vector_type(4)));
typedef short s16x4 __attribute__((ext_vector_type(4)));
typedef short s16x8 __attribute__((ext_vector_type(8)));
typedef __bf16 bf16x2 __attribute__((ext_vector_type(2)));
typedef __bf16 bf16x8 __attribute__((ext_vector_type(8)));
typedef __amdgpu_buffer_rsrc_t rsrc_t;
#define LAS __attribute__((address_space(3)))
#define GAS __attribute__((address_space(1)))
#define DI __device__ __forceinline__

#define USE_MEMSET 0
#define CODE_PF 0
#define STAMPS 0

constexpr u32 WS_EPOCH = 0;
constexpr u32 WS_F1 = 4096;
constexpr u32 WS_F2 = WS_F1 + 8192;
constexpr u32 WS_FA = WS_F2 + 8192;
constexpr u32 WS_FY = WS_FA + 8192;
constexpr u32 WS_CTRL_END = WS_FY + 8192;
constexpr u32 WS_Z = 512u << 10;
constexpr u32 WS_PART = 1u << 20;
constexpr u32 WS_O = 4u << 20;
constexpr u32 WS_KVT = 8u << 20;
constexpr u32 WS_YT = 12u << 20;
constexpr u32 WS_BYTES = 16u << 20;
constexpr u32 WS_DBG = 32u << 20;

constexpr int SLOT = 40960;
constexpr int L_X1 = 98304;
constexpr int L_X2 = L_X1 + 4 * 2304;
constexpr int CH_STRIDE = 12960;
constexpr int L_CONV = 163840 - 4 * CH_STRIDE;
constexpr int C_O = 4608, C_E = C_O + 4160;
constexpr int L_FLAG = 163840 - 64;
constexpr int LDS_BYTES = 163840;
static_assert(L_X2 + 4096 <= L_CONV, "lds map");

constexpr int AUX_SC1 = 16;
constexpr u32 SPIN_CAP = 1u << 20;

DI f32x4 mfma(u32x4 a, u32x4 b, f32x4 c) {
  return __builtin_amdgcn_mfma_f32_16x16x32_bf16(__builtin_bit_cast(bf16x8, a), __builtin_bit_cast(bf16x8, b), c, 0, 0, 0);
}
DI u32 pk2(float lo, float hi) {
  bf16x2 r = __builtin_convertvector(f32x2{lo, hi}, bf16x2);
  return __builtin_bit_cast(u32, r);
}
DI float blo(u32 x) { return __uint_as_float(x << 16); }
DI float bhi(u32 x) { return __uint_as_float(x & 0xffff0000u); }
template <int N> DI void waitv() { asm volatile("s_waitcnt vmcnt(%0)" ::"n"(N) : "memory"); }
DI void waitl() { asm volatile("s_waitcnt lgkmcnt(0)" ::: "memory"); }
#define BAR() do { asm volatile("" ::: "memory"); __builtin_amdgcn_s_barrier(); asm volatile("" ::: "memory"); } while (0)
#define SCHED() __builtin_amdgcn_sched_barrier(0)
DI rsrc_t mkrsrc(const void* p, u32 bytes) { return __builtin_amdgcn_make_buffer_rsrc((void*)p, 0, bytes, 0x20000); }
DI void dma16(rsrc_t r, u32 lds, u32 voff, u32 soff) {
  asm volatile("s_mov_b32 m0, %0\n\ts_nop 0\n\tbuffer_load_dwordx4 %1, %2, %3 offen lds" ::"s"(lds), "v"(voff), "s"(r), "s"(soff) : "memory");
}
DI void ald64(u32x2& d, rsrc_t r, u32 voff, u32 soff) {
  asm volatile("buffer_load_dwordx2 %0, %1, %2, %3 offen" : "=v"(d) : "v"(voff), "s"(r), "s"(soff) : "memory");
}
DI void ald64_sc(u32x2& d, rsrc_t r, u32 voff, u32 soff) {
  asm volatile("buffer_load_dwordx2 %0, %1, %2, %3 offen sc1" : "=v"(d) : "v"(voff), "s"(r), "s"(soff) : "memory");
}
DI void ald128(u32x4& d, rsrc_t r, u32 voff, u32 soff) {
  asm volatile("buffer_load_dwordx4 %0, %1, %2, %3 offen" : "=v"(d) : "v"(voff), "s"(r), "s"(soff) : "memory");
}
DI void ald128_sc(u32x4& d, rsrc_t r, u32 voff, u32 soff) {
  asm volatile("buffer_load_dwordx4 %0, %1, %2, %3 offen sc1" : "=v"(d) : "v"(voff), "s"(r), "s"(soff) : "memory");
}
#define PIN(x) asm volatile("" : "+v"(x))
DI u32x4 ld128_sc(rsrc_t r, u32 voff, u32 soff) { return __builtin_amdgcn_raw_buffer_load_b128(r, voff, soff, AUX_SC1); }
DI void st128_sc(u32x4 v, rsrc_t r, u32 voff, u32 soff) { __builtin_amdgcn_raw_buffer_store_b128(v, r, voff, soff, AUX_SC1); }

DI void publish(rsrc_t ws, u32 off, u32x2 tag, int lane) {
  waitv<0>();
  if (lane == 0) __builtin_amdgcn_raw_buffer_store_b64(tag, ws, off, 0, AUX_SC1);
}
DI void await(rsrc_t ws, u32 off, int mask, u32x2 tag, int lane) {
  const u32 vo = off + 8u * (lane & mask);
#pragma unroll 1
  for (u32 spins = 0; spins < SPIN_CAP; ++spins) {
    u32x2 f;
    asm volatile("buffer_load_dwordx2 %0, %1, %2, 0 offen sc1\n\ts_waitcnt vmcnt(0)" : "=&v"(f) : "v"(vo), "s"(ws) : "memory");
    if (__all(f.x == tag.x && f.y == tag.y)) break;
    __builtin_amdgcn_s_sleep(1);
  }
  asm volatile("" ::: "memory");
}
DI void lds_signal(LAS char* smem, int word, int lane) {
  waitl();
  if (lane == 0) __hip_atomic_fetch_add((LAS u32*)(smem + L_FLAG) + word, 1u, __ATOMIC_RELAXED, __HIP_MEMORY_SCOPE_WORKGROUP);
}
DI void lds_await(LAS char* smem, int word, u32 want) {
  for (u32 spins = 0; spins < (SPIN_CAP << 3); ++spins) {
    const u32 v = __hip_atomic_load((LAS u32*)(smem + L_FLAG) + word, __ATOMIC_RELAXED, __HIP_MEMORY_SCOPE_WORKGROUP);
    if (__builtin_amdgcn_readfirstlane(v) >= want) break;
  }
  asm volatile("" ::: "memory");
}

#define STAMP(i) do { if ((i)==0 || (i)==P0 || (i)==P1) { u64 t_; asm volatile("s_memrealtime %0\n\ts_waitcnt lgkmcnt(0)" : "=s"(t_) :: "memory"); if((i)==0) ts0=t_; if((i)==P0) tsa=t_; if((i)==P1) tsb=t_; } } while(0)
#define STAMP_OUT() do { if (PCOND) { const u64 until = ts0 + 10000 + 10*(tsb-tsa); for(int z=0;z<100000;++z){ u64 t_; asm volatile("s_memrealtime %0\n\ts_waitcnt lgkmcnt(0)" : "=s"(t_) :: "memory"); if(t_>=until) break; __builtin_amdgcn_s_sleep(2);} } } while(0)

struct Params {
  const u16 *q, *k, *v, *sk, *h, *dd, *x, *wqkv, *wo;
  u16* out;
  char* ws;
};

struct Frags { u32x4 x[4]; u32x4 w[6]; };
DI u32x4 tr_frag(LAS char* a, int d) {
  s16x4 lo = __builtin_amdgcn_ds_read_tr16_b64_v4i16((LAS s16x4*)a);
  s16x4 hi = __builtin_amdgcn_ds_read_tr16_b64_v4i16((LAS s16x4*)(a + d));
  return __builtin_bit_cast(u32x4, __builtin_shufflevector(lo, hi, 0, 1, 2, 3, 4, 5, 6, 7));
}
DI void ldfr(Frags& f, LAS char* xa, LAS char* wb0, LAS char* wb1, int ks) {
#pragma unroll
  for (int mt = 0; mt < 4; ++mt) f.x[mt] = *(LAS u32x4*)(xa + mt * 2048);
#pragma unroll
  for (int sg = 0; sg < 3; ++sg)
#pragma unroll
    for (int t = 0; t < 2; ++t) f.w[sg * 2 + t] = tr_frag((t ? wb1 : wb0) + sg * 8192 + ks * 4096, 512);
}
DI void mm(f32x4 (&acc)[4][6], const Frags& f) {
#pragma unroll
  for (int n = 0; n < 6; ++n)
#pragma unroll
    for (int m_ = 0; m_ < 4; ++m_) {
      const int mt = (n & 1) ? 3 - m_ : m_;
      if (n < 2) acc[mt][n] = mfma(f.w[n], f.x[mt], acc[mt][n]);
      else acc[mt][n] = mfma(f.x[mt], f.w[n], acc[mt][n]);
    }
}
DI void interleave_b1() {
#pragma unroll
  for (int i = 0; i < 16; ++i) { __builtin_amdgcn_sched_group_barrier(0x8, 1, 0); __builtin_amdgcn_sched_group_barrier(0x100, 1, 0); }
  __builtin_amdgcn_sched_group_barrier(0x8, 8, 0);
}
constexpr float EPS = 1e-6f;
DI float phi(float t) {
  const float e = __builtin_amdgcn_fmed3f(__builtin_amdgcn_exp2f(__builtin_fmaf(t, 1.44269504088896f, -EPS * 1.44269504088896f)), 0.f, 1.f);
  return e + __builtin_fmaxf(t, EPS);
}
DI u32x4 pack_phi(f32x4 a, f32x4 b) { return u32x4{pk2(phi(a[0]), phi(a[1])), pk2(phi(a[2]), phi(a[3])), pk2(phi(b[0]), phi(b[1])), pk2(phi(b[2]), phi(b[3]))}; }
DI u32x4 pack8(f32x4 a, f32x4 b) { return u32x4{pk2(a[0], a[1]), pk2(a[2], a[3]), pk2(b[0], b[1]), pk2(b[2], b[3])}; }
template <int CTRL> DI float dpp_add(float v) {
  return v + __int_as_float(__builtin_amdgcn_update_dpp(0, __float_as_int(v), CTRL, 0xf, 0xf, false));
}
DI float row_allsum(float v) { v = dpp_add<0x128>(v); v = dpp_add<0x124>(v); v = dpp_add<0x122>(v); v = dpp_add<0x121>(v); return v; }

struct CFr { u32x4 a[8]; u32x4 b[8]; };
DI void ldconv(CFr& f, LAS char* ha, LAS char* xb) {
#pragma unroll
  for (int i = 0; i < 8; ++i) {
    f.b[i] = *(LAS u32x4*)(xb + (7 - i) * 512);
    LAS u32* hp = (LAS u32*)(ha + i * 512);
    f.a[i] = u32x4{hp[0], hp[1], hp[2], hp[3]};
  }
}
DI void mmconv(f32x4 (&acc)[8], const CFr& f) {
#pragma unroll
  for (int n = 0; n < 8; ++n)
#pragma unroll
    for (int q = 0; q <= n; ++q) acc[n] = mfma(f.a[q], f.b[n - q], acc[n]);
}
DI void interleave_conv() {
#pragma unroll
  for (int i = 0; i < 24; ++i) { __builtin_amdgcn_sched_group_barrier(0x8, 1, 0); __builtin_amdgcn_sched_group_barrier(0x100, 1, 0); }
  __builtin_amdgcn_sched_group_barrier(0x8, 12, 0);
}

__global__ __launch_bounds__(512, 2) void fused_h3_linattn(Params p) {
  extern __shared__ __attribute__((aligned(16))) char smem_[];
  LAS char* smem = (LAS char*)smem_;
  const int bid = blockIdx.x, xcd = bid & 7, j = bid >> 3;
  const int rb = (xcd & 1) * 8 + (j & 7), cb = (xcd >> 1) * 4 + (j >> 3);
  const int rb2 = xcd * 2 + (j & 1), cb2 = j >> 1;
  const int hx = (cb2 & 1) * 16;
  const int wave = __builtin_amdgcn_readfirstlane(threadIdx.x >> 6), lane = threadIdx.x & 63;
  const rsrc_t ws = mkrsrc(p.ws, WS_BYTES);
  u64 ts0=0,tsa=0,tsb=0;
  STAMP(0);

  if(bid==100 && wave==0){
    u64 ta_, tb_; u32 o_=0; u32 d_;
    const rsrc_t r_=mkrsrc(p.q,2u<<20);
    asm volatile("buffer_load_dword %0, %1, %2, 0 offen\n\ts_waitcnt vmcnt(0)" : "=&v"(d_) : "v"(o_), "s"(r_) : "memory");
    asm volatile("s_memrealtime %0\n\ts_waitcnt lgkmcnt(0)" : "=s"(ta_) :: "memory");
    for(int e=1;e<=16;++e){ o_ = (o_ + 113*1024+ 256)&0x1fffff; asm volatile("buffer_load_dword %0, %1, %2, 0 offen\n\ts_waitcnt vmcnt(0)" : "=&v"(d_) : "v"(o_), "s"(r_) : "memory"); }
    asm volatile("s_memrealtime %0\n\ts_waitcnt lgkmcnt(0)" : "=s"(tb_) :: "memory");
    tsa=ta_; tsb=tb_;
  } else { for(int z=0;z<100000;++z){ u64 t_; asm volatile("s_memrealtime %0\n\ts_waitcnt lgkmcnt(0)" : "=s"(t_) :: "memory"); if(t_>=ts0+1500) break; __builtin_amdgcn_s_sleep(2);} }

  if (wave >= 4) {
    const int hw = wave - 4;
    const rsrc_t rx = mkrsrc(p.x, 2048 * 1024 * 2), rw = mkrsrc(p.wqkv, 1024 * 3072 * 2);
    const int r8 = lane >> 3, c8 = lane & 7;
    u32 vx[2], vw[2];
#pragma unroll
    for (int e = 0; e < 2; ++e) {
      vx[e] = r8 * 2048 + ((c8 ^ (4 * e + (r8 >> 1))) * 16);
      vw[e] = r8 * 6144 + ((c8 ^ ((((r8 >> 1) & 1) | (e << 1)) << 1)) * 16);
    }
    const u32 sx0 = (rb * 128 + hw * 32) * 2048, sw0 = (hw * 16) * 6144 + cb * 128;
    const int rot = 4 * ((rb + cb) & 3);
    auto issue = [&](int s_) {
      const u32 s = (s_ + rot) & 15, base = ((s_ + 1) % 3) * SLOT;
#pragma unroll
      for (int e = 0; e < 4; ++e) dma16(rx, base + (hw * 4 + e) * 1024, vx[e & 1], sx0 + e * 8 * 2048 + s * 128);
#pragma unroll
      for (int sg = 0; sg < 3; ++sg)
#pragma unroll
        for (int e = 0; e < 2; ++e) dma16(rw, base + 16384 + sg * 8192 + (hw * 2 + e) * 1024, vw[e], sw0 + e * 8 * 6144 + sg * 2048 + s * 64 * 6144);
    };
    issue(0); issue(1);
    waitv<10>(); BAR();
    issue(2);
    const u32x2 tag = *(LAS u32x2*)(smem + L_FLAG + 48);
    waitv<10>(); BAR(); issue(3);
    waitv<10>(); BAR(); issue(4);
    const int cg = lane & 15, rg = lane >> 4;
    const int R0 = rb * 128 + hw * 32 + rg * 8;
    u32x2 kk[11], vv[8]; u32x4 tw[2];
    {
      const rsrc_t rk = mkrsrc(p.k, 2048 * 1024 * 2), rv = mkrsrc(p.v, 2048 * 1024 * 2), rs = mkrsrc(p.sk, 1024 * 4 * 2);
      const u32 vo = R0 * 2048 + cb * 128 + cg * 8;
      const u32 vh = R0 == 0 ? 0x40000000u : vo - 3 * 2048;
#pragma unroll
      for (int i = 0; i < 3; ++i) ald64(kk[i], rk, vh, i * 2048);
#pragma unroll
      for (int i = 0; i < 8; ++i) ald64(kk[3 + i], rk, vo, i * 2048);
#pragma unroll
      for (int i = 0; i < 8; ++i) ald64(vv[i], rv, vo, i * 2048);
      ald128(tw[0], rs, cb * 512 + cg * 32, 0); ald128(tw[1], rs, cb * 512 + cg * 32, 16);
    }
    waitv<31>(); BAR(); issue(5);
    waitv<31>(); BAR(); issue(6);
    const u32 kvt_vo = WS_KVT + cb * 262144 + (cg >> 2) * 65536 + (255 - (R0 >> 3)) * 64 + (cg & 3) * 16;
    auto shortconv = [&](int c) {
      float kf[11], wf[4], o[8];
#pragma unroll
      for (int i = 0; i < 11; ++i) kf[i] = (c & 1) ? bhi(kk[i][c >> 1]) : blo(kk[i][c >> 1]);
#pragma unroll
      for (int t = 0; t < 4; ++t) { const u32 d = tw[c >> 1][(c & 1) * 2 + (t >> 1)]; wf[t] = (t & 1) ? bhi(d) : blo(d); }
#pragma unroll
      for (int i = 0; i < 8; ++i) {
        float a = kf[i] * wf[3];
        a = __builtin_fmaf(kf[i + 1], wf[2], a); a = __builtin_fmaf(kf[i + 2], wf[1], a); a = __builtin_fmaf(kf[i + 3], wf[0], a);
        o[i] = a * ((c & 1) ? bhi(vv[i][c >> 1]) : blo(vv[i][c >> 1]));
      }
      st128_sc(u32x4{pk2(o[7], o[6]), pk2(o[5], o[4]), pk2(o[3], o[2]), pk2(o[1], o[0])}, ws, kvt_vo, c * 16384);
    };
    waitv<10>(); BAR(); issue(7);
#pragma unroll
    for (int i = 0; i < 11; ++i) PIN(kk[i]);
#pragma unroll
    for (int i = 0; i < 8; ++i) PIN(vv[i]);
    PIN(tw[0]); PIN(tw[1]);
    shortconv(0); SCHED();
    waitv<11>(); BAR(); issue(8); shortconv(1); SCHED();
    waitv<12>(); BAR(); issue(9); shortconv(2); SCHED();
    waitv<12>(); BAR(); issue(10); shortconv(3); SCHED();
    const int ch = cb * 64 + (rb >> 2) * 16 + hw * 4 + (rb & 3);
    u32x4 hh4[4]; u32x2 dd2;
    {
      const rsrc_t rh = mkrsrc(p.h, 1024 * 2048 * 2), rd = mkrsrc(p.dd, 1024 * 2);
#pragma unroll
      for (int n = 0; n < 4; ++n) ald128(hh4[n], rh, lane * 16, ch * 4096 + n * 1024);
      ald64(dd2, rd, 0, (ch >> 2) * 8);
    }
    waitv<17>(); BAR(); issue(11);
    waitv<16>(); BAR();
    publish(ws, WS_FA + ((cb * 16 + rb) * 4 + hw) * 8, tag, lane);
    issue(12);
    waitv<10>(); BAR(); issue(13);
    waitv<10>(); BAR(); issue(14);
    waitv<10>(); BAR();
    const u32 fa_off = WS_FA + (cb * 16 + (3 - hw) * 4) * 32;
    u32x2 fl;
    ald64_sc(fl, ws, fa_off + (lane & 15) * 8, 0);
    issue(15);
    waitv<10>(); PIN(fl);
    if (!__all(fl.x == tag.x && fl.y == tag.y)) await(ws, fa_off, 15, tag, lane);
    u32x4 xs[4];
#pragma unroll
    for (int n = 0; n < 4; ++n) ald128_sc(xs[n], ws, lane * 16, WS_KVT + cb * 262144 + rb * 16384 + (hw * 4 + n) * 1024);
    BAR();
    LAS char* cr = smem + L_CONV + hw * CH_STRIDE;
#pragma unroll
    for (int n = 0; n < 4; ++n) PIN(hh4[n]);
    PIN(dd2);
    {
      const u32x4 z4 = {0, 0, 0, 0};
      if (lane < 30) *(LAS u32x4*)(cr + 4096 + lane * 16) = z4;
      if (lane < 4) *(LAS u32x4*)(cr + C_O + (lane >> 1) * (C_E - C_O) + (lane & 1) * 16) = z4;
#pragma unroll
      for (int n = 0; n < 4; ++n) *(LAS u32x4*)(cr + C_O + 32 + n * 1024 + lane * 16) = hh4[n];
      waitl();
#pragma unroll
      for (int n = 0; n < 4; ++n) {
        const u32 dp = *(LAS u32*)(cr + C_O + 28 + n * 1024 + lane * 16);
        const u32x4 d = hh4[n];
        *(LAS u32x4*)(cr + C_E + 32 + n * 1024 + lane * 16) =
            u32x4{__builtin_amdgcn_alignbit(d[0], dp, 16), __builtin_amdgcn_alignbit(d[1], d[0], 16),
                  __builtin_amdgcn_alignbit(d[2], d[1], 16), __builtin_amdgcn_alignbit(d[3], d[2], 16)};
      }
    }
    waitv<4>(); BAR();
    waitv<0>();
#pragma unroll
    for (int n = 0; n < 4; ++n) PIN(xs[n]);
#pragma unroll
    for (int n = 0; n < 4; ++n) *(LAS u32x4*)(smem + L_CONV + (lane & 3) * CH_STRIDE + ((hw * 4 + n) * 16 + (lane >> 2)) * 16) = xs[n];
    waitl(); BAR();
    STAMP(1);
    const int R2 = rb * 128 + ((hw >> 1) * 4 + rg) * 16 + (hw & 1) * 8;
    u32x2 qq[8];
    {
      const rsrc_t rq = mkrsrc(p.q, 2048 * 1024 * 2);
#pragma unroll
      for (int i = 0; i < 8; ++i) ald64(qq[i], rq, R2 * 2048 + cb * 128 + cg * 8, i * 2048);
    }
    const rsrc_t rwo = mkrsrc(p.wo, 1024 * 1024 * 2);
    const u32 vwo = r8 * 2048 + ((c8 ^ (((r8 >> 1) & 3) << 1)) * 16);
    const int b16 = lane & 15, kg = lane >> 4;
    LAS char* ha = cr + ((b16 & 1) ? C_O : C_E) + 4 + 4 * (b16 >> 1) + 16 * kg;
    LAS char* xb = cr + (254 - 224 - 2 * b16 + kg) * 16;
    f32x4 acc[8];
#pragma unroll
    for (int n = 0; n < 8; ++n) acc[n] = f32x4{0, 0, 0, 0};
    CFr C0, C1;
    ldconv(C0, ha, xb);
#pragma unroll 1
    for (int it = 0; it < 3; ++it) {
#pragma unroll
      for (int e = 0; e < 4; ++e) { const int st = it * 8 + hw; dma16(rwo, st * 4096 + e * 1024, vwo, ((st ^ hx) * 32 + e * 8) * 2048 + cb2 * 128); }
      SCHED();
      ldconv(C1, ha + it * 128 + 64, xb + it * 128 + 64);
      mmconv(acc, C0); interleave_conv(); SCHED();
#pragma unroll
      for (int e = 0; e < 4; ++e) { const int st = it * 8 + 4 + hw; dma16(rwo, st * 4096 + e * 1024, vwo, ((st ^ hx) * 32 + e * 8) * 2048 + cb2 * 128); }
      SCHED();
      ldconv(C0, ha + it * 128 + 128, xb + it * 128 + 128);
      mmconv(acc, C1); interleave_conv(); SCHED();
    }
    ldconv(C1, ha + 448, xb + 448);
    mmconv(acc, C0); interleave_conv(); SCHED();
    u32x2 xv[8];
#pragma unroll
    for (int n = 0; n < 8; ++n) xv[n] = *(LAS u32x2*)(cr + (2044 - 256 * n - 16 * b16 - 4 * kg) * 2);
    mmconv(acc, C1);
    SCHED();
    waitv<0>();
    lds_signal(smem, 8, lane);
    STAMP(2);
#pragma unroll
    for (int e = 0; e < 8; ++e) { const int st = 24 + hw * 2 + (e >> 2); dma16(rwo, L_CONV + hw * CH_STRIDE + e * 1024, vwo, ((st ^ hx) * 32 + (e & 3) * 8) * 2048 + cb2 * 128); }
    {
      const u32 dw = (ch & 2) ? dd2.y : dd2.x;
      const float dv = (ch & 1) ? bhi(dw) : blo(dw);
      u32x2 y[8];
#pragma unroll
      for (int n = 0; n < 8; ++n)
        y[n] = u32x2{pk2(__builtin_fmaf(bhi(xv[n].y), dv, acc[n][0]), __builtin_fmaf(blo(xv[n].y), dv, acc[n][1])),
                     pk2(__builtin_fmaf(bhi(xv[n].x), dv, acc[n][2]), __builtin_fmaf(blo(xv[n].x), dv, acc[n][3]))};
      const u32 yvo = WS_YT + cb * 262144 + ((kg & 1) * 2 + (b16 >> 3)) * 16384 + (ch & 63) * 256 + (kg >> 1) * 128 + (b16 & 7) * 16;
#pragma unroll
      for (int n = 0; n < 8; n += 2) {
        auto s0 = __builtin_amdgcn_permlane16_swap(y[n].x, y[n + 1].x, false, false);
        auto s1 = __builtin_amdgcn_permlane16_swap(y[n].y, y[n + 1].y, false, false);
        st128_sc(u32x4{s0[0], s1[0], s0[1], s1[1]}, ws, yvo, n * 32768);
      }
    }
    publish(ws, WS_FY + ((cb * 16 + rb) * 4 + hw) * 8, tag, lane);
    lds_signal(smem, 9, lane);
    STAMP(3);
    await(ws, WS_FY + cb * 512, 63, tag, lane);
    STAMP(4);
    {
      u32x4 yy[4];
      const u32 yo = WS_YT + cb * 262144 + rb * 16384 + (lane >> 2) * 1024 + (hw & 1) * 128 + ((hw >> 1) * 4 + (lane & 3)) * 16;
#pragma unroll
      for (int c = 0; c < 4; ++c) yy[c] = ld128_sc(ws, yo, c * 256);
      LAS char* sc = cr + 8192;
#pragma unroll
      for (int c = 0; c < 4; ++c) *(LAS u32x4*)(sc + c * 1024 + lane * 16) = yy[c];
      asm volatile("" ::: "memory");
#pragma unroll
      for (int c = 0; c < 4; ++c) yy[c] = *(LAS u32x4*)(sc + c * 1024 + (cg * 4 + rg) * 16);
#pragma unroll
      for (int i = 0; i < 8; ++i) PIN(qq[i]);
      const rsrc_t ro = mkrsrc(p.out, 2048 * 2048 * 2);
#pragma unroll
      for (int i = 0; i < 8; ++i) {
        float f[4];
#pragma unroll
        for (int c = 0; c < 4; ++c) {
          const float yv = (i & 1) ? bhi(yy[c][i >> 1]) : blo(yy[c][i >> 1]);
          const float qv = (c & 1) ? bhi(qq[i][c >> 1]) : blo(qq[i][c >> 1]);
          f[c] = yv * qv;
        }
        __builtin_amdgcn_raw_buffer_store_b64(u32x2{pk2(f[0], f[1]), pk2(f[2], f[3])}, ro, R2 * 4096 + cb * 128 + cg * 8, i * 4096, OAUX);
      }
    }
    STAMP(5);
    STAMP_OUT();
    return;
  }

  __builtin_amdgcn_s_setprio(1);
  u32x2 tag;
  {
    const u32x2 e = __builtin_amdgcn_raw_buffer_load_b64(ws, WS_EPOCH, 0, AUX_SC1);
    const u64 t = (((u64)e.y << 32) | e.x) * 6364136223846793005ull + 1442695040888963407ull;
    tag = u32x2{(u32)t, (u32)(t >> 32)};
    if (wave == 0 && lane < 16) ((LAS u32*)(smem + L_FLAG))[lane] = lane == 12 ? tag.x : lane == 13 ? tag.y : 0u;
    waitl();
  }
  const int mh = wave & 1, hh = wave >> 1;
  const int r16 = lane & 15, g = lane >> 4, q4 = r16 >> 2, p4 = lane & 3;
  f32x4 acc[4][6];
  {
    const int xo = (64 * mh + r16) * 128 + ((g ^ (r16 >> 1)) * 16);
    const int f = ((q4 >> 1) & 1) | ((g & 1) << 1);
    const int wo = 16384 + (8 * g + q4) * 128 + (((4 * hh + (p4 >> 1)) ^ (f << 1)) * 16) + 8 * (p4 & 1);
#pragma unroll
    for (int a = 0; a < 4; ++a)
#pragma unroll
      for (int b = 0; b < 6; ++b) { const float i0 = b < 4 ? EPS : 0.f; acc[a][b] = f32x4{i0, i0, i0, i0}; PIN(acc[a][b]); }
    Frags F0, F1;
    BAR();
    STAMP(1);
    int so = SLOT;
    ldfr(F0, smem + so + xo, smem + so + wo, smem + so + (wo ^ 32), 0);
#pragma unroll 1
    for (int s = 0; s < 15; ++s) {
      ldfr(F1, smem + so + (xo ^ 64), smem + so + wo, smem + so + (wo ^ 32), 1);
      mm(acc, F0); interleave_b1(); SCHED();
      waitl(); BAR(); SCHED();
      so = (so == 2 * SLOT) ? 0 : so + SLOT;
      ldfr(F0, smem + so + xo, smem + so + wo, smem + so + (wo ^ 32), 0);
      mm(acc, F1); interleave_b1(); SCHED();
    }
    ldfr(F1, smem + so + (xo ^ 64), smem + so + wo, smem + so + (wo ^ 32), 1);
    mm(acc, F0); interleave_b1(); SCHED();
    waitl(); BAR(); SCHED();
    mm(acc, F1);
  }
  SCHED();
  STAMP(2);
  const u32 pslot = (cb * 2 + hh) * 2 + mh;
  {
    f32x4 kvp[2][2], zp[2];
    const f32x4 zero = {0, 0, 0, 0};
    const u32x4 ones = {0x3f803f80u, 0x3f803f80u, 0x3f803f80u, 0x3f803f80u};
#pragma unroll
    for (int u = 0; u < 2; ++u) {
      u32x4 pkf[2], vf[2];
#pragma unroll
      for (int t = 0; t < 2; ++t) { pkf[t] = pack_phi(acc[2 * u][2 + t], acc[2 * u + 1][2 + t]); vf[t] = pack8(acc[2 * u][4 + t], acc[2 * u + 1][4 + t]); }
#pragma unroll
      for (int dt = 0; dt < 2; ++dt) {
#pragma unroll
        for (int et = 0; et < 2; ++et) kvp[dt][et] = mfma(pkf[dt], vf[et], u ? kvp[dt][et] : zero);
        zp[dt] = mfma(pkf[dt], ones, u ? zp[dt] : zero);
      }
    }
    LAS char* mine = smem + L_X1 + wave * 2304;
    LAS char* theirs = smem + L_X1 + (wave ^ 1) * 2304;
    f32x4 keep[2];
#pragma unroll
    for (int dt = 0; dt < 2; ++dt) {
      keep[dt] = mh ? kvp[dt][1] : kvp[dt][0];
      *(LAS f32x4*)(mine + dt * 1024 + lane * 16) = mh ? kvp[dt][0] : kvp[dt][1];
      if (r16 == 0) *(LAS f32x4*)(mine + 2048 + dt * 64 + g * 16) = zp[dt];
    }
    lds_signal(smem, wave, lane);
    lds_await(smem, wave ^ 1, 1);
#pragma unroll
    for (int dt = 0; dt < 2; ++dt) {
      keep[dt] += *(LAS f32x4*)(theirs + dt * 1024 + lane * 16);
      zp[dt] += *(LAS f32x4*)(theirs + 2048 + dt * 64 + g * 16);
      st128_sc(__builtin_bit_cast(u32x4, keep[dt]), ws, lane * 16, WS_PART + pslot * 32768 + rb * 2048 + dt * 1024);
    }
    if (r16 < 2) st128_sc(__builtin_bit_cast(u32x4, r16 ? zp[1] : zp[0]), ws, r16 * 64 + g * 16, WS_Z + pslot * 2048 + rb * 128);
  }
  SCHED();
  u32x4 pq[4];
#pragma unroll
  for (int mt = 0; mt < 4; ++mt) { pq[mt] = pack_phi(acc[mt][0], acc[mt][1]); PIN(pq[mt]); }
  SCHED();
  publish(ws, WS_F1 + ((cb * 4 + wave) * 16 + rb) * 8, tag, lane);
  STAMP(3);
  await(ws, WS_F1 + (cb * 4 + wave) * 128, 15, tag, lane);
  STAMP(4);
  {
    u32x4 pr[16][2], zr[2];
#pragma unroll
    for (int dt = 0; dt < 2; ++dt) zr[dt] = ld128_sc(ws, r16 * 128 + dt * 64 + g * 16, WS_Z + pslot * 2048);
#pragma unroll
    for (int r = 0; r < 16; ++r)
#pragma unroll
      for (int dt = 0; dt < 2; ++dt) pr[r][dt] = ld128_sc(ws, lane * 16 + ((r & 1) * 2 + dt) * 1024, WS_PART + pslot * 32768 + (r >> 1) * 4096);
    SCHED();
    f32x4 zs[2], sum[2];
#pragma unroll
    for (int dt = 0; dt < 2; ++dt) {
#pragma unroll
      for (int r = 0; r < 4; ++r) zs[dt][r] = row_allsum(__uint_as_float(zr[dt][r]));
      sum[dt] = __builtin_bit_cast(f32x4, pr[0][dt]);
    }
#pragma unroll
    for (int r = 1; r < 16; ++r)
#pragma unroll
      for (int dt = 0; dt < 2; ++dt) sum[dt] += __builtin_bit_cast(f32x4, pr[r][dt]);
    const u32x4 zf = pack8(zs[0], zs[1]);
    *(LAS u32x4*)(smem + L_X2 + wave * 1024 + lane * 16) = pack8(sum[0], sum[1]);
    lds_signal(smem, 4 + wave, lane);
    lds_await(smem, 4 + (wave ^ 1), 1);
    const u32x4 kv0 = *(LAS u32x4*)(smem + L_X2 + hh * 2048 + lane * 16);
    const u32x4 kv1 = *(LAS u32x4*)(smem + L_X2 + hh * 2048 + 1024 + lane * 16);
    const f32x4 zero = {0, 0, 0, 0}, eps4 = {EPS, EPS, EPS, EPS};
#pragma unroll
    for (int mt = 0; mt < 4; ++mt) {
      const f32x4 den = mfma(zf, pq[mt], eps4);
      const f32x4 n0 = mfma(kv0, pq[mt], zero), n1 = mfma(kv1, pq[mt], zero);
      const float inv = __builtin_amdgcn_rcpf(den[0]);
      st128_sc(pack8(n0 * inv, n1 * inv), ws, lane * 16 + (mt & 1) * 1024, WS_O + rb * 262144 + (mh * 2 + (mt >> 1)) * 65536 + (cb * 2 + hh) * 2048);
    }
  }
  STAMP(5);
  publish(ws, WS_F2 + (((rb * 2 + mh) * 16 + cb) * 2 + hh) * 8, tag, lane);
  STAMP(6);
  await(ws, WS_F2 + (rb2 * 2 + (wave >> 1)) * 256, 31, tag, lane);
  STAMP(7);
  if (bid == 0 && wave == 0 && lane == 0) __builtin_amdgcn_raw_buffer_store_b64(tag, ws, WS_EPOCH, 0, AUX_SC1);
  {
    constexpr int WIN = 16;
    u32x4 of[WIN][2];
    const u32 ob = WS_O + rb2 * 262144 + wave * 65536;
    auto ldo = [&](int st) {
#pragma unroll
      for (int m = 0; m < 2; ++m) of[st % WIN][m] = ld128_sc(ws, lane * 16 + ((st & 1) * 2 + m) * 1024, ob + (((st & ~1) ^ hx) * 2048));
    };
#pragma unroll
    for (int st = 0; st < WIN; ++st) ldo(st);
    SCHED();
    lds_await(smem, 8, 4);
    const int fw = (2 * g + (q4 >> 1)) & 3;
    int wb[4];
#pragma unroll
    for (int t = 0; t < 4; ++t) wb[t] = (4 * g + q4) * 128 + ((t ^ fw) * 32) + p4 * 8;
    auto ldw = [&](u32x4 (&w)[4], int st) {
      const int base = st < 24 ? st * 4096 : L_CONV + ((st - 24) >> 1) * CH_STRIDE + (st & 1) * 4096;
#pragma unroll
      for (int t = 0; t < 4; ++t) w[t] = tr_frag(smem + base + wb[t], 2048);
    };
    f32x4 oc[2][4];
#pragma unroll
    for (int m = 0; m < 2; ++m)
#pragma unroll
      for (int t = 0; t < 4; ++t) oc[m][t] = f32x4{0, 0, 0, 0};
    u32x4 wf[2][4];
    ldw(wf[0], 0);
#pragma unroll
    for (int st = 0; st < 32; ++st) {
      if (st == 23) lds_await(smem, 9, 4);
      if (st < 31) ldw(wf[(st + 1) & 1], st + 1);
#pragma unroll
      for (int t = 0; t < 4; ++t)
#pragma unroll
        for (int m = 0; m < 2; ++m) oc[m][t] = mfma(wf[st & 1][t], of[st % WIN][m], oc[m][t]);
#pragma unroll
      for (int i = 0; i < 8; ++i) { __builtin_amdgcn_sched_group_barrier(0x8, 1, 0); __builtin_amdgcn_sched_group_barrier(0x100, 1, 0); }
      SCHED();
      if (st + WIN < 32) { ldo(st + WIN); SCHED(); }
    }
    STAMP(8);
    const rsrc_t ro = mkrsrc(p.out, 2048 * 2048 * 2);
    LAS char* sc = smem + L_X1 + (wave ^ 1) * 2304;
#pragma unroll
    for (int m = 0; m < 2; ++m) {
#pragma unroll
      for (int t = 0; t < 4; ++t) *(LAS u32x2*)(sc + r16 * 144 + t * 32 + g * 8) = u32x2{pk2(oc[m][t][0], oc[m][t][1]), pk2(oc[m][t][2], oc[m][t][3])};
      asm volatile("" ::: "memory");
#pragma unroll
      for (int e = 0; e < 2; ++e) {
        const u32x4 r = *(LAS u32x4*)(sc + (e * 8 + (lane >> 3)) * 144 + (lane & 7) * 16);
        __builtin_amdgcn_raw_buffer_store_b128(r, ro, (lane >> 3) * 4096 + (lane & 7) * 16, (rb2 * 128 + wave * 32 + m * 16 + e * 8) * 4096 + 2048 + cb2 * 128, OAUX);
      }
      asm volatile("" ::: "memory");
    }
  }
  STAMP(9);
  STAMP_OUT();
}

extern "C" void kernel_launch(void* const* d_in, const int* in_sizes, int n_in, void* d_out, int out_size, void* d_ws, size_t ws_size,
                              hipStream_t stream) {
  Params p;
  p.q = (const u16*)d_in[0]; p.k = (const u16*)d_in[1]; p.v = (const u16*)d_in[2]; p.sk = (const u16*)d_in[3];
  p.h = (const u16*)d_in[4]; p.dd = (const u16*)d_in[5]; p.x = (const u16*)d_in[6]; p.wqkv = (const u16*)d_in[7];
  p.wo = (const u16*)d_in[8]; p.out = (u16*)d_out; p.ws = (char*)d_ws;
  (void)hipFuncSetAttribute((const void*)fused_h3_linattn, hipFuncAttributeMaxDynamicSharedMemorySize, LDS_BYTES);
  fused_h3_linattn<<<256, 512, LDS_BYTES, stream>>>(p);
}
```

```cpp
#include <hip/hip_runtime.h>
#include <hip/hip_bf16.h>

typedef unsigned short u16;
typedef unsigned int u32;
typedef unsigned long long u64;
typedef float f32x2 __attribute__((ext_vector_type(2)));
typedef float f32x4 __attribute__((ext_vector_type(4)));
typedef u32 u32x2 __attribute__((ext_vector_type(2)));
typedef u32 u32x4 __attribute__((ext_vector_type(4)));
typedef short s16x4 __attribute__((ext_vector_type(4)));
typedef __bf16 bf16x2 __attribute__((ext_vector_type(2)));
typedef __bf16 bf16x8 __attribute__((ext_vector_type(8)));
typedef __amdgpu_buffer_rsrc_t rsrc_t;
#define LAS __attribute__((address_space(3)))
#define DI __device__ __forceinline__
#if defined(__HIP_DEVICE_COMPILE__)
#define NOPK __attribute__((target("no-packed-fp32-ops")))
#else
#define NOPK
#endif

#define CODE_PF 27136
#define ROT 2
#define QPOS 0
#define S0 0
#define NSG 4
#define WIN 5
#define STAMPS 0

constexpr u32 WS_EPOCH = 0;
constexpr u32 WS_F1 = 65536;
constexpr u32 WS_F2 = WS_F1 + 65536;
constexpr u32 WS_FA = WS_F2 + 65536;
constexpr u32 WS_FY = WS_FA + 131072;
constexpr u32 WS_Z = 512u << 10;
constexpr u32 WS_PART = 1u << 20;
constexpr u32 WS_O = 2u << 20;
constexpr u32 WS_KVT = 6u << 20;
constexpr u32 WS_YT = 10u << 20;
constexpr u32 WS_BYTES = 14u << 20;
constexpr u32 WS_DBG = 32u << 20;

constexpr u32 SLOT = 40960;
constexpr u32 L_X1 = 98304;
constexpr u32 L_X2 = L_X1 + 4 * 2304;
constexpr u32 CX_STRIDE = 4640, CH_STRIDE = 8320, C_E = 4160;
constexpr u32 L_FLAG = 163840 - 128;
constexpr u32 L_CH = L_FLAG - 4 * CH_STRIDE;
constexpr u32 L_CX = L_CH - 4 * CX_STRIDE;
constexpr u32 LDS_BYTES = 163840;
static_assert(L_X2 + 4096 + 256 <= L_CX && L_CH >= 3 * SLOT, "lds map");
enum { W_TAG = 0, W_WO1 = 2, W_WO2 = 3, W_X1 = 4, W_X2 = 8, W_CNT1 = 12, W_CNT2, W_CNTY, W_GOT1, W_GOT2, W_STAGED, W_PST };

constexpr int AUX_SC1 = 16;
constexpr u32 SPIN_CAP = 1u << 16;

DI float f_(u32 x) { return __builtin_bit_cast(float, x); }
DI u32 u_(float x) { return __builtin_bit_cast(u32, x); }
DI f32x4 mfma(u32x4 a, u32x4 b, f32x4 c) {
  return __builtin_amdgcn_mfma_f32_16x16x32_bf16(__builtin_bit_cast(bf16x8, a), __builtin_bit_cast(bf16x8, b), c, 0, 0, 0);
}
DI u32 pk2(float lo, float hi) { return __builtin_bit_cast(u32, __builtin_convertvector(f32x2{lo, hi}, bf16x2)); }
DI float blo(u32 x) { return f_(x << 16); }
DI float bhi(u32 x) { return f_(x & 0xffff0000u); }
template <int N> DI void waitv() { asm volatile("s_waitcnt vmcnt(%0)" ::"n"(N) : "memory"); }
DI void waitl() { asm volatile("" ::: "memory"); __builtin_amdgcn_s_waitcnt(0xC07F); asm volatile("" ::: "memory"); }
#define SCHED() __builtin_amdgcn_sched_barrier(0)
#define FENCE() asm volatile("" ::: "memory")
#define BAR() do { FENCE(); SCHED(); __builtin_amdgcn_s_barrier(); SCHED(); FENCE(); } while (0)
#define PIN(x) asm volatile("" : "+v"(x))
template <class T> DI LAS T* L(u32 a) { return (LAS T*)(size_t)a; }
DI u32 opaque(u32 a) { PIN(a); return a; }
DI rsrc_t mkrsrc(const void* p, u32 bytes) { return __builtin_amdgcn_make_buffer_rsrc((void*)p, 0, bytes, 0x20000); }
template <int LI, int SI> DI void dma(rsrc_t r, u32 lds, u32 voff, u32 soff) {
  u32 t;
  asm volatile("s_add_u32 m0, %1, %2\n\ts_add_u32 %0, %5, %6\n\tbuffer_load_dwordx4 %3, %4, %0 offen lds"
               : "=&s"(t) : "s"(lds), "i"(LI), "v"(voff), "s"(r), "s"(soff), "i"(SI) : "memory", "scc");
}
DI u32x4 ld128(rsrc_t r, u32 voff, u32 soff) { return __builtin_amdgcn_raw_buffer_load_b128(r, voff, soff, 0); }
DI u32x4 ld128_sc(rsrc_t r, u32 voff, u32 soff) { return __builtin_amdgcn_raw_buffer_load_b128(r, voff, soff, AUX_SC1); }
DI void st128_sc(u32x4 v, rsrc_t r, u32 voff) { __builtin_amdgcn_raw_buffer_store_b128(v, r, voff, 0, AUX_SC1); }

DI u32 lds_get(int word) { return __builtin_amdgcn_readfirstlane(__hip_atomic_load(L<u32>(L_FLAG + 4 * word), __ATOMIC_RELAXED, __HIP_MEMORY_SCOPE_WORKGROUP)); }
DI void lds_set(int word, u32 v) {
  asm volatile("s_mov_b64 exec, 1\n\tds_write_b32 %0, %1\n\ts_mov_b64 exec, -1" :: "v"(L_FLAG + 4 * word), "v"(v) : "memory");
}
DI void lds_inc(int word) {
  asm volatile("s_mov_b64 exec, 1\n\tds_add_u32 %0, %1\n\ts_mov_b64 exec, -1" :: "v"(L_FLAG + 4 * word), "v"(1u) : "memory");
}
template <int SL> DI void lds_await(int word, u32 want) {
#pragma unroll 1
  for (u32 spins = 0; spins < (SPIN_CAP << 3); ++spins) {
    if (lds_get(word) >= want) break;
    if (SL) __builtin_amdgcn_s_sleep(SL);
  }
  FENCE();
}
DI bool drain_last(int word, u32 n) {
  u32 old;
  asm volatile("s_waitcnt vmcnt(0)\n\ts_mov_b64 exec, 1\n\tds_add_rtn_u32 %0, %1, %2\n\ts_mov_b64 exec, -1\n\ts_waitcnt lgkmcnt(0)"
               : "=&v"(old) : "v"(L_FLAG + 4 * word), "v"(1u) : "memory");
  return __builtin_amdgcn_readfirstlane(old) == n - 1;
}
DI void post(rsrc_t ws, u32 voff, u32x2 tag, bool on) { __builtin_amdgcn_raw_buffer_store_b64(tag, ws, on ? voff : 0x40000000u, 0, AUX_SC1); }
template <int SL> DI void await(rsrc_t ws, u32 voff, u32x2 tag) {
#pragma unroll 1
  for (u32 spins = 0; spins < SPIN_CAP; ++spins) {
    u32x2 f;
    asm volatile("buffer_load_dwordx2 %0, %1, %2, 0 offen sc1\n\ts_waitcnt vmcnt(0)" : "=&v"(f) : "v"(voff), "s"(ws) : "memory");
    if (__builtin_amdgcn_ballot_w64(f.x != tag.x || f.y != tag.y) == 0) break;
    if (SL) __builtin_amdgcn_s_sleep(SL);
  }
  FENCE();
}

#define STAMPINS "s_memrealtime"
#define STAMP(i)
#define STAMP_OUT()

struct Params {
  const u16 *x, *wqkv;
  char* ws;
  const u16 *wo, *q, *k, *v, *sk, *h, *dd;
  u16* out;
};

struct Frags { u32x4 x[4]; u32x4 w[6]; };
DI u32x4 tr_frag(u32 a, u32 d) {
  s16x4 lo = __builtin_amdgcn_ds_read_tr16_b64_v4i16(L<s16x4>(a));
  s16x4 hi = __builtin_amdgcn_ds_read_tr16_b64_v4i16(L<s16x4>(a + d));
  return __builtin_bit_cast(u32x4, __builtin_shufflevector(lo, hi, 0, 1, 2, 3, 4, 5, 6, 7));
}
DI void ldfr(Frags& f, const u32 (&b)[4], u32 off, int ks) {
#pragma unroll
  for (int mt = 0; mt < 4; ++mt) f.x[mt] = *L<u32x4>(b[ks] + off + mt * 2048);
#pragma unroll
  for (int s_ = 1; s_ < 4; ++s_)
#pragma unroll
    for (int t = 0; t < 2; ++t) { const int sg = s_ % 3; f.w[sg * 2 + t] = tr_frag(b[2 + t] + off + sg * 8192 + ks * 4096, 512); }
}
DI void mm(f32x4 (&acc)[4][6], const Frags& f) {
#pragma unroll
  for (int i = 0; i < 24; ++i) {
    const int n_ = i >> 2, n = n_ < 4 ? n_ + 2 : n_ - 4, mt = (n_ & 1) ? 3 - (i & 3) : (i & 3);
    if (n < 2) acc[mt][n] = mfma(f.w[n], f.x[mt], acc[mt][n]);
    else acc[mt][n] = mfma(f.x[mt], f.w[n], acc[mt][n]);
  }
}
template <int NR, int NM> DI void interleave() {
#pragma unroll
  for (int i = 0; i < NR; ++i) { __builtin_amdgcn_sched_group_barrier(0x8, 1, 0); __builtin_amdgcn_sched_group_barrier(0x100, 1, 0); }
  if (NM > NR) __builtin_amdgcn_sched_group_barrier(0x8, NM - NR, 0);
}
constexpr float EPS = 1e-6f;
DI float phi(float t) {
  const float e = __builtin_amdgcn_fmed3f(__builtin_amdgcn_exp2f(__builtin_fmaf(t, 1.44269504088896f, -EPS * 1.44269504088896f)), 0.f, 1.f);
  return e + __builtin_bit_cast(float, __builtin_elementwise_max(__builtin_bit_cast(int, t), __builtin_bit_cast(int, EPS)));
}
DI u32x4 pack_phi(f32x4 a, f32x4 b) { return u32x4{pk2(phi(a[0]), phi(a[1])), pk2(phi(a[2]), phi(a[3])), pk2(phi(b[0]), phi(b[1])), pk2(phi(b[2]), phi(b[3]))}; }
DI u32x4 pack8(f32x4 a, f32x4 b) { return u32x4{pk2(a[0], a[1]), pk2(a[2], a[3]), pk2(b[0], b[1]), pk2(b[2], b[3])}; }
DI float sum_8_16_32(float v) {
  v += f_(__builtin_amdgcn_update_dpp(0, u_(v), 0x128, 0xf, 0xf, false));
  auto a = __builtin_amdgcn_permlane16_swap(u_(v), u_(v), false, false);
  v = f_(a[0]) + f_(a[1]);
  auto b = __builtin_amdgcn_permlane32_swap(u_(v), u_(v), false, false);
  return f_(b[0]) + f_(b[1]);
}

DI void mfma_ip(f32x4& c, u32x4 a, u32x4 b) { asm volatile("v_mfma_f32_16x16x32_bf16 %0, %1, %2, %0" : "+v"(c) : "v"(a), "v"(b)); }
struct CFr { u32x2 a[8][2]; u32x4 b[8]; };
DI void ldconv1(CFr& f, const u32 (&hb)[4], u32 xb, u32 off, int k) {
  const int i = k / 3, e = k % 3;
  if (e == 0) f.b[i] = *L<u32x4>(xb + off + (7 - i) * 512);
  else { LAS u32* hp = L<u32>(hb[i >> 1] + off + (i & 1) * 512 + (e - 1) * 8); f.a[i][e - 1] = u32x2{hp[0], hp[1]}; }
}
DI void mmconv1(f32x4 (&acc)[8], const CFr& f, int k) {
  const int n = (k >= 1) + (k >= 3) + (k >= 6) + (k >= 10) + (k >= 15) + (k >= 21) + (k >= 28);
  k -= n * (n + 1) / 2;
  mfma_ip(acc[n], u32x4{f.a[k][0].x, f.a[k][0].y, f.a[k][1].x, f.a[k][1].y}, f.b[n - k]);
}

__global__ NOPK __launch_bounds__(512, 2) void fused_h3_linattn(Params p) {
  const int bid = __builtin_amdgcn_workgroup_id_x(), xcd = bid & 7, j = bid >> 3;
  const int rb = (xcd & 1) * 8 + (j & 7), cb = (xcd >> 1) * 4 + (j >> 3);
  const int cq = (cb & 12) | ((cb + 1) & 3);
  const int rb2 = (xcd >> 1) * 4 + (j & 3), cb2 = (xcd & 1) * 8 + (j >> 2);
  const int hx = (((j >> 2) + 2 * (j & 3)) & 7) * 4;
  const int wave = __builtin_amdgcn_readfirstlane(__builtin_amdgcn_workitem_id_x() >> 6), lane = __builtin_amdgcn_workitem_id_x() & 63;
  const int r8 = lane >> 3, c8 = lane & 7;
  STAMP(0);
  const rsrc_t ws = mkrsrc(p.ws, WS_BYTES), rx = mkrsrc(p.x, 2048 * 1024 * 2), rw = mkrsrc(p.wqkv, 1024 * 3072 * 2);
  const int rot = ROT == 0 ? 0 : ROT == 1 ? rb + cb : ROT == 2 ? rb - cb : 8 * ((rb + cb) & 1);
  const u32 qd = (cq - cb) * 128;
  u32 vx[2], vw[2];
#pragma unroll
  for (int e = 0; e < 2; ++e) {
    vx[e] = r8 * 2048 + ((c8 ^ (4 * e + (r8 >> 1))) * 16);
    vw[e] = r8 * 6144 + ((c8 ^ ((((r8 >> 1) & 1) | (e << 1)) << 1)) * 16);
  }
  {
    const u32 s = rot & 15, sx = (rb * 128 + wave * 16) * 2048 + s * 128, sw = (s * 64 + wave * 8) * 6144 + cb * 128;
    const u32 vwp = vw[0] ^ ((wave & 1) * 64);
    dma<SLOT, 0>(rx, wave * 2048, vx[0], sx);
    dma<SLOT + 16384 + 8192, 2048>(rw, wave * 1024, vwp, sw);
    dma<SLOT + 16384, 0>(rw, wave * 1024, vwp, sw + qd);
    dma<SLOT + 16384 + 16384, 4096>(rw, wave * 1024, vwp, sw);
    dma<SLOT + 1024, 16384>(rx, wave * 2048, vx[1], sx);
  }
  STAMP(24);

  if (wave >= 4) {
    const int hw = wave - 4, aw = hw & 1, ca = (cb + aw) & 15;
    const rsrc_t rwo = mkrsrc(p.wo, 1024 * 1024 * 2);
    const u32 vwo = r8 * 2048 + ((c8 ^ (((r8 >> 1) & 3) << 1)) * 16);
    const int R2 = rb * 128 + r8 * 16 + aw * 8;
    const rsrc_t rq = mkrsrc(p.q, 2048 * 1024 * 2);
    u32x4 qq[8];
    if (hw < 2) {
      const u32 sx0 = (rb * 128 + hw * 64) * 2048, sw0 = (hw * 32) * 6144 + cb * 128;
      auto issue = [&](int s_, u32 base, int half) __attribute__((always_inline)) {
        const u32 s = (s_ + rot) & 15, sx = sx0 + s * 128 + half * 65536, sw = sw0 + s * (64 * 6144) + half * 98304, sq = sw + qd;
        const u32 lx = base + hw * 8192 + half * 4096, lw = base + 16384 + hw * 4096 + half * 2048;
        dma<0, 0>(rx, lx, vx[0], sx); dma<1024, 16384>(rx, lx, vx[1], sx);
        dma<8192, 2048>(rw, lw, vw[0], sw); dma<9216, 2048 + 49152>(rw, lw, vw[1], sw);
        dma<0, 0>(rw, lw, vw[0], sq); dma<1024, 49152>(rw, lw, vw[1], sq);
        dma<16384, 4096>(rw, lw, vw[0], sw); dma<17408, 4096 + 49152>(rw, lw, vw[1], sw);
        dma<2048, 32768>(rx, lx, vx[0], sx); dma<3072, 49152>(rx, lx, vx[1], sx);
      };
      issue(1, 2 * SLOT, 0);
      waitv<10>(); BAR();
      issue(1, 2 * SLOT, 1);
      u32 base = 0;
#pragma unroll 1
      for (int s = 2; s < 16; ++s) {
        issue(s, base, 0); issue(s, base, 1);
        base = base == 2 * SLOT ? 0 : base + SLOT;
        waitv<20>(); BAR();
      }
      if (QPOS == 2) {
#pragma unroll
        for (int i = 0; i < 8; ++i) asm volatile("" : "=v"(qq[i]));
      }
    } else {
      waitv<0>(); BAR();
      const int R0 = rb * 128 + aw * 64 + r8 * 8;
      const rsrc_t rk = mkrsrc(p.k, 2048 * 1024 * 2), rv = mkrsrc(p.v, 2048 * 1024 * 2), rs = mkrsrc(p.sk, 1024 * 4 * 2);
      const u32 vo = R0 * 2048 + ca * 128 + c8 * 16;
      const u32 vh = R0 == 0 ? 0x40000000u : vo - 3 * 2048;
      const u32 kvt_vo = WS_KVT + ca * 262144 + (c8 >> 2) * 131072 + (255 - (R0 >> 3)) * 64 + (c8 & 3) * 16;
      u32x4 kk[11], vv[8], tw[4];
#define AB(i) do { BAR(); STAMP(8 + (i)); } while (0)
      const int sg = S0 + (NSG > 1 ? (j + (j >> 3) + xcd) % NSG : 0);
#pragma unroll 1
      for (int i = 0; i <= sg; ++i) AB(i);
#pragma unroll
      for (int i = 0; i < 8; ++i) kk[3 + i] = ld128(rk, vo, i * 2048);
#pragma unroll
      for (int i = 0; i < 3; ++i) kk[i] = ld128(rk, vh, i * 2048);
#pragma unroll
      for (int i = 0; i < 8; ++i) vv[i] = ld128(rv, vo, i * 2048);
#pragma unroll
      for (int i = 0; i < 4; ++i) tw[i] = ld128(rs, ca * 512 + c8 * 64 + i * 16, 0);
      if (QPOS == 2) {
#pragma unroll
        for (int i = 0; i < 8; ++i) qq[i] = ld128(rq, R2 * 2048 + ca * 128 + c8 * 16, i * 2048);
      }
      AB(sg + 1);
#pragma unroll
      for (int c = 0; c < 8; ++c) {
        AB(sg + 2 + c);
        float kf[11], wf[4], o[8];
#pragma unroll
        for (int i = 0; i < 11; ++i) { u32 d = kk[i][c >> 1]; PIN(d); kf[i] = (c & 1) ? bhi(d) : blo(d); }
#pragma unroll
        for (int t = 0; t < 4; ++t) { u32 d = tw[c >> 1][(c & 1) * 2 + (t >> 1)]; PIN(d); wf[t] = (t & 1) ? bhi(d) : blo(d); }
#pragma unroll
        for (int i = 0; i < 8; ++i) {
          float a = kf[i] * wf[3];
          a = __builtin_fmaf(kf[i + 1], wf[2], a); a = __builtin_fmaf(kf[i + 2], wf[1], a); a = __builtin_fmaf(kf[i + 3], wf[0], a);
          u32 d = vv[i][c >> 1]; PIN(d);
          o[i] = a * ((c & 1) ? bhi(d) : blo(d));
        }
        st128_sc(u32x4{pk2(o[7], o[6]), pk2(o[5], o[4]), pk2(o[3], o[2]), pk2(o[1], o[0])}, ws, kvt_vo + c * 16384);
      }
      AB(sg + 10);
      waitv<0>();
      post(ws, WS_FA + ((ca * 16 + lane) * 2 + (rb < 8)) * 256 + ((rb & 7) * 2 + aw) * 8, *L<u32x2>(L_FLAG + 4 * W_TAG), lane < 16);
#pragma unroll 1
      for (int i = sg + 11; i <= 13; ++i) AB(i);
    }
    const int chl = (rb >> 3) * 32 + hw * 8 + (rb & 7);
    const u32 cx = L_CX + hw * CX_STRIDE, chr = L_CH + hw * CH_STRIDE;
    u32x4 hh4[4], xs[4];
    {
      const rsrc_t rh = mkrsrc(p.h, 1024 * 2048 * 2);
#pragma unroll
      for (int n = 0; n < 4; ++n) hh4[n] = ld128(rh, lane * 16 + n * 1024, (cb * 64 + chl) * 4096);
    }
    if (hw < 2) waitv<4>();
    BAR();
    STAMP(22);
    const u32x2 tag = *L<u32x2>(L_FLAG + 4 * W_TAG);
    const float dv = blo(p.dd[cb * 64 + chl]);
    if (lane < 34) *L<u32x4>(lane < 30 ? cx + 4096 + lane * 16 : chr + (lane >> 1 & 1) * C_E + (lane & 1) * 16) = u32x4{0, 0, 0, 0};
    {
      const u32 hr = opaque(chr + lane * 16);
#pragma unroll
      for (int n = 0; n < 4; ++n) *L<u32x4>(hr + 32 + n * 1024) = hh4[n];
      FENCE();
      u32 dp[4];
#pragma unroll
      for (int n = 0; n < 4; ++n) dp[n] = *L<u32>(hr + 28 + n * 1024);
#pragma unroll
      for (int n = 0; n < 4; ++n) {
        const u32x4 d = hh4[n];
        *L<u32x4>(hr + C_E + 32 + n * 1024) =
            u32x4{__builtin_amdgcn_alignbit(d[0], dp[n], 16), __builtin_amdgcn_alignbit(d[1], d[0], 16),
                  __builtin_amdgcn_alignbit(d[2], d[1], 16), __builtin_amdgcn_alignbit(d[3], d[2], 16)};
      }
      await<4>(ws, WS_FA + ((cb * 16 + rb) * 2 + (hw >> 1)) * 256 + (lane & 15) * 8, tag);
      STAMP(23);
#pragma unroll
      for (int n = 0; n < 4; ++n) xs[n] = ld128_sc(ws, lane * 16 + n * 1024, WS_KVT + cb * 262144 + rb * 16384 + hw * 4096);
      if (QPOS == 0) {
        if (hw >= 2) {
#pragma unroll
          for (int i = 0; i < 8; ++i) qq[i] = ld128(rq, R2 * 2048 + ca * 128 + c8 * 16, i * 2048);
        } else {
#pragma unroll
          for (int i = 0; i < 8; ++i) asm volatile("" : "=v"(qq[i]));
        }
      }
      const u32 xw = opaque(L_CX + (lane & 3) * CX_STRIDE + (hw * 64 + (lane >> 2)) * 16);
#pragma unroll
      for (int n = 0; n < 4; ++n) *L<u32x4>(xw + n * 256) = xs[n];
      lds_inc(W_STAGED);
    }
    const int b16 = lane & 15, kg = lane >> 4;
    u32 hb[4], xb = opaque(cx + (254 - 224 - 2 * b16 + kg) * 16);
#pragma unroll
    for (int i = 0; i < 4; ++i) hb[i] = opaque(chr + ((b16 & 1) ? 0 : C_E) + 4 + 4 * (b16 >> 1) + 16 * kg + i * 1024);
    f32x4 acc[8];
#pragma unroll
    for (int n = 0; n < 8; ++n) { acc[n] = f32x4{0, 0, 0, 0}; PIN(acc[n]); }
    lds_await<1>(W_STAGED, 4);
    STAMP(6);
    CFr C0, C1;
#pragma unroll
    for (int k = 0; k < 24; ++k) ldconv1(C0, hb, xb, 0, k);
    lds_await<1>(W_PST, 4);
    waitl();
    STAMP(1);
#define RESIDUE(CUR, NXT, OFF, ST) do { \
      const u32 so_ = ((ST) ^ hx) * 65536 + cb2 * 128, ld_ = (ST) * 4096; \
      _Pragma("unroll") for (int k = 0; k < 36; ++k) { \
        mmconv1(acc, CUR, k); \
        if (k % 9 == 8) { if (k == 8) dma<0, 0>(rwo, ld_, vwo, so_); else if (k == 17) dma<1024, 16384>(rwo, ld_, vwo, so_); \
                          else if (k == 26) dma<2048, 32768>(rwo, ld_, vwo, so_); else dma<3072, 49152>(rwo, ld_, vwo, so_); } \
        else if (k - k / 9 < 24) ldconv1(NXT, hb, xb, OFF, k - k / 9); \
        SCHED(); \
      } \
      waitl(); SCHED(); \
    } while (0)
#pragma unroll 1
    for (int it = 0; it < 3; ++it) {
      RESIDUE(C0, C1, 64, it * 8 + hw);
      RESIDUE(C1, C0, 128, it * 8 + 4 + hw);
      xb = opaque(xb + 128);
#pragma unroll
      for (int i = 0; i < 4; ++i) hb[i] = opaque(hb[i] + 128);
    }
    u32x2 xv[8];
    const u32 xe = opaque(cx + (252 - 16 * b16 - 4 * kg) * 2);
#pragma unroll
    for (int k = 0; k < 36; ++k) {
      mmconv1(acc, C0, k);
      if (k < 24) ldconv1(C1, hb, xb, 64, k); else if (k < 32) xv[k - 24] = *L<u32x2>(xe + (31 - k) * 512);
      SCHED();
    }
    waitv<0>(); waitl();
    lds_inc(W_WO1);
    {
      const u32 so = ((24 + hw * 2) ^ hx) * 65536 + cb2 * 128;
#define WO2(k, e) if (k == 4 * e + 3) dma<e * 1024, e * 16384>(rwo, chr, vwo, so);
#pragma unroll
      for (int k = 0; k < 36; ++k) {
        mmconv1(acc, C1, k);
        WO2(k, 0) WO2(k, 1) WO2(k, 2) WO2(k, 3) WO2(k, 4) WO2(k, 5) WO2(k, 6) WO2(k, 7)
        SCHED();
      }
    }
    asm volatile("s_nop 7" : "+v"(acc[7]));
    SCHED();
    STAMP(2);
    {
      u32x2 y[8];
#pragma unroll
      for (int n = 0; n < 8; ++n)
        y[n] = u32x2{pk2(__builtin_fmaf(bhi(xv[n].y), dv, acc[n][0]), __builtin_fmaf(blo(xv[n].y), dv, acc[n][1])),
                     pk2(__builtin_fmaf(bhi(xv[n].x), dv, acc[n][2]), __builtin_fmaf(blo(xv[n].x), dv, acc[n][3]))};
      const u32 yvo = WS_YT + cb * 262144 + ((kg & 1) * 2 + (b16 >> 3)) * 16384 + chl * 256 + (kg >> 1) * 128 + (b16 & 7) * 16;
#pragma unroll
      for (int n = 0; n < 8; n += 2) {
        auto s0 = __builtin_amdgcn_permlane16_swap(y[n].x, y[n + 1].x, false, false);
        auto s1 = __builtin_amdgcn_permlane16_swap(y[n].y, y[n + 1].y, false, false);
        st128_sc(u32x4{s0[0], s1[0], s0[1], s1[1]}, ws, yvo + n * 32768);
      }
    }
    if (drain_last(W_CNTY, 4)) {
      lds_set(W_WO2, 1);
      post(ws, WS_FY + (cb * 32 + lane) * 256 + rb * 8, tag, lane < 32);
    }
    STAMP(3);
    if (hw >= 2) {
      if (QPOS == 1) {
#pragma unroll
        for (int i = 0; i < 8; ++i) qq[i] = ld128(rq, R2 * 2048 + ca * 128 + c8 * 16, i * 2048);
      } else __builtin_amdgcn_s_sleep(16);
      await<8>(ws, WS_FY + ((ca * 16 + rb) * 2 + aw) * 256 + (lane & 15) * 8, tag);
      STAMP(4);
      u32x4 yy[8];
      const u32 yo = WS_YT + ca * 262144 + rb * 16384 + r8 * 2048 + aw * 128 + c8 * 16;
#pragma unroll
      for (int c = 0; c < 8; ++c) yy[c] = ld128_sc(ws, yo + c * 256, 0);
      const u32 tw_ = opaque(cx + r8 * 144 + c8 * 16), tr_ = opaque(cx + c8 * 144 + r8 * 16);
#pragma unroll
      for (int hf = 0; hf < 2; ++hf) {
#pragma unroll
        for (int c = 0; c < 4; ++c) *L<u32x4>(tw_ + c * 1152) = yy[hf * 4 + c];
        FENCE();
#pragma unroll
        for (int c = 0; c < 4; ++c) yy[hf * 4 + c] = *L<u32x4>(tr_ + c * 1152);
        FENCE();
      }
      const rsrc_t ro = mkrsrc(p.out, 2048 * 2048 * 2);
#pragma unroll
      for (int i = 0; i < 8; ++i) {
        u32x4 o;
#pragma unroll
        for (int d = 0; d < 4; ++d) {
          const u32 y0 = yy[2 * d][i >> 1], y1 = yy[2 * d + 1][i >> 1];
          o[d] = pk2(((i & 1) ? bhi(y0) : blo(y0)) * blo(qq[i][d]), ((i & 1) ? bhi(y1) : blo(y1)) * bhi(qq[i][d]));
        }
        st128_sc(o, ro, (R2 + i) * 4096 + ca * 128 + c8 * 16);
      }
    } else { STAMP(4); }
    STAMP(5);
    STAMP_OUT();
    return;
  }

  __builtin_amdgcn_s_setprio(1);
  if (wave == 0) {
    const u32x2 e = __builtin_amdgcn_raw_buffer_load_b64(ws, WS_EPOCH, 0, AUX_SC1);
    const u64 t = (((u64)e.y << 32) | e.x) * 6364136223846793005ull + 1442695040888963407ull;
    if (lane < 32) *L<u32>(L_FLAG + 4 * lane) = lane == W_TAG ? (u32)t : lane == W_TAG + 1 ? (u32)(t >> 32) : 0u;
    waitl();
  }
  if (wave == 1) {
    u64 pc;
    asm volatile("s_getpc_b64 %0" : "=s"(pc));
    u32 pf;
    asm volatile("buffer_load_dword %0, %1, %2, 0 offen\n\ts_waitcnt vmcnt(0)" : "=&v"(pf) : "v"(((j & 3) * 64 + lane) * 128), "s"(mkrsrc((const void*)(pc & ~127ull), CODE_PF)) : "memory");
  }
  const int mh = wave & 1, hh = wave >> 1;
  const int r16 = lane & 15, g = lane >> 4, q4 = r16 >> 2, p4 = lane & 3;
  f32x4 acc[4][6];
  {
    const u32 xo = (64 * mh + r16) * 128 + ((g ^ (r16 >> 1)) * 16);
    const int f = ((q4 >> 1) & 1) | ((g & 1) << 1);
    const u32 wo = 16384 + (8 * g + q4) * 128 + (((4 * hh + (p4 >> 1)) ^ (f << 1)) * 16) + 8 * (p4 & 1);
    const u32 bA[4] = {opaque(xo), opaque(xo ^ 64), opaque(wo), opaque(wo ^ 32)};
    const u32 bB[4] = {opaque(xo + 2 * SLOT), opaque((xo ^ 64) + 2 * SLOT), opaque(wo + 2 * SLOT), opaque((wo ^ 32) + 2 * SLOT)};
#pragma unroll
    for (int a = 0; a < 4; ++a)
#pragma unroll
      for (int b = 0; b < 6; ++b) { const float i0 = b < 4 ? EPS : 0.f; acc[a][b] = f32x4{i0, i0, i0, i0}; PIN(acc[a][b]); }
    Frags F0, F1;
    waitv<0>(); BAR();
    STAMP(1);
    ldfr(F0, bA, SLOT, 0);
#define STAGE(B0, O0, B1, O1) \
      ldfr(F1, B0, O0, 1); mm(acc, F0); interleave<16, 24>(); SCHED(); \
      waitl(); BAR(); \
      ldfr(F0, B1, O1, 0); mm(acc, F1); interleave<16, 24>(); SCHED();
#pragma unroll 1
    for (int s = 0; s < 5; ++s) { STAGE(bA, SLOT, bB, 0) STAGE(bB, 0, bA, 0) STAGE(bA, 0, bA, SLOT) }
    ldfr(F1, bA, SLOT, 1); mm(acc, F0); interleave<16, 24>(); SCHED();
    mm(acc, F1);
  }
  STAMP(2);
  const u32x2 tag = *L<u32x2>(L_FLAG + 4 * W_TAG);
  {
    f32x4 kvp[2][2], zp[2];
    const f32x4 zero = {0, 0, 0, 0};
    const u32x4 ones = {0x3f803f80u, 0x3f803f80u, 0x3f803f80u, 0x3f803f80u};
#pragma unroll
    for (int u = 0; u < 2; ++u) {
      u32x4 pkf[2], vf[2];
#pragma unroll
      for (int t = 0; t < 2; ++t) { pkf[t] = pack_phi(acc[2 * u][2 + t], acc[2 * u + 1][2 + t]); vf[t] = pack8(acc[2 * u][4 + t], acc[2 * u + 1][4 + t]); }
      const u32x4 vk = mh ? vf[1] : vf[0], vs = mh ? vf[0] : vf[1];
      const u32x4 zk = mh ? pkf[1] : pkf[0], zs = mh ? pkf[0] : pkf[1];
#pragma unroll
      for (int dt = 0; dt < 2; ++dt) kvp[dt][1] = mfma(pkf[dt], vs, u ? kvp[dt][1] : zero);
      zp[1] = mfma(zs, ones, u ? zp[1] : zero);
#pragma unroll
      for (int dt = 0; dt < 2; ++dt) kvp[dt][0] = mfma(pkf[dt], vk, u ? kvp[dt][0] : zero);
      zp[0] = mfma(zk, ones, u ? zp[0] : zero);
    }
    const u32 mine = L_X1 + wave * 2304, theirs = L_X1 + (wave ^ 1) * 2304;
#pragma unroll
    for (int dt = 0; dt < 2; ++dt) *L<f32x4>(mine + dt * 1024 + lane * 16) = kvp[dt][1];
    if (r16 == 0) *L<f32x4>(mine + 2048 + g * 16) = zp[1];
    lds_set(W_X1 + wave, 1);
    f32x4 o0, o1, oz;
#pragma unroll 1
    for (u32 spins = 0; spins < (SPIN_CAP << 3); ++spins) {
      u32 fl;
      asm volatile("ds_read_b32 %0, %4\n\tds_read_b128 %1, %5\n\tds_read_b128 %2, %5 offset:1024\n\tds_read_b128 %3, %6\n\ts_waitcnt lgkmcnt(0)"
                   : "=&v"(fl), "=&v"(o0), "=&v"(o1), "=&v"(oz)
                   : "v"(L_FLAG + 4 * (W_X1 + (wave ^ 1))), "v"(theirs + lane * 16), "v"(theirs + 2048 + g * 16) : "memory");
      if (__builtin_amdgcn_readfirstlane(fl)) break;
    }
    const u32 head = cb * 2 + hh;
    st128_sc(pack8(kvp[0][0] + o0, kvp[1][0] + o1), ws, WS_PART + (head * 2 + mh) * 16384 + rb * 1024 + lane * 16);
    st128_sc(__builtin_bit_cast(u32x4, zp[0] + oz), ws, r16 == 0 ? WS_Z + head * 2048 + rb * 128 + mh * 64 + g * 16 : 0x40000000u);
  }
  lds_inc(W_PST);
  SCHED();
  STAMP(10);
  u32x4 pq[4];
#pragma unroll
  for (int mt = 0; mt < 1; ++mt) { pq[mt] = pack_phi(acc[mt][0], acc[mt][1]); PIN(pq[mt]); }
  SCHED();
  const bool last1 = drain_last(W_CNT1, 4);
  if (last1) post(ws, WS_F1 + (cb * 16 + lane) * 256 + rb * 8, tag, lane < 16);
  STAMP(3);
  SCHED();
#pragma unroll
  for (int mt = 1; mt < 4; ++mt) { pq[mt] = pack_phi(acc[mt][0], acc[mt][1]); PIN(pq[mt]); }
  u32x4 sel[2];
  {
    const u32 one = (g == q4) ? ((p4 & 1) ? 0x3f800000u : 0x3f80u) : 0u, a = (p4 & 2) ? 0u : one, b = (p4 & 2) ? one : 0u;
    sel[0] = u32x4{a, b, 0, 0}; sel[1] = u32x4{0, 0, a, b};
    PIN(sel[0]); PIN(sel[1]);
  }
  SCHED();
  if (last1) { await<0>(ws, WS_F1 + (cq * 16 + rb) * 256 + r16 * 8, tag); lds_set(W_GOT1, 1); }
  else lds_await<1>(W_GOT1, 1);
  STAMP(4);
  {
    const u32 head = cq * 2 + hh;
    u32x4 pr[16], zr[2];
#pragma unroll
    for (int n = 0; n < 2; ++n) zr[n] = ld128_sc(ws, lane * 16 + n * 1024, WS_Z + head * 2048);
#pragma unroll
    for (int r = 0; r < 16; ++r) pr[r] = ld128_sc(ws, lane * 16 + (r & 1) * 1024, WS_PART + (head * 2 + mh) * 16384 + ((r >> 1) ^ (rb & 7)) * 2048);
    SCHED();
    f32x4 zs = __builtin_bit_cast(f32x4, zr[0]) + __builtin_bit_cast(f32x4, zr[1]);
#pragma unroll
    for (int r = 0; r < 4; ++r) zs[r] = sum_8_16_32(zs[r]);
    const u32 zb = L_X2 + 4096 + wave * 64;
    if (lane < 8) *L<u32x2>(zb + (lane & 3) * 16 + (lane >> 2) * 8) = u32x2{pk2(zs[0], zs[1]), pk2(zs[2], zs[3])};
    FENCE();
    const u32x4 zf = *L<u32x4>(zb + g * 16);
    const f32x4 zero = {0, 0, 0, 0}, eps4 = {EPS, EPS, EPS, EPS};
    float inv[4];
#pragma unroll
    for (int mt = 0; mt < 4; ++mt) { inv[mt] = __builtin_amdgcn_rcpf(mfma(zf, pq[mt], eps4)[0]); PIN(inv[mt]); }
    SCHED();
    f32x4 sum[2] = {zero, zero};
#pragma unroll
    for (int r = 0; r < 16; ++r) { sum[0] = mfma(sel[0], pr[r], sum[0]); sum[1] = mfma(sel[1], pr[r], sum[1]); }
    const u32x4 kvm = pack8(sum[0], sum[1]);
    *L<u32x4>(L_X2 + wave * 1024 + lane * 16) = kvm;
    lds_set(W_X2 + wave, 1);
    STAMP(11);
    u32x2 om[4];
#pragma unroll
    for (int mt = 0; mt < 4; ++mt) { const f32x4 n = mfma(kvm, pq[mt], zero) * inv[mt]; om[mt] = u32x2{pk2(n[0], n[1]), pk2(n[2], n[3])}; PIN(om[mt]); }
    u32x4 kvo;
#pragma unroll 1
    for (u32 spins = 0; spins < (SPIN_CAP << 3); ++spins) {
      u32 fl;
      asm volatile("ds_read_b32 %0, %2\n\tds_read_b128 %1, %3\n\ts_waitcnt lgkmcnt(0)" : "=&v"(fl), "=&v"(kvo)
                   : "v"(L_FLAG + 4 * (W_X2 + (wave ^ 1))), "v"(L_X2 + (wave ^ 1) * 1024 + lane * 16) : "memory");
      if (__builtin_amdgcn_readfirstlane(fl)) break;
    }
    const u32 oo = WS_O + rb * 262144 + mh * 131072 + head * 2048 + lane * 16;
#pragma unroll
    for (int mt = 0; mt < 4; ++mt) {
      const f32x4 n = mfma(kvo, pq[mt], zero) * inv[mt];
      const u32x2 ot = {pk2(n[0], n[1]), pk2(n[2], n[3])};
      st128_sc(mh ? u32x4{ot.x, ot.y, om[mt].x, om[mt].y} : u32x4{om[mt].x, om[mt].y, ot.x, ot.y}, ws, oo + (mt >> 1) * 65536 + (mt & 1) * 1024);
    }
  }
  STAMP(5);
  u32 wb[4];
  {
    const int fw = (2 * g + (q4 >> 1)) & 3;
#pragma unroll
    for (int t = 0; t < 4; ++t) wb[t] = opaque((4 * g + q4) * 128 + ((t ^ fw) * 32) + p4 * 8);
  }
  f32x4 oc[2][4];
#pragma unroll
  for (int m = 0; m < 2; ++m)
#pragma unroll
    for (int t = 0; t < 4; ++t) { oc[m][t] = f32x4{0, 0, 0, 0}; PIN(oc[m][t]); }
  if (drain_last(W_CNT2, 4)) {
    post(ws, WS_F2 + (rb * 16 + lane) * 256 + cb * 8, tag, lane < 16);
    STAMP(6);
    await<0>(ws, WS_F2 + (rb2 * 16 + cb2) * 256 + r16 * 8, tag);
    lds_set(W_GOT2, 1);
    if (bid == 0) post(ws, WS_EPOCH, tag, lane == 0);
  } else { STAMP(6); lds_await<1>(W_GOT2, 1); }
  STAMP(7);
  {
    u32x4 of[WIN][2];
    const u32 ob = WS_O + rb2 * 262144 + wave * 65536, vo = opaque(lane * 16);
    auto ldo = [&](int st, int m) __attribute__((always_inline)) { of[st % WIN][m] = ld128_sc(ws, vo + ((st & 1) * 2 + m) * 1024, ob + (((st & ~1) ^ hx) * 2048)); };
#pragma unroll
    for (int st = 0; st < WIN; ++st) { ldo(st, 0); ldo(st, 1); SCHED(); }
    lds_await<0>(W_WO1, 4);
    const bool wo2 = lds_get(W_WO2);
    auto ldw = [&](u32x4 (&w)[4], int st) __attribute__((always_inline)) {
      const u32 off = st < 24 ? (st & 15) * 4096 : ((st - 24) >> 1) * CH_STRIDE + (st & 1) * 4096;
#pragma unroll
      for (int t = 0; t < 4; ++t) w[t] = tr_frag(wb[t] + off, 2048);
    };
    u32x4 wf[2][4];
    ldw(wf[0], 0);
#pragma unroll
    for (int st = 0; st < 32; ++st) {
      if (st == 15 || st == 23) {
#pragma unroll
        for (int t = 0; t < 4; ++t) wb[t] = opaque(wb[t] + (st == 15 ? 65536 : L_CH - 65536));
        if (st == 23 && !wo2) lds_await<0>(W_WO2, 1);
      }
      if (st < 31) ldw(wf[(st + 1) & 1], st + 1);
#pragma unroll
      for (int m = 0; m < 2; ++m)
#pragma unroll
        for (int t = 0; t < 4; ++t) oc[m][t] = mfma(wf[st & 1][t], of[st % WIN][m], oc[m][t]);
      if (st + WIN < 32) { ldo(st + WIN, 0); ldo(st + WIN, 1); }
#pragma unroll
      for (int m = 0; m < 2; ++m) {
        __builtin_amdgcn_sched_group_barrier(0x8, 1, 0); __builtin_amdgcn_sched_group_barrier(0x100, 2, 0);
        __builtin_amdgcn_sched_group_barrier(0x8, 1, 0); __builtin_amdgcn_sched_group_barrier(0x100, 1, 0);
        __builtin_amdgcn_sched_group_barrier(0x8, 1, 0); __builtin_amdgcn_sched_group_barrier(0x100, 1, 0);
        __builtin_amdgcn_sched_group_barrier(0x8, 1, 0); __builtin_amdgcn_sched_group_barrier(0x20, 1, 0);
      }
      SCHED();
    }
    STAMP(8);
    const rsrc_t ro = mkrsrc(p.out, 2048 * 2048 * 2);
    const u32 sc = L_X1 + (wave ^ 1) * 2304;
    const u32 sw_ = opaque(sc + r16 * 144 + g * 8), sr_ = opaque(sc + r8 * 144 + c8 * 16);
    const u32 oo = (rb2 * 128 + wave * 32 + r8) * 4096 + 2048 + cb2 * 128 + c8 * 16;
#pragma unroll
    for (int m = 0; m < 2; ++m) {
#pragma unroll
      for (int t = 0; t < 4; ++t) *L<u32x2>(sw_ + t * 32) = u32x2{pk2(oc[m][t][0], oc[m][t][1]), pk2(oc[m][t][2], oc[m][t][3])};
      FENCE();
      const u32x4 r0 = *L<u32x4>(sr_), r1 = *L<u32x4>(sr_ + 1152);
      FENCE();
      st128_sc(r0, ro, oo + m * 65536); st128_sc(r1, ro, oo + m * 65536 + 32768);
    }
  }
  STAMP(9);
  STAMP_OUT();
}

extern "C" void kernel_launch(void* const* d_in, const int* in_sizes, int n_in, void* d_out, int out_size, void* d_ws, size_t ws_size,
                              hipStream_t stream) {
  Params p;
  p.q = (const u16*)d_in[0]; p.k = (const u16*)d_in[1]; p.v = (const u16*)d_in[2]; p.sk = (const u16*)d_in[3];
  p.h = (const u16*)d_in[4]; p.dd = (const u16*)d_in[5]; p.x = (const u16*)d_in[6]; p.wqkv = (const u16*)d_in[7];
  p.wo = (const u16*)d_in[8]; p.out = (u16*)d_out; p.ws = (char*)d_ws;
  (void)hipFuncSetAttribute((const void*)fused_h3_linattn, hipFuncAttributeMaxDynamicSharedMemorySize, LDS_BYTES);
  fused_h3_linattn<<<256, 512, LDS_BYTES, stream>>>(p);
}
```

```cpp
#include <hip/hip_runtime.h>
#include <hip/hip_bf16.h>

typedef unsigned short u16;
typedef unsigned int u32;
typedef unsigned long long u64;
typedef float f32x2 __attribute__((ext_vector_type(2)));
typedef float f32x4 __attribute__((ext_vector_type(4)));
typedef u32 u32x2 __attribute__((ext_vector_type(2)));
typedef u32 u32x4 __attribute__((ext_vector_type(4)));
typedef short s16x4 __attribute__((ext_vector_type(4)));
typedef __bf16 bf16x2 __attribute__((ext_vector_type(2)));
typedef __bf16 bf16x8 __attribute__((ext_vector_type(8)));
typedef __amdgpu_buffer_rsrc_t rsrc_t;
#define LAS __attribute__((address_space(3)))
#define DI __device__ __forceinline__
#if defined(__HIP_DEVICE_COMPILE__)
#define NOPK __attribute__((target("no-packed-fp32-ops")))
#else
#define NOPK
#endif

#define CODE_PF 27392
#define KTAB 1
#define S0 0
#define NSG 3
#define XL 0
#define KSL 8
#define VCH 3
#define QP 2
#define A2D 24
#define STG 0
#define XB 0
#define TLBW 1
#define WIN 5
#define STAMPS 0

constexpr u32 WS_EPOCH = 0;
constexpr u32 WS_F1 = 65536;
constexpr u32 WS_F2 = WS_F1 + 65536;
constexpr u32 WS_FA = WS_F2 + 65536;
constexpr u32 WS_FY = WS_FA + 131072;
constexpr u32 WS_Z = 512u << 10;
constexpr u32 WS_PART = 1u << 20;
constexpr u32 WS_O = 2u << 20;
constexpr u32 WS_KVT = 6u << 20;
constexpr u32 WS_YT = 10u << 20;
constexpr u32 WS_BYTES = 14u << 20;
constexpr u32 WS_DBG = 32u << 20;

constexpr u32 SLOT = 40960;
constexpr u32 L_X1 = 98304;
constexpr u32 L_X2 = L_X1 + 4 * 2304;
constexpr u32 CX_STRIDE = 4640, CH_STRIDE = 8320, C_E = 4160;
constexpr u32 L_FLAG = 163840 - 128;
constexpr u32 L_CH = L_FLAG - 4 * CH_STRIDE;
constexpr u32 L_CX = L_CH - 4 * CX_STRIDE;
constexpr u32 LDS_BYTES = 163840;
static_assert(L_X2 + 4096 + 256 <= L_CX && L_CH >= 3 * SLOT, "lds map");
enum { W_TAG = 0, W_WO1 = 2, W_WO2 = 3, W_X1 = 4, W_X2 = 8, W_CNT1 = 12, W_CNT2, W_CNTY, W_GOT1, W_GOT2, W_STAGED, W_PST };

constexpr int AUX_SC1 = 16;
constexpr u32 SPIN_CAP = 1u << 16;

DI float f_(u32 x) { return __builtin_bit_cast(float, x); }
DI u32 u_(float x) { return __builtin_bit_cast(u32, x); }
DI f32x4 mfma(u32x4 a, u32x4 b, f32x4 c) {
  return __builtin_amdgcn_mfma_f32_16x16x32_bf16(__builtin_bit_cast(bf16x8, a), __builtin_bit_cast(bf16x8, b), c, 0, 0, 0);
}
DI u32 pk2(float lo, float hi) { return __builtin_bit_cast(u32, __builtin_convertvector(f32x2{lo, hi}, bf16x2)); }
DI float blo(u32 x) { return f_(x << 16); }
DI float bhi(u32 x) { return f_(x & 0xffff0000u); }
template <int N> DI void waitv() { asm volatile("s_waitcnt vmcnt(%0)" ::"n"(N) : "memory"); }
DI void waitl() { asm volatile("" ::: "memory"); __builtin_amdgcn_s_waitcnt(0xC07F); asm volatile("" ::: "memory"); }
#define SCHED() __builtin_amdgcn_sched_barrier(0)
#define FENCE() asm volatile("" ::: "memory")
#define BAR() do { FENCE(); SCHED(); __builtin_amdgcn_s_barrier(); SCHED(); FENCE(); } while (0)
#define PIN(x) asm volatile("" : "+v"(x))
template <class T> DI LAS T* L(u32 a) { return (LAS T*)(size_t)a; }
DI u32 opaque(u32 a) { PIN(a); return a; }
DI rsrc_t mkrsrc(const void* p, u32 bytes) { return __builtin_amdgcn_make_buffer_rsrc((void*)p, 0, bytes, 0x20000); }
template <int LI, int SI> DI void dma(rsrc_t r, u32 lds, u32 voff, u32 soff) {
  u32 t;
  asm volatile("s_add_u32 m0, %1, %2\n\ts_add_u32 %0, %5, %6\n\tbuffer_load_dwordx4 %3, %4, %0 offen lds"
               : "=&s"(t) : "s"(lds), "i"(LI), "v"(voff), "s"(r), "s"(soff), "i"(SI) : "memory", "scc");
}
DI u32x4 ld128(rsrc_t r, u32 voff, u32 soff) { return __builtin_amdgcn_raw_buffer_load_b128(r, voff, soff, 0); }
DI u32x4 ld128_sc(rsrc_t r, u32 voff, u32 soff) { return __builtin_amdgcn_raw_buffer_load_b128(r, voff, soff, AUX_SC1); }
DI void st128_sc(u32x4 v, rsrc_t r, u32 voff) { __builtin_amdgcn_raw_buffer_store_b128(v, r, voff, 0, AUX_SC1); }

DI u32 lds_get(int word) { return __builtin_amdgcn_readfirstlane(__hip_atomic_load(L<u32>(L_FLAG + 4 * word), __ATOMIC_RELAXED, __HIP_MEMORY_SCOPE_WORKGROUP)); }
DI void lds_set(int word, u32 v) {
  asm volatile("s_mov_b64 exec, 1\n\tds_write_b32 %0, %1\n\ts_mov_b64 exec, -1" :: "v"(L_FLAG + 4 * word), "v"(v) : "memory");
}
DI void lds_inc(int word) {
  asm volatile("s_mov_b64 exec, 1\n\tds_add_u32 %0, %1\n\ts_mov_b64 exec, -1" :: "v"(L_FLAG + 4 * word), "v"(1u) : "memory");
}
template <int SL> DI void lds_await(int word, u32 want) {
#pragma unroll 1
  for (u32 spins = 0; spins < (SPIN_CAP << 3); ++spins) {
    if (lds_get(word) >= want) break;
    if (SL) __builtin_amdgcn_s_sleep(SL);
  }
  FENCE();
}
DI bool drain_last(int word, u32 n) {
  u32 old;
  asm volatile("s_waitcnt vmcnt(0)\n\ts_mov_b64 exec, 1\n\tds_add_rtn_u32 %0, %1, %2\n\ts_mov_b64 exec, -1\n\ts_waitcnt lgkmcnt(0)"
               : "=&v"(old) : "v"(L_FLAG + 4 * word), "v"(1u) : "memory");
  return __builtin_amdgcn_readfirstlane(old) == n - 1;
}
DI void post(rsrc_t ws, u32 voff, u32x2 tag, bool on) { __builtin_amdgcn_raw_buffer_store_b64(tag, ws, on ? voff : 0x40000000u, 0, AUX_SC1); }
template <int SL> DI void await(rsrc_t ws, u32 voff, u32x2 tag) {
#pragma unroll 1
  for (u32 spins = 0; spins < SPIN_CAP; ++spins) {
    u32x2 f;
    asm volatile("buffer_load_dwordx2 %0, %1, %2, 0 offen sc1\n\ts_waitcnt vmcnt(0)" : "=&v"(f) : "v"(voff), "s"(ws) : "memory");
    if (__builtin_amdgcn_ballot_w64(f.x != tag.x || f.y != tag.y) == 0) break;
    if (SL) __builtin_amdgcn_s_sleep(SL);
  }
  FENCE();
}

#define STAMPINS "s_memrealtime"
#define STAMP(i)
#define STAMP_OUT()

struct Params {
  const u16 *x, *wqkv;
  char* ws;
  const u16 *wo, *q, *k, *v, *sk, *h, *dd;
  u16* out;
  u64 kord[32];
};

struct Frags { u32x4 x[4]; u32x4 w[6]; };
DI u32x4 tr_frag(u32 a, u32 d) {
  s16x4 lo = __builtin_amdgcn_ds_read_tr16_b64_v4i16(L<s16x4>(a));
  s16x4 hi = __builtin_amdgcn_ds_read_tr16_b64_v4i16(L<s16x4>(a + d));
  return __builtin_bit_cast(u32x4, __builtin_shufflevector(lo, hi, 0, 1, 2, 3, 4, 5, 6, 7));
}
DI void ldfr(Frags& f, const u32 (&b)[4], u32 off, int ks) {
#pragma unroll
  for (int mt = 0; mt < 4; ++mt) f.x[mt] = *L<u32x4>(b[ks] + off + mt * 2048);
#pragma unroll
  for (int s_ = 1; s_ < 4; ++s_)
#pragma unroll
    for (int t = 0; t < 2; ++t) { const int sg = s_ % 3; f.w[sg * 2 + t] = tr_frag(b[2 + t] + off + sg * 8192 + ks * 4096, 512); }
}
DI void mm(f32x4 (&acc)[4][6], const Frags& f) {
#pragma unroll
  for (int i = 0; i < 24; ++i) {
    const int n_ = i >> 2, n = n_ < 4 ? n_ + 2 : n_ - 4, mt = (n_ & 1) ? 3 - (i & 3) : (i & 3);
    if (n < 2) acc[mt][n] = mfma(f.w[n], f.x[mt], acc[mt][n]);
    else acc[mt][n] = mfma(f.x[mt], f.w[n], acc[mt][n]);
  }
}
template <int NR, int NM> DI void interleave() {
#pragma unroll
  for (int i = 0; i < NR; ++i) { __builtin_amdgcn_sched_group_barrier(0x8, 1, 0); __builtin_amdgcn_sched_group_barrier(0x100, 1, 0); }
  if (NM > NR) __builtin_amdgcn_sched_group_barrier(0x8, NM - NR, 0);
}
constexpr float EPS = 1e-6f;
DI float phi(float t) {
  const float e = __builtin_amdgcn_fmed3f(__builtin_amdgcn_exp2f(__builtin_fmaf(t, 1.44269504088896f, -EPS * 1.44269504088896f)), 0.f, 1.f);
  return e + __builtin_bit_cast(float, __builtin_elementwise_max(__builtin_bit_cast(int, t), __builtin_bit_cast(int, EPS)));
}
DI u32x4 pack_phi(f32x4 a, f32x4 b) { return u32x4{pk2(phi(a[0]), phi(a[1])), pk2(phi(a[2]), phi(a[3])), pk2(phi(b[0]), phi(b[1])), pk2(phi(b[2]), phi(b[3]))}; }
DI u32x4 pack8(f32x4 a, f32x4 b) { return u32x4{pk2(a[0], a[1]), pk2(a[2], a[3]), pk2(b[0], b[1]), pk2(b[2], b[3])}; }
DI float sum_8_16_32(float v) {
  v += f_(__builtin_amdgcn_update_dpp(0, u_(v), 0x128, 0xf, 0xf, false));
  auto a = __builtin_amdgcn_permlane16_swap(u_(v), u_(v), false, false);
  v = f_(a[0]) + f_(a[1]);
  auto b = __builtin_amdgcn_permlane32_swap(u_(v), u_(v), false, false);
  return f_(b[0]) + f_(b[1]);
}

DI void mfma_ip(f32x4& c, u32x4 a, u32x4 b) { asm volatile("v_mfma_f32_16x16x32_bf16 %0, %1, %2, %0" : "+v"(c) : "v"(a), "v"(b)); }
struct CFr { u32x2 a[8][2]; u32x4 b[8]; };
DI void ldconv1(CFr& f, const u32 (&hb)[4], u32 xb, u32 off, int k) {
  const int i = k / 3, e = k % 3;
  if (e == 0) f.b[i] = *L<u32x4>(xb + off + (7 - i) * 512);
  else { LAS u32* hp = L<u32>(hb[i >> 1] + off + (i & 1) * 512 + (e - 1) * 8); f.a[i][e - 1] = u32x2{hp[0], hp[1]}; }
}
DI void mmconv1(f32x4 (&acc)[8], const CFr& f, int k) {
  const int n = (k >= 1) + (k >= 3) + (k >= 6) + (k >= 10) + (k >= 15) + (k >= 21) + (k >= 28);
  k -= n * (n + 1) / 2;
  mfma_ip(acc[n], u32x4{f.a[k][0].x, f.a[k][0].y, f.a[k][1].x, f.a[k][1].y}, f.b[n - k]);
}

__global__ NOPK __launch_bounds__(512, 2) void fused_h3_linattn(Params p) {
  const int bid = __builtin_amdgcn_workgroup_id_x(), xcd = bid & 7, j = bid >> 3;
  const int rb = (xcd & 1) * 8 + (j & 7), cb = (xcd >> 1) * 4 + (j >> 3);
  const int cq = (cb & 12) | ((cb + 1) & 3);
  const int rb2 = (xcd >> 1) * 4 + (j & 3), cb2 = (xcd & 1) * 8 + (j >> 2);
  const int hx = (((j >> 2) + 2 * (j & 3)) & 7) * 4;
  const int wave = __builtin_amdgcn_readfirstlane(__builtin_amdgcn_workitem_id_x() >> 6), lane = __builtin_amdgcn_workitem_id_x() & 63;
  const int r8 = lane >> 3, c8 = lane & 7;
  STAMP(0);
  const u16 *pwo = p.wo, *pq_ = p.q, *pk_ = p.k, *pv_ = p.v, *psk = p.sk, *ph_ = p.h, *pdd = p.dd;
  u16* pout = p.out;
  asm volatile("" : "+s"(pwo), "+s"(pq_), "+s"(pk_), "+s"(pv_), "+s"(psk), "+s"(ph_), "+s"(pdd), "+s"(pout));
  const rsrc_t ws = mkrsrc(p.ws, WS_BYTES), rx = mkrsrc(p.x, 2048 * 1024 * 2), rw = mkrsrc(p.wqkv, 1024 * 3072 * 2);
  const int rot = rb - cb;
  const u32 qd = (cq - cb) * 128;
  u32 vx[2], vw[2];
#pragma unroll
  for (int e = 0; e < 2; ++e) {
    vx[e] = r8 * 2048 + ((c8 ^ (4 * e + (r8 >> 1))) * 16);
    vw[e] = r8 * 6144 + ((c8 ^ ((((r8 >> 1) & 1) | (e << 1)) << 1)) * 16);
  }
  {
    const u32 s = rot & 15, sx = (rb * 128 + wave * 16) * 2048 + s * 128, sw = (s * 64 + wave * 8) * 6144 + cb * 128;
    const u32 vwp = vw[0] ^ ((wave & 1) * 64);
    dma<SLOT, 0>(rx, wave * 2048, vx[0], sx);
    dma<SLOT + 16384 + 8192, 2048>(rw, wave * 1024, vwp, sw);
    dma<SLOT + 16384, 0>(rw, wave * 1024, vwp, sw + qd);
    dma<SLOT + 16384 + 16384, 4096>(rw, wave * 1024, vwp, sw);
    dma<SLOT + 1024, 16384>(rx, wave * 2048, vx[1], sx);
  }
  STAMP(24);
  u64 ep0, ep, kord0;
  asm volatile("s_load_dwordx2 %0, %2, 0\n\ts_load_dwordx2 %1, %3, %4" : "=&s"(ep0), "=&s"(kord0)
               : "s"(p.ws), "s"(__builtin_amdgcn_kernarg_segment_ptr()), "s"((u32)offsetof(Params, kord) + j * 8) : "memory");
#define GET_TAG() do { asm volatile("s_waitcnt lgkmcnt(0)\n\ts_mov_b64 %0, %1" : "=s"(ep) : "s"(ep0), "s"(kord0) : "memory"); ep = ep * 6364136223846793005ull + 1442695040888963407ull; } while (0)
#define tag (u32x2{(u32)ep, (u32)(ep >> 32)})

  if (wave >= 4) {
    const int hw = wave - 4, aw = hw & 1, ca = (cb + aw) & 15;
    const rsrc_t rwo = mkrsrc(pwo, XL >= 4 ? 0 : 1024 * 1024 * 2);
    const u32 vwo = r8 * 2048 + ((c8 ^ (((r8 >> 1) & 3) << 1)) * 16);
    const int R2 = rb * 128 + r8 * 16 + aw * 8;
    const rsrc_t rq = mkrsrc(pq_, hw >= 2 && XL < 1 ? 2048 * 1024 * 2 : 0);
    u32x4 qq[8];
    const int chl = (rb >> 3) * 32 + hw * 8 + (rb & 7);
    const u32 cx = L_CX + hw * CX_STRIDE, chr = L_CH + hw * CH_STRIDE;
    if (hw < 2) {
      const u32 sx0 = (rb * 128 + hw * 64) * 2048, sw0 = (hw * 32) * 6144 + cb * 128;
      const int kbase = (xcd & 1) * 8 - (xcd >> 1) * 4;
      u64 kord = (((j & 16) ? 0x2433220043433f3full : 0x442432f650332105ull) >> (4 * (j & 15))) << 4;
      auto issue = [&](int s_, u32 base, int half) __attribute__((always_inline)) {
        const u32 s = (KTAB ? (u32)(kord >> (4 * s_)) + kbase : s_ + rot) & 15;
        const u32 sx = sx0 + s * 128 + half * 65536, sw = sw0 + s * (64 * 6144) + half * 98304, sq = sw + qd;
        const u32 lx = base + hw * 8192 + half * 4096, lw = base + 16384 + hw * 4096 + half * 2048;
        dma<0, 0>(rx, lx, vx[0], sx); dma<1024, 16384>(rx, lx, vx[1], sx);
        dma<8192, 2048>(rw, lw, vw[0], sw); dma<9216, 2048 + 49152>(rw, lw, vw[1], sw);
        dma<0, 0>(rw, lw, vw[0], sq); dma<1024, 49152>(rw, lw, vw[1], sq);
        dma<16384, 4096>(rw, lw, vw[0], sw); dma<17408, 4096 + 49152>(rw, lw, vw[1], sw);
        dma<2048, 32768>(rx, lx, vx[0], sx); dma<3072, 49152>(rx, lx, vx[1], sx);
      };
      issue(1, 2 * SLOT, 0);
      waitv<10>(); GET_TAG();
      asm volatile("s_mov_b64 %0, %1" : "=s"(kord) : "s"(kord0) : "memory");
      BAR();
      issue(1, 2 * SLOT, 1);
      u32 base = 0;
#pragma unroll 1
      for (int s = 2; s < 16; ++s) {
        issue(s, base, 0); issue(s, base, 1);
        base = base == 2 * SLOT ? 0 : base + SLOT;
        waitv<20>(); BAR();
      }
      waitv<0>(); BAR();
      if (QP == 2) {
#pragma unroll
        for (int i = 0; i < 8; ++i) asm volatile("" : "=v"(qq[i]));
      }
    } else {
      const int R0 = rb * 128 + aw * 64 + r8 * 8;
      const rsrc_t rk = mkrsrc(pk_, (XB & 1) ? 0 : 2048 * 1024 * 2), rv = mkrsrc(pv_, (XB & 1) ? 0 : 2048 * 1024 * 2), rs = mkrsrc(psk, 1024 * 4 * 2);
      const u32 vo = R0 * 2048 + ca * 128 + c8 * 16;
      const u32 vh = R0 == 0 ? 0x40000000u : vo - 3 * 2048;
      const u32 kvt_vo = WS_KVT + ca * 262144 + (c8 >> 2) * 131072 + (255 - (R0 >> 3)) * 64 + (c8 & 3) * 16;
      u32x4 kk[11], vv[8], tw[4];
      waitv<0>(); GET_TAG(); BAR();
#pragma unroll
      for (int i = 0; i < 4; ++i) tw[i] = ld128(rs, ca * 512 + c8 * 64 + i * 16, 0);
#define AB(i) do { BAR(); if (STG) STAMP(8 + (i)); } while (0)
      if (lane < 8) *L<u32x4>(L_CH + aw * 2 * CH_STRIDE + (lane >> 1) * C_E + (lane & 1) * 16) = u32x4{0, 0, 0, 0};
      const int sg = S0 + (NSG > 1 ? (j + (j >> 3) + xcd) % NSG : 0);
#pragma unroll 1
      for (int i = 0; i <= sg; ++i) AB(i);
      if (KSL) __builtin_amdgcn_s_sleep(KSL);
#pragma unroll
      for (int i = 0; i < 8; ++i) kk[3 + i] = ld128(rk, vo, i * 2048);
#pragma unroll
      for (int i = 0; i < 3; ++i) kk[i] = ld128(rk, vh, i * 2048);
      AB(sg + 1);
      float wf[8][4], ks[8][8], kf[11];
#pragma unroll
      for (int c = 0; c < 8; ++c) {
        if (!(c & 1)) PIN(tw[c >> 1]);
#pragma unroll
        for (int t = 0; t < 4; ++t) { const u32 d = tw[c >> 1][(c & 1) * 2 + (t >> 1)]; wf[c][t] = (t & 1) ? bhi(d) : blo(d); PIN(wf[c][t]); }
      }
#pragma unroll
      for (int u = 0; u < 24; ++u) {
        if (u % 3 == 0) {
          if (u == 3 * VCH) {
#pragma unroll
            for (int i = 0; i < 8; ++i) vv[i] = ld128(rv, vo, i * 2048);
          }
          AB(sg + 2 + u / 3);
        }
        if (XB & 4) continue;
        if (u < 16) {
          const int c = u >> 1, hf = u & 1;
#pragma unroll
          for (int i = hf ? 7 : 0; i < (hf ? 11 : 7); ++i) { PIN(kk[i]); const u32 d = kk[i][c >> 1]; kf[i] = (c & 1) ? bhi(d) : blo(d); }
#pragma unroll
          for (int i = hf * 4; i < hf * 4 + 4; ++i) {
            if (hf && i < 7) PIN(kf[i]);
            float a = kf[i] * wf[c][3];
            a = __builtin_fmaf(kf[i + 1], wf[c][2], a); a = __builtin_fmaf(kf[i + 2], wf[c][1], a); a = __builtin_fmaf(kf[i + 3], wf[c][0], a);
            ks[c][i] = a; PIN(ks[c][i]);
          }
        } else {
          const int c = u - 16;
          float o[8];
#pragma unroll
          for (int i = 0; i < 8; ++i) { PIN(vv[i]); const u32 d = vv[i][c >> 1]; o[i] = ks[c][i] * ((c & 1) ? bhi(d) : blo(d)); }
          st128_sc(u32x4{pk2(o[7], o[6]), pk2(o[5], o[4]), pk2(o[3], o[2]), pk2(o[1], o[0])}, ws, (XB & 2) ? 0x40000000u : kvt_vo + c * 16384);
        }
      }
      AB(sg + 10);
      const u32 pa = opaque(lane < 16 ? WS_FA + ((ca * 16 + lane) * 2 + (rb < 8)) * 256 + ((rb & 7) * 2 + aw) * 8 : 0x40000000u);
      AB(sg + 11);
      waitv<0>();
      post(ws, pa, tag, true);
#pragma unroll 1
      for (int i = sg + 12; i <= 13; ++i) AB(i);
      u32x4 hh4[2][4], xs[8];
      const u32 fa = WS_FA + ((cb * 16 + rb) * 2 + aw) * 256 + (lane & 15) * 8;
      u32x2 fl;
      asm volatile("buffer_load_dwordx2 %0, %1, %2, 0 offen sc1" : "=&v"(fl) : "v"(fa), "s"(ws) : "memory");
      {
        const rsrc_t rh = mkrsrc(ph_, XL >= 5 ? 0 : 1024 * 2048 * 2);
#pragma unroll
        for (int c = 0; c < 2; ++c)
#pragma unroll
          for (int n = 0; n < 4; ++n) hh4[c][n] = ld128(rh, lane * 16 + n * 1024, (cb * 64 + (rb >> 3) * 32 + (aw * 2 + c) * 8 + (rb & 7)) * 4096);
      }
      BAR();
      if (STG) STAMP(22);
      asm volatile("s_waitcnt vmcnt(8)" : "+v"(fl) :: "memory");
      if (__builtin_amdgcn_ballot_w64(fl.x != tag.x || fl.y != tag.y)) await<4>(ws, fa, tag);
      STAMP(23);
#pragma unroll
      for (int n = 0; n < 8; ++n) xs[n] = ld128_sc(ws, XL >= 5 ? 0x40000000u : lane * 16 + (n & 3) * 1024, WS_KVT + cb * 262144 + rb * 16384 + aw * 8192 + (n >> 2) * 4096);
      if (QP == 2) {
#pragma unroll
        for (int i = 0; i < 8; ++i) qq[i] = ld128(rq, R2 * 2048 + ca * 128 + c8 * 16, i * 2048);
      }
      SCHED();
      if (lane < 60) *L<u32x4>(L_CX + (aw * 2 + (lane & 1)) * CX_STRIDE + 4096 + (lane >> 1) * 16) = u32x4{0, 0, 0, 0};
      const u32 hr = opaque(L_CH + aw * 2 * CH_STRIDE + lane * 16);
#pragma unroll
      for (int c = 0; c < 2; ++c)
#pragma unroll
        for (int n = 0; n < 4; ++n) *L<u32x4>(hr + c * CH_STRIDE + 32 + n * 1024) = hh4[c][n];
      FENCE();
      u32 dp[2][4];
#pragma unroll
      for (int c = 0; c < 2; ++c)
#pragma unroll
        for (int n = 0; n < 4; ++n) dp[c][n] = *L<u32>(hr + c * CH_STRIDE + 28 + n * 1024);
#pragma unroll
      for (int c = 0; c < 2; ++c)
#pragma unroll
        for (int n = 0; n < 4; ++n) {
          const u32x4 d = hh4[c][n];
          *L<u32x4>(hr + c * CH_STRIDE + C_E + 32 + n * 1024) =
              u32x4{__builtin_amdgcn_alignbit(d[0], dp[c][n], 16), __builtin_amdgcn_alignbit(d[1], d[0], 16),
                    __builtin_amdgcn_alignbit(d[2], d[1], 16), __builtin_amdgcn_alignbit(d[3], d[2], 16)};
        }
      const u32 xw = opaque(L_CX + (lane & 3) * CX_STRIDE + (aw * 128 + (lane >> 2)) * 16);
#pragma unroll
      for (int n = 0; n < 8; ++n) *L<u32x4>(xw + n * 256) = xs[n];
      lds_inc(W_STAGED);
    }
    u32 dvw;
    asm volatile("s_load_dword %0, %1, 0\n\ts_waitcnt lgkmcnt(0)" : "=s"(dvw) : "s"(pdd + ((cb * 64 + chl) & ~1)));
    const float dv = blo(dvw >> ((chl & 1) * 16));
    const int b16 = lane & 15, kg = lane >> 4;
    u32 hb[4], xb = opaque(cx + (254 - 224 - 2 * b16 + kg) * 16);
#pragma unroll
    for (int i = 0; i < 4; ++i) hb[i] = opaque(chr + ((b16 & 1) ? 0 : C_E) + 4 + 4 * (b16 >> 1) + 16 * kg + i * 1024);
    f32x4 acc[8];
#pragma unroll
    for (int n = 0; n < 8; ++n) { acc[n] = f32x4{0, 0, 0, 0}; PIN(acc[n]); }
    lds_await<1>(W_STAGED, 2);
    STAMP(6);
    CFr C0, C1;
#pragma unroll
    for (int k = 0; k < 24; ++k) ldconv1(C0, hb, xb, 0, k);
    lds_await<1>(W_PST, 4);
    waitl();
    STAMP(1);
#define RESIDUE(CUR, NXT, OFF, ST) do { \
      const u32 so_ = ((ST) ^ hx) * 65536 + cb2 * 128, ld_ = (ST) * 4096; \
      _Pragma("unroll") for (int k = 0; k < 36; ++k) { \
        mmconv1(acc, CUR, k); \
        if (k % 9 == 8) { if (k == 8) dma<0, 0>(rwo, ld_, vwo, so_); else if (k == 17) dma<1024, 16384>(rwo, ld_, vwo, so_); \
                          else if (k == 26) dma<2048, 32768>(rwo, ld_, vwo, so_); else dma<3072, 49152>(rwo, ld_, vwo, so_); } \
        else if (k - k / 9 < 24) ldconv1(NXT, hb, xb, OFF, k - k / 9); \
        SCHED(); \
      } \
      waitl(); SCHED(); \
    } while (0)
#pragma unroll 1
    for (int it = 0; it < (XL >= 3 ? 0 : 3); ++it) {
      RESIDUE(C0, C1, 64, it * 8 + hw);
      RESIDUE(C1, C0, 128, it * 8 + 4 + hw);
      xb = opaque(xb + 128);
#pragma unroll
      for (int i = 0; i < 4; ++i) hb[i] = opaque(hb[i] + 128);
    }
    u32x2 xv[8];
    const u32 xe = opaque(cx + (252 - 16 * b16 - 4 * kg) * 2), vq = opaque(R2 * 2048 + ca * 128 + c8 * 16);
#pragma unroll
    for (int k = 0; k < 36; ++k) {
      mmconv1(acc, C0, k);
      if (k < 24) ldconv1(C1, hb, xb, 64, k); else if (k < 32) xv[k - 24] = *L<u32x2>(xe + (31 - k) * 512);
      if (QP == 0 && (k & 3) == 3 && k < 32) qq[k >> 2] = ld128(rq, vq, (k >> 2) * 2048);
      SCHED();
    }
    if (QP == 0) waitv<8>(); else waitv<0>();
    waitl();
    lds_inc(W_WO1);
    {
      const u32 so = ((24 + hw * 2) ^ hx) * 65536 + cb2 * 128;
#define WO2(k, e) if (k == 4 * e + 3) dma<e * 1024, e * 16384>(rwo, chr, vwo, so);
#pragma unroll
      for (int k = 0; k < 36; ++k) {
        mmconv1(acc, C1, k);
        WO2(k, 0) WO2(k, 1) WO2(k, 2) WO2(k, 3) WO2(k, 4) WO2(k, 5) WO2(k, 6) WO2(k, 7)
        SCHED();
      }
    }
    asm volatile("s_nop 7" : "+v"(acc[7]));
    SCHED();
    STAMP(2);
    {
      u32x2 y[8];
#pragma unroll
      for (int n = 0; n < 8; ++n)
        y[n] = u32x2{pk2(__builtin_fmaf(bhi(xv[n].y), dv, acc[n][0]), __builtin_fmaf(blo(xv[n].y), dv, acc[n][1])),
                     pk2(__builtin_fmaf(bhi(xv[n].x), dv, acc[n][2]), __builtin_fmaf(blo(xv[n].x), dv, acc[n][3]))};
      const u32 yvo = WS_YT + cb * 262144 + ((kg & 1) * 2 + (b16 >> 3)) * 16384 + chl * 256 + (kg >> 1) * 128 + (b16 & 7) * 16;
#pragma unroll
      for (int n = 0; n < 8; n += 2) {
        auto s0 = __builtin_amdgcn_permlane16_swap(y[n].x, y[n + 1].x, false, false);
        auto s1 = __builtin_amdgcn_permlane16_swap(y[n].y, y[n + 1].y, false, false);
        st128_sc(u32x4{s0[0], s1[0], s0[1], s1[1]}, ws, XL >= 2 ? 0x40000000u : yvo + n * 32768);
      }
    }
    if (drain_last(W_CNTY, 4)) {
      lds_set(W_WO2, 1);
      post(ws, WS_FY + (cb * 32 + lane) * 256 + rb * 8, tag, XL < 2 && lane < 32);
    }
    STAMP(3);
    if (hw >= 2 && XL < 1) {
      if (A2D) { lds_await<8>(W_GOT2, 1); __builtin_amdgcn_s_sleep(A2D); } else __builtin_amdgcn_s_sleep(16);
      if (QP == 1) {
#pragma unroll
        for (int i = 0; i < 8; ++i) qq[i] = ld128(rq, vq, i * 2048);
      }
      await<8>(ws, WS_FY + ((ca * 16 + rb) * 2 + aw) * 256 + (lane & 15) * 8, tag);
      STAMP(4);
      u32x4 yy[8];
      const u32 yo = WS_YT + ca * 262144 + rb * 16384 + r8 * 2048 + aw * 128 + c8 * 16;
#pragma unroll
      for (int c = 0; c < 8; ++c) yy[c] = ld128_sc(ws, yo + c * 256, 0);
      const u32 tw_ = opaque(cx + r8 * 144 + c8 * 16), tr_ = opaque(cx + c8 * 144 + r8 * 16);
#pragma unroll
      for (int hf = 0; hf < 2; ++hf) {
#pragma unroll
        for (int c = 0; c < 4; ++c) *L<u32x4>(tw_ + c * 1152) = yy[hf * 4 + c];
        FENCE();
#pragma unroll
        for (int c = 0; c < 4; ++c) yy[hf * 4 + c] = *L<u32x4>(tr_ + c * 1152);
        FENCE();
      }
      const rsrc_t ro = mkrsrc(pout, 2048 * 2048 * 2);
#pragma unroll
      for (int i = 0; i < 8; ++i) {
        u32x4 o;
#pragma unroll
        for (int d = 0; d < 4; ++d) {
          const u32 y0 = yy[2 * d][i >> 1], y1 = yy[2 * d + 1][i >> 1];
          o[d] = pk2(((i & 1) ? bhi(y0) : blo(y0)) * blo(qq[i][d]), ((i & 1) ? bhi(y1) : blo(y1)) * bhi(qq[i][d]));
        }
        st128_sc(o, ro, (R2 + i) * 4096 + ca * 128 + c8 * 16);
      }
    } else { STAMP(4); }
    STAMP(5);
    STAMP_OUT();
    return;
  }

  __builtin_amdgcn_s_setprio(1);
  if (wave == 0 && lane < 32) *L<u32>(L_FLAG + 4 * lane) = 0;
  u32 tlb = 0;
  if (TLBW && wave == 2) {
    const char* a = p.ws + (lane & 7) * (2u << 20) + 8192 + bid * 128;
    if (lane >= 7) a = (const char*)p.wqkv + (lane >= 10 ? 0 : (lane - 7) * (2u << 20)) + bid * 128;
    if (lane == 10) a = (const char*)pk_ + rb * 262144;
    if (lane == 11) a = (const char*)pv_ + rb * 262144;
    if (lane == 12) a = (const char*)pq_ + rb * 262144;
    if (lane == 13) a = (const char*)ph_ + cb * 262144;
    if (lane == 14) a = (const char*)pwo + bid * 128;
    if (lane == 15) a = (const char*)pout + rb * 524288;
    if (lane == 16) a = (const char*)pout + rb2 * 524288;
    if (lane == 17) a = (const char*)psk;
    asm volatile("global_load_dword %0, %1, off sc1" : "=v"(tlb) : "v"(a) : "memory");
  }
  if (wave == 1) {
    u64 pc;
    asm volatile("s_getpc_b64 %0" : "=s"(pc));
    u32 pf;
    asm volatile("buffer_load_dword %0, %1, %2, 0 offen\n\ts_waitcnt vmcnt(0)" : "=&v"(pf) : "v"(((j & 3) * 64 + lane) * 128), "s"(mkrsrc((const void*)(pc & ~127ull), CODE_PF)) : "memory");
  }
  const int mh = wave & 1, hh = wave >> 1;
  const int r16 = lane & 15, g = lane >> 4, q4 = r16 >> 2, p4 = lane & 3;
  f32x4 acc[4][6];
  {
    const u32 xo = (64 * mh + r16) * 128 + ((g ^ (r16 >> 1)) * 16);
    const int f = ((q4 >> 1) & 1) | ((g & 1) << 1);
    const u32 wo = 16384 + (8 * g + q4) * 128 + (((4 * hh + (p4 >> 1)) ^ (f << 1)) * 16) + 8 * (p4 & 1);
    const u32 bA[4] = {opaque(xo), opaque(xo ^ 64), opaque(wo), opaque(wo ^ 32)};
    const u32 bB[4] = {opaque(xo + 2 * SLOT), opaque((xo ^ 64) + 2 * SLOT), opaque(wo + 2 * SLOT), opaque((wo ^ 32) + 2 * SLOT)};
#pragma unroll
    for (int a = 0; a < 4; ++a)
#pragma unroll
      for (int b = 0; b < 6; ++b) { const float i0 = b < 4 ? EPS : 0.f; acc[a][b] = f32x4{i0, i0, i0, i0}; PIN(acc[a][b]); }
    Frags F0, F1;
    if (TLBW && wave == 2) waitv<1>(); else waitv<0>();
    GET_TAG(); BAR();
    STAMP(1);
    ldfr(F0, bA, SLOT, 0);
#define STAGE(B0, O0, B1, O1) \
      ldfr(F1, B0, O0, 1); mm(acc, F0); interleave<16, 24>(); SCHED(); \
      waitl(); BAR(); \
      ldfr(F0, B1, O1, 0); mm(acc, F1); interleave<16, 24>(); SCHED();
#pragma unroll 1
    for (int s = 0; s < 5; ++s) { STAGE(bA, SLOT, bB, 0) STAGE(bB, 0, bA, 0) STAGE(bA, 0, bA, SLOT) }
    ldfr(F1, bA, SLOT, 1); mm(acc, F0); interleave<16, 24>(); SCHED();
    mm(acc, F1);
  }
  STAMP(2);
  if (TLBW) asm volatile("s_waitcnt vmcnt(0)" : "+v"(tlb) :: "memory");
  {
    f32x4 kvp[2][2], zp[2];
    const f32x4 zero = {0, 0, 0, 0};
    const u32x4 ones = {0x3f803f80u, 0x3f803f80u, 0x3f803f80u, 0x3f803f80u};
#pragma unroll
    for (int u = 0; u < 2; ++u) {
      u32x4 pkf[2], vf[2];
#pragma unroll
      for (int t = 0; t < 2; ++t) { pkf[t] = pack_phi(acc[2 * u][2 + t], acc[2 * u + 1][2 + t]); vf[t] = pack8(acc[2 * u][4 + t], acc[2 * u + 1][4 + t]); }
      const u32x4 vk = mh ? vf[1] : vf[0], vs = mh ? vf[0] : vf[1];
      const u32x4 zk = mh ? pkf[1] : pkf[0], zs = mh ? pkf[0] : pkf[1];
#pragma unroll
      for (int dt = 0; dt < 2; ++dt) kvp[dt][1] = mfma(pkf[dt], vs, u ? kvp[dt][1] : zero);
      zp[1] = mfma(zs, ones, u ? zp[1] : zero);
#pragma unroll
      for (int dt = 0; dt < 2; ++dt) kvp[dt][0] = mfma(pkf[dt], vk, u ? kvp[dt][0] : zero);
      zp[0] = mfma(zk, ones, u ? zp[0] : zero);
    }
    const u32 mine = L_X1 + wave * 2304, theirs = L_X1 + (wave ^ 1) * 2304;
#pragma unroll
    for (int dt = 0; dt < 2; ++dt) *L<f32x4>(mine + dt * 1024 + lane * 16) = kvp[dt][1];
    if (r16 == 0) *L<f32x4>(mine + 2048 + g * 16) = zp[1];
    lds_set(W_X1 + wave, 1);
    f32x4 o0, o1, oz;
#pragma unroll 1
    for (u32 spins = 0; spins < (SPIN_CAP << 3); ++spins) {
      u32 fl;
      asm volatile("ds_read_b32 %0, %4\n\tds_read_b128 %1, %5\n\tds_read_b128 %2, %5 offset:1024\n\tds_read_b128 %3, %6\n\ts_waitcnt lgkmcnt(0)"
                   : "=&v"(fl), "=&v"(o0), "=&v"(o1), "=&v"(oz)
                   : "v"(L_FLAG + 4 * (W_X1 + (wave ^ 1))), "v"(theirs + lane * 16), "v"(theirs + 2048 + g * 16) : "memory");
      if (__builtin_amdgcn_readfirstlane(fl)) break;
    }
    const u32 head = cb * 2 + hh;
    st128_sc(pack8(kvp[0][0] + o0, kvp[1][0] + o1), ws, WS_PART + (head * 2 + mh) * 16384 + rb * 1024 + lane * 16);
    st128_sc(__builtin_bit_cast(u32x4, zp[0] + oz), ws, r16 == 0 ? WS_Z + head * 2048 + rb * 128 + mh * 64 + g * 16 : 0x40000000u);
  }
  lds_inc(W_PST);
  SCHED();
  STAMP(10);
  u32x4 pq[4];
#pragma unroll
  for (int mt = 0; mt < 1; ++mt) { pq[mt] = pack_phi(acc[mt][0], acc[mt][1]); PIN(pq[mt]); }
  SCHED();
  const bool last1 = drain_last(W_CNT1, 4);
  if (last1) post(ws, WS_F1 + (cb * 16 + lane) * 256 + rb * 8, tag, lane < 16);
  STAMP(3);
  SCHED();
#pragma unroll
  for (int mt = 1; mt < 4; ++mt) { pq[mt] = pack_phi(acc[mt][0], acc[mt][1]); PIN(pq[mt]); }
  u32x4 sel[2];
  {
    const u32 one = (g == q4) ? ((p4 & 1) ? 0x3f800000u : 0x3f80u) : 0u, a = (p4 & 2) ? 0u : one, b = (p4 & 2) ? one : 0u;
    sel[0] = u32x4{a, b, 0, 0}; sel[1] = u32x4{0, 0, a, b};
    PIN(sel[0]); PIN(sel[1]);
  }
  SCHED();
  if (last1) { await<0>(ws, WS_F1 + (cq * 16 + rb) * 256 + r16 * 8, tag); lds_set(W_GOT1, 1); }
  else lds_await<1>(W_GOT1, 1);
  STAMP(4);
  {
    const u32 head = cq * 2 + hh;
    u32x4 pr[16], zr[2];
#pragma unroll
    for (int n = 0; n < 2; ++n) zr[n] = ld128_sc(ws, lane * 16 + n * 1024, WS_Z + head * 2048);
#pragma unroll
    for (int r = 0; r < 16; ++r) pr[r] = ld128_sc(ws, lane * 16 + (r & 1) * 1024, WS_PART + (head * 2 + mh) * 16384 + ((r >> 1) ^ (rb & 7)) * 2048);
    SCHED();
    f32x4 zs = __builtin_bit_cast(f32x4, zr[0]) + __builtin_bit_cast(f32x4, zr[1]);
#pragma unroll
    for (int r = 0; r < 4; ++r) zs[r] = sum_8_16_32(zs[r]);
    const u32 zb = L_X2 + 4096 + wave * 64;
    if (lane < 8) *L<u32x2>(zb + (lane & 3) * 16 + (lane >> 2) * 8) = u32x2{pk2(zs[0], zs[1]), pk2(zs[2], zs[3])};
    FENCE();
    const u32x4 zf = *L<u32x4>(zb + g * 16);
    const f32x4 zero = {0, 0, 0, 0}, eps4 = {EPS, EPS, EPS, EPS};
    float inv[4];
#pragma unroll
    for (int mt = 0; mt < 4; ++mt) { inv[mt] = __builtin_amdgcn_rcpf(mfma(zf, pq[mt], eps4)[0]); PIN(inv[mt]); }
    SCHED();
    f32x4 sum[2] = {zero, zero};
#pragma unroll
    for (int r = 0; r < 16; ++r) { sum[0] = mfma(sel[0], pr[r], sum[0]); sum[1] = mfma(sel[1], pr[r], sum[1]); }
    const u32x4 kvm = pack8(sum[0], sum[1]);
    *L<u32x4>(L_X2 + wave * 1024 + lane * 16) = kvm;
    lds_set(W_X2 + wave, 1);
    STAMP(11);
    u32x2 om[4];
#pragma unroll
    for (int mt = 0; mt < 4; ++mt) { const f32x4 n = mfma(kvm, pq[mt], zero) * inv[mt]; om[mt] = u32x2{pk2(n[0], n[1]), pk2(n[2], n[3])}; PIN(om[mt]); }
    u32x4 kvo;
#pragma unroll 1
    for (u32 spins = 0; spins < (SPIN_CAP << 3); ++spins) {
      u32 fl;
      asm volatile("ds_read_b32 %0, %2\n\tds_read_b128 %1, %3\n\ts_waitcnt lgkmcnt(0)" : "=&v"(fl), "=&v"(kvo)
                   : "v"(L_FLAG + 4 * (W_X2 + (wave ^ 1))), "v"(L_X2 + (wave ^ 1) * 1024 + lane * 16) : "memory");
      if (__builtin_amdgcn_readfirstlane(fl)) break;
    }
    const u32 oo = WS_O + rb * 262144 + mh * 131072 + head * 2048 + lane * 16;
#pragma unroll
    for (int mt = 0; mt < 4; ++mt) {
      const f32x4 n = mfma(kvo, pq[mt], zero) * inv[mt];
      const u32x2 ot = {pk2(n[0], n[1]), pk2(n[2], n[3])};
      st128_sc(mh ? u32x4{ot.x, ot.y, om[mt].x, om[mt].y} : u32x4{om[mt].x, om[mt].y, ot.x, ot.y}, ws, oo + (mt >> 1) * 65536 + (mt & 1) * 1024);
    }
  }
  STAMP(5);
  u32 wb[4];
  {
    const int fw = (2 * g + (q4 >> 1)) & 3;
#pragma unroll
    for (int t = 0; t < 4; ++t) wb[t] = opaque((4 * g + q4) * 128 + ((t ^ fw) * 32) + p4 * 8);
  }
  f32x4 oc[2][4];
#pragma unroll
  for (int m = 0; m < 2; ++m)
#pragma unroll
    for (int t = 0; t < 4; ++t) { oc[m][t] = f32x4{0, 0, 0, 0}; PIN(oc[m][t]); }
  if (drain_last(W_CNT2, 4)) {
    post(ws, WS_F2 + (rb * 16 + lane) * 256 + cb * 8, tag, lane < 16);
    STAMP(6);
    await<0>(ws, WS_F2 + (rb2 * 16 + cb2) * 256 + r16 * 8, tag);
    lds_set(W_GOT2, 1);
    if (bid == 0) post(ws, WS_EPOCH, tag, lane == 0);
  } else { STAMP(6); lds_await<1>(W_GOT2, 1); }
  STAMP(7);
  {
    u32x4 of[WIN][2];
    const u32 ob = WS_O + rb2 * 262144 + wave * 65536, vo = opaque(lane * 16);
    auto ldo = [&](int st, int m) __attribute__((always_inline)) { of[st % WIN][m] = ld128_sc(ws, vo + ((st & 1) * 2 + m) * 1024, ob + (((st & ~1) ^ hx) * 2048)); };
#pragma unroll
    for (int st = 0; st < WIN; ++st) { ldo(st, 0); ldo(st, 1); SCHED(); }
    lds_await<0>(W_WO1, 4);
    const bool wo2 = lds_get(W_WO2);
    auto ldw = [&](u32x4 (&w)[4], int st) __attribute__((always_inline)) {
      const u32 off = st < 24 ? (st & 15) * 4096 : ((st - 24) >> 1) * CH_STRIDE + (st & 1) * 4096;
#pragma unroll
      for (int t = 0; t < 4; ++t) w[t] = tr_frag(wb[t] + off, 2048);
    };
    u32x4 wf[2][4];
    ldw(wf[0], 0);
#pragma unroll
    for (int st = 0; st < 32; ++st) {
      if (st == 15 || st == 23) {
#pragma unroll
        for (int t = 0; t < 4; ++t) wb[t] = opaque(wb[t] + (st == 15 ? 65536 : L_CH - 65536));
        if (st == 23 && !wo2) lds_await<0>(W_WO2, 1);
      }
      if (st < 31) ldw(wf[(st + 1) & 1], st + 1);
#pragma unroll
      for (int m = 0; m < 2; ++m)
#pragma unroll
        for (int t = 0; t < 4; ++t) oc[m][t] = mfma(wf[st & 1][t], of[st % WIN][m], oc[m][t]);
      if (st + WIN < 32) { ldo(st + WIN, 0); ldo(st + WIN, 1); }
#pragma unroll
      for (int m = 0; m < 2; ++m) {
        __builtin_amdgcn_sched_group_barrier(0x8, 1, 0); __builtin_amdgcn_sched_group_barrier(0x100, 2, 0);
        __builtin_amdgcn_sched_group_barrier(0x8, 1, 0); __builtin_amdgcn_sched_group_barrier(0x100, 1, 0);
        __builtin_amdgcn_sched_group_barrier(0x8, 1, 0); __builtin_amdgcn_sched_group_barrier(0x100, 1, 0);
        __builtin_amdgcn_sched_group_barrier(0x8, 1, 0); __builtin_amdgcn_sched_group_barrier(0x20, 1, 0);
      }
      SCHED();
    }
    STAMP(8);
    const rsrc_t ro = mkrsrc(pout, 2048 * 2048 * 2);
    const u32 sc = L_X1 + (wave ^ 1) * 2304;
    const u32 sw_ = opaque(sc + r16 * 144 + g * 8), sr_ = opaque(sc + r8 * 144 + c8 * 16);
    const u32 oo = (rb2 * 128 + wave * 32 + r8) * 4096 + 2048 + cb2 * 128 + c8 * 16;
#pragma unroll
    for (int m = 0; m < 2; ++m) {
#pragma unroll
      for (int t = 0; t < 4; ++t) *L<u32x2>(sw_ + t * 32) = u32x2{pk2(oc[m][t][0], oc[m][t][1]), pk2(oc[m][t][2], oc[m][t][3])};
      FENCE();
      const u32x4 r0 = *L<u32x4>(sr_), r1 = *L<u32x4>(sr_ + 1152);
      FENCE();
      st128_sc(r0, ro, oo + m * 65536); st128_sc(r1, ro, oo + m * 65536 + 32768);
    }
  }
  STAMP(9);
  STAMP_OUT();
}

extern "C" void kernel_launch(void* const* d_in, const int* in_sizes, int n_in, void* d_out, int out_size, void* d_ws, size_t ws_size,
                              hipStream_t stream) {
  Params p;
  p.q = (const u16*)d_in[0]; p.k = (const u16*)d_in[1]; p.v = (const u16*)d_in[2]; p.sk = (const u16*)d_in[3];
  p.h = (const u16*)d_in[4]; p.dd = (const u16*)d_in[5]; p.x = (const u16*)d_in[6]; p.wqkv = (const u16*)d_in[7];
  p.wo = (const u16*)d_in[8]; p.out = (u16*)d_out; p.ws = (char*)d_ws;
  const u64 kord[32] = {
      0xc8b49ae7df236150ull, 0x8bc9d5ae264f3701ull, 0x8cbde974af530612ull, 0xb8c7af9d6e514023ull,
      0xb9c8eda5f7026134ull, 0xb8c0da1e7f694235ull, 0x8cdabfe925714306ull, 0x8ba9c31e0fd42657ull,
      0xcb98e74a321d506full, 0x89cbade7542361f0ull, 0xcb89e57d43a60f21ull, 0xb8a9cde7f4605132ull,
      0xbce9a7d80f651243ull, 0xbca87e9f1d056324ull, 0xcb8a9df7e1026345ull, 0xcb89ea1d0732f546ull,
      0x89ca7b436d1520feull, 0x89cdab5720614e3full, 0x89bced7a634152f0ull, 0x8ab97ed56c42f031ull,
      0xb987acd6e5f10432ull, 0x8cba7d961ef50243ull, 0x8cd9fa76b21e0534ull, 0x8a9be1cd67f02345ull,
      0xc89ba73246f15e0dull, 0x98abc54d26713f0eull, 0x8ca9bde47356012full, 0xba9876cf5d4e3120ull,
      0x9abce875d602f431ull, 0xb8adc7e95610f432ull, 0x8adc96b7e25f0143ull, 0x8baec9f71653d024ull};
  for (int i = 0; i < 32; ++i) p.kord[i] = kord[i];
  (void)hipFuncSetAttribute((const void*)fused_h3_linattn, hipFuncAttributeMaxDynamicSharedMemorySize, LDS_BYTES);
  fused_h3_linattn<<<256, 512, LDS_BYTES, stream>>>(p);
}
```

```cpp
#define PCOND (wave<4)
#define P1 1
#include <hip/hip_runtime.h>
#include <hip/hip_bf16.h>

typedef unsigned short u16;
typedef unsigned int u32;
typedef unsigned long long u64;
typedef float f32x2 __attribute__((ext_vector_type(2)));
typedef float f32x4 __attribute__((ext_vector_type(4)));
typedef u32 u32x2 __attribute__((ext_vector_type(2)));
typedef u32 u32x4 __attribute__((ext_vector_type(4)));
typedef short s16x4 __attribute__((ext_vector_type(4)));
typedef __bf16 bf16x2 __attribute__((ext_vector_type(2)));
typedef __bf16 bf16x8 __attribute__((ext_vector_type(8)));
typedef __amdgpu_buffer_rsrc_t rsrc_t;
#define LAS __attribute__((address_space(3)))
#define DI __device__ __forceinline__
#if defined(__HIP_DEVICE_COMPILE__)
#define NOPK __attribute__((target("no-packed-fp32-ops")))
#else
#define NOPK
#endif

#define CODE_PF 0
#define KTAB 1
#define S0 0
#define NSG 3
#define XL 0
#define KSL 8
#define VCH 3
#define QP 2
#define A2D 24
#define STG 0
#define XB 0
#define TLBW 0
#define WIN 5
#define STAMPS 0

constexpr u32 WS_EPOCH = 0;
constexpr u32 WS_F1 = 65536;
constexpr u32 WS_F2 = WS_F1 + 65536;
constexpr u32 WS_FA = WS_F2 + 65536;
constexpr u32 WS_FY = WS_FA + 131072;
constexpr u32 WS_Z = 512u << 10;
constexpr u32 WS_PART = 1u << 20;
constexpr u32 WS_O = 2u << 20;
constexpr u32 WS_KVT = 6u << 20;
constexpr u32 WS_YT = 10u << 20;
constexpr u32 WS_BYTES = 14u << 20;
constexpr u32 WS_DBG = 32u << 20;

constexpr u32 SLOT = 40960;
constexpr u32 L_X1 = 98304;
constexpr u32 L_X2 = L_X1 + 4 * 2304;
constexpr u32 CX_STRIDE = 4640, CH_STRIDE = 8320, C_E = 4160;
constexpr u32 L_FLAG = 163840 - 128;
constexpr u32 L_CH = L_FLAG - 4 * CH_STRIDE;
constexpr u32 L_CX = L_CH - 4 * CX_STRIDE;
constexpr u32 LDS_BYTES = 163840;
static_assert(L_X2 + 4096 + 256 <= L_CX && L_CH >= 3 * SLOT, "lds map");
enum { W_TAG = 0, W_WO1 = 2, W_WO2 = 3, W_X1 = 4, W_X2 = 8, W_CNT1 = 12, W_CNT2, W_CNTY, W_GOT1, W_GOT2, W_STAGED, W_PST };

constexpr int AUX_SC1 = 16;
constexpr u32 SPIN_CAP = 1u << 16;

DI float f_(u32 x) { return __builtin_bit_cast(float, x); }
DI u32 u_(float x) { return __builtin_bit_cast(u32, x); }
DI f32x4 mfma(u32x4 a, u32x4 b, f32x4 c) {
  return __builtin_amdgcn_mfma_f32_16x16x32_bf16(__builtin_bit_cast(bf16x8, a), __builtin_bit_cast(bf16x8, b), c, 0, 0, 0);
}
DI u32 pk2(float lo, float hi) { return __builtin_bit_cast(u32, __builtin_convertvector(f32x2{lo, hi}, bf16x2)); }
DI float blo(u32 x) { return f_(x << 16); }
DI float bhi(u32 x) { return f_(x & 0xffff0000u); }
template <int N> DI void waitv() { asm volatile("s_waitcnt vmcnt(%0)" ::"n"(N) : "memory"); }
DI void waitl() { asm volatile("" ::: "memory"); __builtin_amdgcn_s_waitcnt(0xC07F); asm volatile("" ::: "memory"); }
#define SCHED() __builtin_amdgcn_sched_barrier(0)
#define FENCE() asm volatile("" ::: "memory")
#define BAR() do { FENCE(); SCHED(); __builtin_amdgcn_s_barrier(); SCHED(); FENCE(); } while (0)
#define PIN(x) asm volatile("" : "+v"(x))
template <class T> DI LAS T* L(u32 a) { return (LAS T*)(size_t)a; }
DI u32 opaque(u32 a) { PIN(a); return a; }
DI rsrc_t mkrsrc(const void* p, u32 bytes) { return __builtin_amdgcn_make_buffer_rsrc((void*)p, 0, bytes, 0x20000); }
template <int LI, int SI> DI void dma(rsrc_t r, u32 lds, u32 voff, u32 soff) {
  u32 t;
  asm volatile("s_add_u32 m0, %1, %2\n\ts_add_u32 %0, %5, %6\n\tbuffer_load_dwordx4 %3, %4, %0 offen lds"
               : "=&s"(t) : "s"(lds), "i"(LI), "v"(voff), "s"(r), "s"(soff), "i"(SI) : "memory", "scc");
}
DI u32x4 ld128(rsrc_t r, u32 voff, u32 soff) { return __builtin_amdgcn_raw_buffer_load_b128(r, voff, soff, 0); }
DI u32x4 ld128_sc(rsrc_t r, u32 voff, u32 soff) { return __builtin_amdgcn_raw_buffer_load_b128(r, voff, soff, AUX_SC1); }
DI void st128_sc(u32x4 v, rsrc_t r, u32 voff) { __builtin_amdgcn_raw_buffer_store_b128(v, r, voff, 0, AUX_SC1); }

DI u32 lds_get(int word) { return __builtin_amdgcn_readfirstlane(__hip_atomic_load(L<u32>(L_FLAG + 4 * word), __ATOMIC_RELAXED, __HIP_MEMORY_SCOPE_WORKGROUP)); }
DI void lds_set(int word, u32 v) {
  asm volatile("s_mov_b64 exec, 1\n\tds_write_b32 %0, %1\n\ts_mov_b64 exec, -1" :: "v"(L_FLAG + 4 * word), "v"(v) : "memory");
}
DI void lds_inc(int word) {
  asm volatile("s_mov_b64 exec, 1\n\tds_add_u32 %0, %1\n\ts_mov_b64 exec, -1" :: "v"(L_FLAG + 4 * word), "v"(1u) : "memory");
}
template <int SL> DI void lds_await(int word, u32 want) {
#pragma unroll 1
  for (u32 spins = 0; spins < (SPIN_CAP << 3); ++spins) {
    if (lds_get(word) >= want) break;
    if (SL) __builtin_amdgcn_s_sleep(SL);
  }
  FENCE();
}
DI bool drain_last(int word, u32 n) {
  u32 old;
  asm volatile("s_waitcnt vmcnt(0)\n\ts_mov_b64 exec, 1\n\tds_add_rtn_u32 %0, %1, %2\n\ts_mov_b64 exec, -1\n\ts_waitcnt lgkmcnt(0)"
               : "=&v"(old) : "v"(L_FLAG + 4 * word), "v"(1u) : "memory");
  return __builtin_amdgcn_readfirstlane(old) == n - 1;
}
DI void post(rsrc_t ws, u32 voff, u32x2 tag, bool on) { __builtin_amdgcn_raw_buffer_store_b64(tag, ws, on ? voff : 0x40000000u, 0, AUX_SC1); }
template <int SL> DI void await(rsrc_t ws, u32 voff, u32x2 tag) {
#pragma unroll 1
  for (u32 spins = 0; spins < SPIN_CAP; ++spins) {
    u32x2 f;
    asm volatile("buffer_load_dwordx2 %0, %1, %2, 0 offen sc1\n\ts_waitcnt vmcnt(0)" : "=&v"(f) : "v"(voff), "s"(ws) : "memory");
    if (__builtin_amdgcn_ballot_w64(f.x != tag.x || f.y != tag.y) == 0) break;
    if (SL) __builtin_amdgcn_s_sleep(SL);
  }
  FENCE();
}

#define STAMPINS "s_memrealtime"
#define STAMP(i) do { if ((i)==0 || (PCOND && (i)==P1)) { u64 t_; asm volatile("s_memrealtime %0\n\ts_waitcnt lgkmcnt(0)" : "=s"(t_) :: "memory"); if((i)==0) { ts0=t_; tsb=t_; } else tsb=t_; } } while(0)
#define STAMP_OUT() do { if (PCOND) { const u64 until = ts0 + 10000 + 10*(tsb-ts0); for(int z=0;z<100000;++z){ u64 t_; asm volatile("s_memrealtime %0\n\ts_waitcnt lgkmcnt(0)" : "=s"(t_) :: "memory"); if(t_>=until) break; __builtin_amdgcn_s_sleep(2);} } } while(0)

struct Params {
  const u16 *x, *wqkv;
  char* ws;
  const u16 *wo, *q, *k, *v, *sk, *h, *dd;
  u16* out;
  u64 kord[32];
};

struct Frags { u32x4 x[4]; u32x4 w[6]; };
DI u32x4 tr_frag(u32 a, u32 d) {
  s16x4 lo = __builtin_amdgcn_ds_read_tr16_b64_v4i16(L<s16x4>(a));
  s16x4 hi = __builtin_amdgcn_ds_read_tr16_b64_v4i16(L<s16x4>(a + d));
  return __builtin_bit_cast(u32x4, __builtin_shufflevector(lo, hi, 0, 1, 2, 3, 4, 5, 6, 7));
}
DI void ldfr(Frags& f, const u32 (&b)[4], u32 off, int ks) {
#pragma unroll
  for (int mt = 0; mt < 4; ++mt) f.x[mt] = *L<u32x4>(b[ks] + off + mt * 2048);
#pragma unroll
  for (int s_ = 1; s_ < 4; ++s_)
#pragma unroll
    for (int t = 0; t < 2; ++t) { const int sg = s_ % 3; f.w[sg * 2 + t] = tr_frag(b[2 + t] + off + sg * 8192 + ks * 4096, 512); }
}
DI void mm(f32x4 (&acc)[4][6], const Frags& f) {
#pragma unroll
  for (int i = 0; i < 24; ++i) {
    const int n_ = i >> 2, n = n_ < 4 ? n_ + 2 : n_ - 4, mt = (n_ & 1) ? 3 - (i & 3) : (i & 3);
    if (n < 2) acc[mt][n] = mfma(f.w[n], f.x[mt], acc[mt][n]);
    else acc[mt][n] = mfma(f.x[mt], f.w[n], acc[mt][n]);
  }
}
template <int NR, int NM> DI void interleave() {
#pragma unroll
  for (int i = 0; i < NR; ++i) { __builtin_amdgcn_sched_group_barrier(0x8, 1, 0); __builtin_amdgcn_sched_group_barrier(0x100, 1, 0); }
  if (NM > NR) __builtin_amdgcn_sched_group_barrier(0x8, NM - NR, 0);
}
constexpr float EPS = 1e-6f;
DI float phi(float t) {
  const float e = __builtin_amdgcn_fmed3f(__builtin_amdgcn_exp2f(__builtin_fmaf(t, 1.44269504088896f, -EPS * 1.44269504088896f)), 0.f, 1.f);
  return e + __builtin_bit_cast(float, __builtin_elementwise_max(__builtin_bit_cast(int, t), __builtin_bit_cast(int, EPS)));
}
DI u32x4 pack_phi(f32x4 a, f32x4 b) { return u32x4{pk2(phi(a[0]), phi(a[1])), pk2(phi(a[2]), phi(a[3])), pk2(phi(b[0]), phi(b[1])), pk2(phi(b[2]), phi(b[3]))}; }
DI u32x4 pack8(f32x4 a, f32x4 b) { return u32x4{pk2(a[0], a[1]), pk2(a[2], a[3]), pk2(b[0], b[1]), pk2(b[2], b[3])}; }
DI float sum_8_16_32(float v) {
  v += f_(__builtin_amdgcn_update_dpp(0, u_(v), 0x128, 0xf, 0xf, false));
  auto a = __builtin_amdgcn_permlane16_swap(u_(v), u_(v), false, false);
  v = f_(a[0]) + f_(a[1]);
  auto b = __builtin_amdgcn_permlane32_swap(u_(v), u_(v), false, false);
  return f_(b[0]) + f_(b[1]);
}

DI void mfma_ip(f32x4& c, u32x4 a, u32x4 b) { asm volatile("v_mfma_f32_16x16x32_bf16 %0, %1, %2, %0" : "+v"(c) : "v"(a), "v"(b)); }
struct CFr { u32x2 a[8][2]; u32x4 b[8]; };
DI void ldconv1(CFr& f, const u32 (&hb)[4], u32 xb, u32 off, int k) {
  const int i = k / 3, e = k % 3;
  if (e == 0) f.b[i] = *L<u32x4>(xb + off + (7 - i) * 512);
  else { LAS u32* hp = L<u32>(hb[i >> 1] + off + (i & 1) * 512 + (e - 1) * 8); f.a[i][e - 1] = u32x2{hp[0], hp[1]}; }
}
DI void mmconv1(f32x4 (&acc)[8], const CFr& f, int k) {
  const int n = (k >= 1) + (k >= 3) + (k >= 6) + (k >= 10) + (k >= 15) + (k >= 21) + (k >= 28);
  k -= n * (n + 1) / 2;
  mfma_ip(acc[n], u32x4{f.a[k][0].x, f.a[k][0].y, f.a[k][1].x, f.a[k][1].y}, f.b[n - k]);
}

__global__ NOPK __launch_bounds__(512, 2) void fused_h3_linattn(Params p) {
  const int bid = __builtin_amdgcn_workgroup_id_x(), xcd = bid & 7, j = bid >> 3;
  const int rb = (xcd & 1) * 8 + (j & 7), cb = (xcd >> 1) * 4 + (j >> 3);
  const int cq = (cb & 12) | ((cb + 1) & 3);
  const int rb2 = (xcd >> 1) * 4 + (j & 3), cb2 = (xcd & 1) * 8 + (j >> 2);
  const int hx = (((j >> 2) + 2 * (j & 3)) & 7) * 4;
  const int wave = __builtin_amdgcn_readfirstlane(__builtin_amdgcn_workitem_id_x() >> 6), lane = __builtin_amdgcn_workitem_id_x() & 63;
  const int r8 = lane >> 3, c8 = lane & 7;
  u64 ts0=0,tsb=0;
  STAMP(0);
  const u16 *pwo = p.wo, *pq_ = p.q, *pk_ = p.k, *pv_ = p.v, *psk = p.sk, *ph_ = p.h, *pdd = p.dd;
  u16* pout = p.out;
  asm volatile("" : "+s"(pwo), "+s"(pq_), "+s"(pk_), "+s"(pv_), "+s"(psk), "+s"(ph_), "+s"(pdd), "+s"(pout));
  const rsrc_t ws = mkrsrc(p.ws, WS_BYTES), rx = mkrsrc(p.x, 2048 * 1024 * 2), rw = mkrsrc(p.wqkv, 1024 * 3072 * 2);
  const int rot = rb - cb;
  const u32 qd = (cq - cb) * 128;
  u32 vx[2], vw[2];
#pragma unroll
  for (int e = 0; e < 2; ++e) {
    vx[e] = r8 * 2048 + ((c8 ^ (4 * e + (r8 >> 1))) * 16);
    vw[e] = r8 * 6144 + ((c8 ^ ((((r8 >> 1) & 1) | (e << 1)) << 1)) * 16);
  }
  {
    const u32 s = rot & 15, sx = (rb * 128 + wave * 16) * 2048 + s * 128, sw = (s * 64 + wave * 8) * 6144 + cb * 128;
    const u32 vwp = vw[0] ^ ((wave & 1) * 64);
    dma<SLOT, 0>(rx, wave * 2048, vx[0], sx);
    dma<SLOT + 16384 + 8192, 2048>(rw, wave * 1024, vwp, sw);
    dma<SLOT + 16384, 0>(rw, wave * 1024, vwp, sw + qd);
    dma<SLOT + 16384 + 16384, 4096>(rw, wave * 1024, vwp, sw);
    dma<SLOT + 1024, 16384>(rx, wave * 2048, vx[1], sx);
  }
  STAMP(24);
  u64 ep0, ep, kord0;
  asm volatile("s_load_dwordx2 %0, %2, 0\n\ts_load_dwordx2 %1, %3, %4" : "=&s"(ep0), "=&s"(kord0)
               : "s"(p.ws), "s"(__builtin_amdgcn_kernarg_segment_ptr()), "s"((u32)offsetof(Params, kord) + j * 8) : "memory");
#define GET_TAG() do { asm volatile("s_waitcnt lgkmcnt(0)\n\ts_mov_b64 %0, %1" : "=s"(ep) : "s"(ep0), "s"(kord0) : "memory"); ep = ep * 6364136223846793005ull + 1442695040888963407ull; } while (0)
#define tag (u32x2{(u32)ep, (u32)(ep >> 32)})

  if (wave >= 4) {
    const int hw = wave - 4, aw = hw & 1, ca = (cb + aw) & 15;
    const rsrc_t rwo = mkrsrc(pwo, XL >= 4 ? 0 : 1024 * 1024 * 2);
    const u32 vwo = r8 * 2048 + ((c8 ^ (((r8 >> 1) & 3) << 1)) * 16);
    const int R2 = rb * 128 + r8 * 16 + aw * 8;
    const rsrc_t rq = mkrsrc(pq_, hw >= 2 && XL < 1 ? 2048 * 1024 * 2 : 0);
    u32x4 qq[8];
    const int chl = (rb >> 3) * 32 + hw * 8 + (rb & 7);
    const u32 cx = L_CX + hw * CX_STRIDE, chr = L_CH + hw * CH_STRIDE;
    if (hw < 2) {
      const u32 sx0 = (rb * 128 + hw * 64) * 2048, sw0 = (hw * 32) * 6144 + cb * 128;
      const int kbase = (xcd & 1) * 8 - (xcd >> 1) * 4;
      u64 kord = (((j & 16) ? 0x2433220043433f3full : 0x442432f650332105ull) >> (4 * (j & 15))) << 4;
      auto issue = [&](int s_, u32 base, int half) __attribute__((always_inline)) {
        const u32 s = (KTAB ? (u32)(kord >> (4 * s_)) + kbase : s_ + rot) & 15;
        const u32 sx = sx0 + s * 128 + half * 65536, sw = sw0 + s * (64 * 6144) + half * 98304, sq = sw + qd;
        const u32 lx = base + hw * 8192 + half * 4096, lw = base + 16384 + hw * 4096 + half * 2048;
        dma<0, 0>(rx, lx, vx[0], sx); dma<1024, 16384>(rx, lx, vx[1], sx);
        dma<8192, 2048>(rw, lw, vw[0], sw); dma<9216, 2048 + 49152>(rw, lw, vw[1], sw);
        dma<0, 0>(rw, lw, vw[0], sq); dma<1024, 49152>(rw, lw, vw[1], sq);
        dma<16384, 4096>(rw, lw, vw[0], sw); dma<17408, 4096 + 49152>(rw, lw, vw[1], sw);
        dma<2048, 32768>(rx, lx, vx[0], sx); dma<3072, 49152>(rx, lx, vx[1], sx);
      };
      issue(1, 2 * SLOT, 0);
      waitv<10>(); GET_TAG();
      asm volatile("s_mov_b64 %0, %1" : "=s"(kord) : "s"(kord0) : "memory");
      BAR();
      issue(1, 2 * SLOT, 1);
      u32 base = 0;
#pragma unroll 1
      for (int s = 2; s < 16; ++s) {
        issue(s, base, 0); issue(s, base, 1);
        base = base == 2 * SLOT ? 0 : base + SLOT;
        waitv<20>(); BAR();
      }
      waitv<0>(); BAR();
      if (QP == 2) {
#pragma unroll
        for (int i = 0; i < 8; ++i) asm volatile("" : "=v"(qq[i]));
      }
    } else {
      const int R0 = rb * 128 + aw * 64 + r8 * 8;
      const rsrc_t rk = mkrsrc(pk_, (XB & 1) ? 0 : 2048 * 1024 * 2), rv = mkrsrc(pv_, (XB & 1) ? 0 : 2048 * 1024 * 2), rs = mkrsrc(psk, 1024 * 4 * 2);
      const u32 vo = R0 * 2048 + ca * 128 + c8 * 16;
      const u32 vh = R0 == 0 ? 0x40000000u : vo - 3 * 2048;
      const u32 kvt_vo = WS_KVT + ca * 262144 + (c8 >> 2) * 131072 + (255 - (R0 >> 3)) * 64 + (c8 & 3) * 16;
      u32x4 kk[11], vv[8], tw[4];
      waitv<0>(); GET_TAG(); BAR();
#pragma unroll
      for (int i = 0; i < 4; ++i) tw[i] = ld128(rs, ca * 512 + c8 * 64 + i * 16, 0);
#define AB(i) do { BAR(); if (STG) STAMP(8 + (i)); } while (0)
      if (lane < 8) *L<u32x4>(L_CH + aw * 2 * CH_STRIDE + (lane >> 1) * C_E + (lane & 1) * 16) = u32x4{0, 0, 0, 0};
      const int sg = S0 + (NSG > 1 ? (j + (j >> 3) + xcd) % NSG : 0);
#pragma unroll 1
      for (int i = 0; i <= sg; ++i) AB(i);
      if (KSL) __builtin_amdgcn_s_sleep(KSL);
#pragma unroll
      for (int i = 0; i < 8; ++i) kk[3 + i] = ld128(rk, vo, i * 2048);
#pragma unroll
      for (int i = 0; i < 3; ++i) kk[i] = ld128(rk, vh, i * 2048);
      AB(sg + 1);
      float wf[8][4], ks[8][8], kf[11];
#pragma unroll
      for (int c = 0; c < 8; ++c) {
        if (!(c & 1)) PIN(tw[c >> 1]);
#pragma unroll
        for (int t = 0; t < 4; ++t) { const u32 d = tw[c >> 1][(c & 1) * 2 + (t >> 1)]; wf[c][t] = (t & 1) ? bhi(d) : blo(d); PIN(wf[c][t]); }
      }
#pragma unroll
      for (int u = 0; u < 24; ++u) {
        if (u % 3 == 0) {
          if (u == 3 * VCH) {
#pragma unroll
            for (int i = 0; i < 8; ++i) vv[i] = ld128(rv, vo, i * 2048);
          }
          AB(sg + 2 + u / 3);
        }
        if (XB & 4) continue;
        if (u < 16) {
          const int c = u >> 1, hf = u & 1;
#pragma unroll
          for (int i = hf ? 7 : 0; i < (hf ? 11 : 7); ++i) { PIN(kk[i]); const u32 d = kk[i][c >> 1]; kf[i] = (c & 1) ? bhi(d) : blo(d); }
#pragma unroll
          for (int i = hf * 4; i < hf * 4 + 4; ++i) {
            if (hf && i < 7) PIN(kf[i]);
            float a = kf[i] * wf[c][3];
            a = __builtin_fmaf(kf[i + 1], wf[c][2], a); a = __builtin_fmaf(kf[i + 2], wf[c][1], a); a = __builtin_fmaf(kf[i + 3], wf[c][0], a);
            ks[c][i] = a; PIN(ks[c][i]);
          }
        } else {
          const int c = u - 16;
          float o[8];
#pragma unroll
          for (int i = 0; i < 8; ++i) { PIN(vv[i]); const u32 d = vv[i][c >> 1]; o[i] = ks[c][i] * ((c & 1) ? bhi(d) : blo(d)); }
          st128_sc(u32x4{pk2(o[7], o[6]), pk2(o[5], o[4]), pk2(o[3], o[2]), pk2(o[1], o[0])}, ws, (XB & 2) ? 0x40000000u : kvt_vo + c * 16384);
        }
      }
      AB(sg + 10);
      const u32 pa = opaque(lane < 16 ? WS_FA + ((ca * 16 + lane) * 2 + (rb < 8)) * 256 + ((rb & 7) * 2 + aw) * 8 : 0x40000000u);
      AB(sg + 11);
      waitv<0>();
      post(ws, pa, tag, true);
#pragma unroll 1
      for (int i = sg + 12; i <= 13; ++i) AB(i);
      u32x4 hh4[2][4], xs[8];
      const u32 fa = WS_FA + ((cb * 16 + rb) * 2 + aw) * 256 + (lane & 15) * 8;
      u32x2 fl;
      asm volatile("buffer_load_dwordx2 %0, %1, %2, 0 offen sc1" : "=&v"(fl) : "v"(fa), "s"(ws) : "memory");
      {
        const rsrc_t rh = mkrsrc(ph_, XL >= 5 ? 0 : 1024 * 2048 * 2);
#pragma unroll
        for (int c = 0; c < 2; ++c)
#pragma unroll
          for (int n = 0; n < 4; ++n) hh4[c][n] = ld128(rh, lane * 16 + n * 1024, (cb * 64 + (rb >> 3) * 32 + (aw * 2 + c) * 8 + (rb & 7)) * 4096);
      }
      BAR();
      if (STG) STAMP(22);
      asm volatile("s_waitcnt vmcnt(8)" : "+v"(fl) :: "memory");
      if (__builtin_amdgcn_ballot_w64(fl.x != tag.x || fl.y != tag.y)) await<4>(ws, fa, tag);
      STAMP(23);
#pragma unroll
      for (int n = 0; n < 8; ++n) xs[n] = ld128_sc(ws, XL >= 5 ? 0x40000000u : lane * 16 + (n & 3) * 1024, WS_KVT + cb * 262144 + rb * 16384 + aw * 8192 + (n >> 2) * 4096);
      if (QP == 2) {
#pragma unroll
        for (int i = 0; i < 8; ++i) qq[i] = ld128(rq, R2 * 2048 + ca * 128 + c8 * 16, i * 2048);
      }
      SCHED();
      if (lane < 60) *L<u32x4>(L_CX + (aw * 2 + (lane & 1)) * CX_STRIDE + 4096 + (lane >> 1) * 16) = u32x4{0, 0, 0, 0};
      const u32 hr = opaque(L_CH + aw * 2 * CH_STRIDE + lane * 16);
#pragma unroll
      for (int c = 0; c < 2; ++c)
#pragma unroll
        for (int n = 0; n < 4; ++n) *L<u32x4>(hr + c * CH_STRIDE + 32 + n * 1024) = hh4[c][n];
      FENCE();
      u32 dp[2][4];
#pragma unroll
      for (int c = 0; c < 2; ++c)
#pragma unroll
        for (int n = 0; n < 4; ++n) dp[c][n] = *L<u32>(hr + c * CH_STRIDE + 28 + n * 1024);
#pragma unroll
      for (int c = 0; c < 2; ++c)
#pragma unroll
        for (int n = 0; n < 4; ++n) {
          const u32x4 d = hh4[c][n];
          *L<u32x4>(hr + c * CH_STRIDE + C_E + 32 + n * 1024) =
              u32x4{__builtin_amdgcn_alignbit(d[0], dp[c][n], 16), __builtin_amdgcn_alignbit(d[1], d[0], 16),
                    __builtin_amdgcn_alignbit(d[2], d[1], 16), __builtin_amdgcn_alignbit(d[3], d[2], 16)};
        }
      const u32 xw = opaque(L_CX + (lane & 3) * CX_STRIDE + (aw * 128 + (lane >> 2)) * 16);
#pragma unroll
      for (int n = 0; n < 8; ++n) *L<u32x4>(xw + n * 256) = xs[n];
      lds_inc(W_STAGED);
    }
    u32 dvw;
    asm volatile("s_load_dword %0, %1, 0\n\ts_waitcnt lgkmcnt(0)" : "=s"(dvw) : "s"(pdd + ((cb * 64 + chl) & ~1)));
    const float dv = blo(dvw >> ((chl & 1) * 16));
    const int b16 = lane & 15, kg = lane >> 4;
    u32 hb[4], xb = opaque(cx + (254 - 224 - 2 * b16 + kg) * 16);
#pragma unroll
    for (int i = 0; i < 4; ++i) hb[i] = opaque(chr + ((b16 & 1) ? 0 : C_E) + 4 + 4 * (b16 >> 1) + 16 * kg + i * 1024);
    f32x4 acc[8];
#pragma unroll
    for (int n = 0; n < 8; ++n) { acc[n] = f32x4{0, 0, 0, 0}; PIN(acc[n]); }
    lds_await<1>(W_STAGED, 2);
    STAMP(6);
    CFr C0, C1;
#pragma unroll
    for (int k = 0; k < 24; ++k) ldconv1(C0, hb, xb, 0, k);
    lds_await<1>(W_PST, 4);
    waitl();
    STAMP(1);
#define RESIDUE(CUR, NXT, OFF, ST) do { \
      const u32 so_ = ((ST) ^ hx) * 65536 + cb2 * 128, ld_ = (ST) * 4096; \
      _Pragma("unroll") for (int k = 0; k < 36; ++k) { \
        mmconv1(acc, CUR, k); \
        if (k % 9 == 8) { if (k == 8) dma<0, 0>(rwo, ld_, vwo, so_); else if (k == 17) dma<1024, 16384>(rwo, ld_, vwo, so_); \
                          else if (k == 26) dma<2048, 32768>(rwo, ld_, vwo, so_); else dma<3072, 49152>(rwo, ld_, vwo, so_); } \
        else if (k - k / 9 < 24) ldconv1(NXT, hb, xb, OFF, k - k / 9); \
        SCHED(); \
      } \
      waitl(); SCHED(); \
    } while (0)
#pragma unroll 1
    for (int it = 0; it < (XL >= 3 ? 0 : 3); ++it) {
      RESIDUE(C0, C1, 64, it * 8 + hw);
      RESIDUE(C1, C0, 128, it * 8 + 4 + hw);
      xb = opaque(xb + 128);
#pragma unroll
      for (int i = 0; i < 4; ++i) hb[i] = opaque(hb[i] + 128);
    }
    u32x2 xv[8];
    const u32 xe = opaque(cx + (252 - 16 * b16 - 4 * kg) * 2), vq = opaque(R2 * 2048 + ca * 128 + c8 * 16);
#pragma unroll
    for (int k = 0; k < 36; ++k) {
      mmconv1(acc, C0, k);
      if (k < 24) ldconv1(C1, hb, xb, 64, k); else if (k < 32) xv[k - 24] = *L<u32x2>(xe + (31 - k) * 512);
      if (QP == 0 && (k & 3) == 3 && k < 32) qq[k >> 2] = ld128(rq, vq, (k >> 2) * 2048);
      SCHED();
    }
    if (QP == 0) waitv<8>(); else waitv<0>();
    waitl();
    lds_inc(W_WO1);
    {
      const u32 so = ((24 + hw * 2) ^ hx) * 65536 + cb2 * 128;
#define WO2(k, e) if (k == 4 * e + 3) dma<e * 1024, e * 16384>(rwo, chr, vwo, so);
#pragma unroll
      for (int k = 0; k < 36; ++k) {
        mmconv1(acc, C1, k);
        WO2(k, 0) WO2(k, 1) WO2(k, 2) WO2(k, 3) WO2(k, 4) WO2(k, 5) WO2(k, 6) WO2(k, 7)
        SCHED();
      }
    }
    asm volatile("s_nop 7" : "+v"(acc[7]));
    SCHED();
    STAMP(2);
    {
      u32x2 y[8];
#pragma unroll
      for (int n = 0; n < 8; ++n)
        y[n] = u32x2{pk2(__builtin_fmaf(bhi(xv[n].y), dv, acc[n][0]), __builtin_fmaf(blo(xv[n].y), dv, acc[n][1])),
                     pk2(__builtin_fmaf(bhi(xv[n].x), dv, acc[n][2]), __builtin_fmaf(blo(xv[n].x), dv, acc[n][3]))};
      const u32 yvo = WS_YT + cb * 262144 + ((kg & 1) * 2 + (b16 >> 3)) * 16384 + chl * 256 + (kg >> 1) * 128 + (b16 & 7) * 16;
#pragma unroll
      for (int n = 0; n < 8; n += 2) {
        auto s0 = __builtin_amdgcn_permlane16_swap(y[n].x, y[n + 1].x, false, false);
        auto s1 = __builtin_amdgcn_permlane16_swap(y[n].y, y[n + 1].y, false, false);
        st128_sc(u32x4{s0[0], s1[0], s0[1], s1[1]}, ws, XL >= 2 ? 0x40000000u : yvo + n * 32768);
      }
    }
    if (drain_last(W_CNTY, 4)) {
      lds_set(W_WO2, 1);
      post(ws, WS_FY + (cb * 32 + lane) * 256 + rb * 8, tag, XL < 2 && lane < 32);
    }
    STAMP(3);
    if (hw >= 2 && XL < 1) {
      if (A2D) { lds_await<8>(W_GOT2, 1); __builtin_amdgcn_s_sleep(A2D); } else __builtin_amdgcn_s_sleep(16);
      if (QP == 1) {
#pragma unroll
        for (int i = 0; i < 8; ++i) qq[i] = ld128(rq, vq, i * 2048);
      }
      await<8>(ws, WS_FY + ((ca * 16 + rb) * 2 + aw) * 256 + (lane & 15) * 8, tag);
      STAMP(4);
      u32x4 yy[8];
      const u32 yo = WS_YT + ca * 262144 + rb * 16384 + r8 * 2048 + aw * 128 + c8 * 16;
#pragma unroll
      for (int c = 0; c < 8; ++c) yy[c] = ld128_sc(ws, yo + c * 256, 0);
      const u32 tw_ = opaque(cx + r8 * 144 + c8 * 16), tr_ = opaque(cx + c8 * 144 + r8 * 16);
#pragma unroll
      for (int hf = 0; hf < 2; ++hf) {
#pragma unroll
        for (int c = 0; c < 4; ++c) *L<u32x4>(tw_ + c * 1152) = yy[hf * 4 + c];
        FENCE();
#pragma unroll
        for (int c = 0; c < 4; ++c) yy[hf * 4 + c] = *L<u32x4>(tr_ + c * 1152);
        FENCE();
      }
      const rsrc_t ro = mkrsrc(pout, 2048 * 2048 * 2);
#pragma unroll
      for (int i = 0; i < 8; ++i) {
        u32x4 o;
#pragma unroll
        for (int d = 0; d < 4; ++d) {
          const u32 y0 = yy[2 * d][i >> 1], y1 = yy[2 * d + 1][i >> 1];
          o[d] = pk2(((i & 1) ? bhi(y0) : blo(y0)) * blo(qq[i][d]), ((i & 1) ? bhi(y1) : blo(y1)) * bhi(qq[i][d]));
        }
        st128_sc(o, ro, (R2 + i) * 4096 + ca * 128 + c8 * 16);
      }
    } else { STAMP(4); }
    STAMP(5);
    STAMP_OUT();
    return;
  }

  __builtin_amdgcn_s_setprio(1);
  if (wave == 0 && lane < 32) *L<u32>(L_FLAG + 4 * lane) = 0;
  u32 tlb = 0;
  if (TLBW && wave == 2) {
    const char* a = p.ws + (lane & 7) * (2u << 20) + 8192 + bid * 128;
    if (lane >= 7) a = (const char*)p.wqkv + (lane >= 10 ? 0 : (lane - 7) * (2u << 20)) + bid * 128;
    if (lane == 10) a = (const char*)pk_ + rb * 262144;
    if (lane == 11) a = (const char*)pv_ + rb * 262144;
    if (lane == 12) a = (const char*)pq_ + rb * 262144;
    if (lane == 13) a = (const char*)ph_ + cb * 262144;
    if (lane == 14) a = (const char*)pwo + bid * 128;
    if (lane == 15) a = (const char*)pout + rb * 524288;
    if (lane == 16) a = (const char*)pout + rb2 * 524288;
    if (lane == 17) a = (const char*)psk;
    asm volatile("global_load_dword %0, %1, off sc1" : "=v"(tlb) : "v"(a) : "memory");
  }
  const int mh = wave & 1, hh = wave >> 1;
  const int r16 = lane & 15, g = lane >> 4, q4 = r16 >> 2, p4 = lane & 3;
  f32x4 acc[4][6];
  {
    const u32 xo = (64 * mh + r16) * 128 + ((g ^ (r16 >> 1)) * 16);
    const int f = ((q4 >> 1) & 1) | ((g & 1) << 1);
    const u32 wo = 16384 + (8 * g + q4) * 128 + (((4 * hh + (p4 >> 1)) ^ (f << 1)) * 16) + 8 * (p4 & 1);
    const u32 bA[4] = {opaque(xo), opaque(xo ^ 64), opaque(wo), opaque(wo ^ 32)};
    const u32 bB[4] = {opaque(xo + 2 * SLOT), opaque((xo ^ 64) + 2 * SLOT), opaque(wo + 2 * SLOT), opaque((wo ^ 32) + 2 * SLOT)};
#pragma unroll
    for (int a = 0; a < 4; ++a)
#pragma unroll
      for (int b = 0; b < 6; ++b) { const float i0 = b < 4 ? EPS : 0.f; acc[a][b] = f32x4{i0, i0, i0, i0}; PIN(acc[a][b]); }
    Frags F0, F1;
    if (TLBW && wave == 2) waitv<1>(); else waitv<0>();
    GET_TAG(); BAR();
    STAMP(1);
    ldfr(F0, bA, SLOT, 0);
#define STAGE(B0, O0, B1, O1) \
      ldfr(F1, B0, O0, 1); mm(acc, F0); interleave<16, 24>(); SCHED(); \
      waitl(); BAR(); \
      ldfr(F0, B1, O1, 0); mm(acc, F1); interleave<16, 24>(); SCHED();
#pragma unroll 1
    for (int s = 0; s < 5; ++s) { STAGE(bA, SLOT, bB, 0) STAGE(bB, 0, bA, 0) STAGE(bA, 0, bA, SLOT) }
    ldfr(F1, bA, SLOT, 1); mm(acc, F0); interleave<16, 24>(); SCHED();
    mm(acc, F1);
  }
  STAMP(2);
  if (TLBW) asm volatile("s_waitcnt vmcnt(0)" : "+v"(tlb) :: "memory");
  {
    f32x4 kvp[2][2], zp[2];
    const f32x4 zero = {0, 0, 0, 0};
    const u32x4 ones = {0x3f803f80u, 0x3f803f80u, 0x3f803f80u, 0x3f803f80u};
#pragma unroll
    for (int u = 0; u < 2; ++u) {
      u32x4 pkf[2], vf[2];
#pragma unroll
      for (int t = 0; t < 2; ++t) { pkf[t] = pack_phi(acc[2 * u][2 + t], acc[2 * u + 1][2 + t]); vf[t] = pack8(acc[2 * u][4 + t], acc[2 * u + 1][4 + t]); }
      const u32x4 vk = mh ? vf[1] : vf[0], vs = mh ? vf[0] : vf[1];
      const u32x4 zk = mh ? pkf[1] : pkf[0], zs = mh ? pkf[0] : pkf[1];
#pragma unroll
      for (int dt = 0; dt < 2; ++dt) kvp[dt][1] = mfma(pkf[dt], vs, u ? kvp[dt][1] : zero);
      zp[1] = mfma(zs, ones, u ? zp[1] : zero);
#pragma unroll
      for (int dt = 0; dt < 2; ++dt) kvp[dt][0] = mfma(pkf[dt], vk, u ? kvp[dt][0] : zero);
      zp[0] = mfma(zk, ones, u ? zp[0] : zero);
    }
    const u32 mine = L_X1 + wave * 2304, theirs = L_X1 + (wave ^ 1) * 2304;
#pragma unroll
    for (int dt = 0; dt < 2; ++dt) *L<f32x4>(mine + dt * 1024 + lane * 16) = kvp[dt][1];
    if (r16 == 0) *L<f32x4>(mine + 2048 + g * 16) = zp[1];
    lds_set(W_X1 + wave, 1);
    f32x4 o0, o1, oz;
#pragma unroll 1
    for (u32 spins = 0; spins < (SPIN_CAP << 3); ++spins) {
      u32 fl;
      asm volatile("ds_read_b32 %0, %4\n\tds_read_b128 %1, %5\n\tds_read_b128 %2, %5 offset:1024\n\tds_read_b128 %3, %6\n\ts_waitcnt lgkmcnt(0)"
                   : "=&v"(fl), "=&v"(o0), "=&v"(o1), "=&v"(oz)
                   : "v"(L_FLAG + 4 * (W_X1 + (wave ^ 1))), "v"(theirs + lane * 16), "v"(theirs + 2048 + g * 16) : "memory");
      if (__builtin_amdgcn_readfirstlane(fl)) break;
    }
    const u32 head = cb * 2 + hh;
    st128_sc(pack8(kvp[0][0] + o0, kvp[1][0] + o1), ws, WS_PART + (head * 2 + mh) * 16384 + rb * 1024 + lane * 16);
    st128_sc(__builtin_bit_cast(u32x4, zp[0] + oz), ws, r16 == 0 ? WS_Z + head * 2048 + rb * 128 + mh * 64 + g * 16 : 0x40000000u);
  }
  lds_inc(W_PST);
  SCHED();
  STAMP(10);
  u32x4 pq[4];
#pragma unroll
  for (int mt = 0; mt < 1; ++mt) { pq[mt] = pack_phi(acc[mt][0], acc[mt][1]); PIN(pq[mt]); }
  SCHED();
  const bool last1 = drain_last(W_CNT1, 4);
  if (last1) post(ws, WS_F1 + (cb * 16 + lane) * 256 + rb * 8, tag, lane < 16);
  STAMP(3);
  SCHED();
#pragma unroll
  for (int mt = 1; mt < 4; ++mt) { pq[mt] = pack_phi(acc[mt][0], acc[mt][1]); PIN(pq[mt]); }
  u32x4 sel[2];
  {
    const u32 one = (g == q4) ? ((p4 & 1) ? 0x3f800000u : 0x3f80u) : 0u, a = (p4 & 2) ? 0u : one, b = (p4 & 2) ? one : 0u;
    sel[0] = u32x4{a, b, 0, 0}; sel[1] = u32x4{0, 0, a, b};
    PIN(sel[0]); PIN(sel[1]);
  }
  SCHED();
  if (last1) { await<0>(ws, WS_F1 + (cq * 16 + rb) * 256 + r16 * 8, tag); lds_set(W_GOT1, 1); }
  else lds_await<1>(W_GOT1, 1);
  STAMP(4);
  {
    const u32 head = cq * 2 + hh;
    u32x4 pr[16], zr[2];
#pragma unroll
    for (int n = 0; n < 2; ++n) zr[n] = ld128_sc(ws, lane * 16 + n * 1024, WS_Z + head * 2048);
#pragma unroll
    for (int r = 0; r < 16; ++r) pr[r] = ld128_sc(ws, lane * 16 + (r & 1) * 1024, WS_PART + (head * 2 + mh) * 16384 + ((r >> 1) ^ (rb & 7)) * 2048);
    SCHED();
    f32x4 zs = __builtin_bit_cast(f32x4, zr[0]) + __builtin_bit_cast(f32x4, zr[1]);
#pragma unroll
    for (int r = 0; r < 4; ++r) zs[r] = sum_8_16_32(zs[r]);
    const u32 zb = L_X2 + 4096 + wave * 64;
    if (lane < 8) *L<u32x2>(zb + (lane & 3) * 16 + (lane >> 2) * 8) = u32x2{pk2(zs[0], zs[1]), pk2(zs[2], zs[3])};
    FENCE();
    const u32x4 zf = *L<u32x4>(zb + g * 16);
    const f32x4 zero = {0, 0, 0, 0}, eps4 = {EPS, EPS, EPS, EPS};
    float inv[4];
#pragma unroll
    for (int mt = 0; mt < 4; ++mt) { inv[mt] = __builtin_amdgcn_rcpf(mfma(zf, pq[mt], eps4)[0]); PIN(inv[mt]); }
    SCHED();
    f32x4 sum[2] = {zero, zero};
#pragma unroll
    for (int r = 0; r < 16; ++r) { sum[0] = mfma(sel[0], pr[r], sum[0]); sum[1] = mfma(sel[1], pr[r], sum[1]); }
    const u32x4 kvm = pack8(sum[0], sum[1]);
    *L<u32x4>(L_X2 + wave * 1024 + lane * 16) = kvm;
    lds_set(W_X2 + wave, 1);
    STAMP(11);
    u32x2 om[4];
#pragma unroll
    for (int mt = 0; mt < 4; ++mt) { const f32x4 n = mfma(kvm, pq[mt], zero) * inv[mt]; om[mt] = u32x2{pk2(n[0], n[1]), pk2(n[2], n[3])}; PIN(om[mt]); }
    u32x4 kvo;
#pragma unroll 1
    for (u32 spins = 0; spins < (SPIN_CAP << 3); ++spins) {
      u32 fl;
      asm volatile("ds_read_b32 %0, %2\n\tds_read_b128 %1, %3\n\ts_waitcnt lgkmcnt(0)" : "=&v"(fl), "=&v"(kvo)
                   : "v"(L_FLAG + 4 * (W_X2 + (wave ^ 1))), "v"(L_X2 + (wave ^ 1) * 1024 + lane * 16) : "memory");
      if (__builtin_amdgcn_readfirstlane(fl)) break;
    }
    const u32 oo = WS_O + rb * 262144 + mh * 131072 + head * 2048 + lane * 16;
#pragma unroll
    for (int mt = 0; mt < 4; ++mt) {
      const f32x4 n = mfma(kvo, pq[mt], zero) * inv[mt];
      const u32x2 ot = {pk2(n[0], n[1]), pk2(n[2], n[3])};
      st128_sc(mh ? u32x4{ot.x, ot.y, om[mt].x, om[mt].y} : u32x4{om[mt].x, om[mt].y, ot.x, ot.y}, ws, oo + (mt >> 1) * 65536 + (mt & 1) * 1024);
    }
  }
  STAMP(5);
  u32 wb[4];
  {
    const int fw = (2 * g + (q4 >> 1)) & 3;
#pragma unroll
    for (int t = 0; t < 4; ++t) wb[t] = opaque((4 * g + q4) * 128 + ((t ^ fw) * 32) + p4 * 8);
  }
  f32x4 oc[2][4];
#pragma unroll
  for (int m = 0; m < 2; ++m)
#pragma unroll
    for (int t = 0; t < 4; ++t) { oc[m][t] = f32x4{0, 0, 0, 0}; PIN(oc[m][t]); }
  if (drain_last(W_CNT2, 4)) {
    post(ws, WS_F2 + (rb * 16 + lane) * 256 + cb * 8, tag, lane < 16);
    STAMP(6);
    await<0>(ws, WS_F2 + (rb2 * 16 + cb2) * 256 + r16 * 8, tag);
    lds_set(W_GOT2, 1);
    if (bid == 0) post(ws, WS_EPOCH, tag, lane == 0);
  } else { STAMP(6); lds_await<1>(W_GOT2, 1); }
  STAMP(7);
  {
    u32x4 of[WIN][2];
    const u32 ob = WS_O + rb2 * 262144 + wave * 65536, vo = opaque(lane * 16);
    auto ldo = [&](int st, int m) __attribute__((always_inline)) { of[st % WIN][m] = ld128_sc(ws, vo + ((st & 1) * 2 + m) * 1024, ob + (((st & ~1) ^ hx) * 2048)); };
#pragma unroll
    for (int st = 0; st < WIN; ++st) { ldo(st, 0); ldo(st, 1); SCHED(); }
    lds_await<0>(W_WO1, 4);
    const bool wo2 = lds_get(W_WO2);
    auto ldw = [&](u32x4 (&w)[4], int st) __attribute__((always_inline)) {
      const u32 off = st < 24 ? (st & 15) * 4096 : ((st - 24) >> 1) * CH_STRIDE + (st & 1) * 4096;
#pragma unroll
      for (int t = 0; t < 4; ++t) w[t] = tr_frag(wb[t] + off, 2048);
    };
    u32x4 wf[2][4];
    ldw(wf[0], 0);
#pragma unroll
    for (int st = 0; st < 32; ++st) {
      if (st == 15 || st == 23) {
#pragma unroll
        for (int t = 0; t < 4; ++t) wb[t] = opaque(wb[t] + (st == 15 ? 65536 : L_CH - 65536));
        if (st == 23 && !wo2) lds_await<0>(W_WO2, 1);
      }
      if (st < 31) ldw(wf[(st + 1) & 1], st + 1);
#pragma unroll
      for (int m = 0; m < 2; ++m)
#pragma unroll
        for (int t = 0; t < 4; ++t) oc[m][t] = mfma(wf[st & 1][t], of[st % WIN][m], oc[m][t]);
      if (st + WIN < 32) { ldo(st + WIN, 0); ldo(st + WIN, 1); }
#pragma unroll
      for (int m = 0; m < 2; ++m) {
        __builtin_amdgcn_sched_group_barrier(0x8, 1, 0); __builtin_amdgcn_sched_group_barrier(0x100, 2, 0);
        __builtin_amdgcn_sched_group_barrier(0x8, 1, 0); __builtin_amdgcn_sched_group_barrier(0x100, 1, 0);
        __builtin_amdgcn_sched_group_barrier(0x8, 1, 0); __builtin_amdgcn_sched_group_barrier(0x100, 1, 0);
        __builtin_amdgcn_sched_group_barrier(0x8, 1, 0); __builtin_amdgcn_sched_group_barrier(0x20, 1, 0);
      }
      SCHED();
    }
    STAMP(8);
    const rsrc_t ro = mkrsrc(pout, 2048 * 2048 * 2);
    const u32 sc = L_X1 + (wave ^ 1) * 2304;
    const u32 sw_ = opaque(sc + r16 * 144 + g * 8), sr_ = opaque(sc + r8 * 144 + c8 * 16);
    const u32 oo = (rb2 * 128 + wave * 32 + r8) * 4096 + 2048 + cb2 * 128 + c8 * 16;
#pragma unroll
    for (int m = 0; m < 2; ++m) {
#pragma unroll
      for (int t = 0; t < 4; ++t) *L<u32x2>(sw_ + t * 32) = u32x2{pk2(oc[m][t][0], oc[m][t][1]), pk2(oc[m][t][2], oc[m][t][3])};
      FENCE();
      const u32x4 r0 = *L<u32x4>(sr_), r1 = *L<u32x4>(sr_ + 1152);
      FENCE();
      st128_sc(r0, ro, oo + m * 65536); st128_sc(r1, ro, oo + m * 65536 + 32768);
    }
  }
  STAMP(9);
  STAMP_OUT();
}

extern "C" void kernel_launch(void* const* d_in, const int* in_sizes, int n_in, void* d_out, int out_size, void* d_ws, size_t ws_size,
                              hipStream_t stream) {
  Params p;
  p.q = (const u16*)d_in[0]; p.k = (const u16*)d_in[1]; p.v = (const u16*)d_in[2]; p.sk = (const u16*)d_in[3];
  p.h = (const u16*)d_in[4]; p.dd = (const u16*)d_in[5]; p.x = (const u16*)d_in[6]; p.wqkv = (const u16*)d_in[7];
  p.wo = (const u16*)d_in[8]; p.out = (u16*)d_out; p.ws = (char*)d_ws;
  const u64 kord[32] = {
      0xc8b49ae7df236150ull, 0x8bc9d5ae264f3701ull, 0x8cbde974af530612ull, 0xb8c7af9d6e514023ull,
      0xb9c8eda5f7026134ull, 0xb8c0da1e7f694235ull, 0x8cdabfe925714306ull, 0x8ba9c31e0fd42657ull,
      0xcb98e74a321d506full, 0x89cbade7542361f0ull, 0xcb89e57d43a60f21ull, 0xb8a9cde7f4605132ull,
      0xbce9a7d80f651243ull, 0xbca87e9f1d056324ull, 0xcb8a9df7e1026345ull, 0xcb89ea1d0732f546ull,
      0x89ca7b436d1520feull, 0x89cdab5720614e3full, 0x89bced7a634152f0ull, 0x8ab97ed56c42f031ull,
      0xb987acd6e5f10432ull, 0x8cba7d961ef50243ull, 0x8cd9fa76b21e0534ull, 0x8a9be1cd67f02345ull,
      0xc89ba73246f15e0dull, 0x98abc54d26713f0eull, 0x8ca9bde47356012full, 0xba9876cf5d4e3120ull,
      0x9abce875d602f431ull, 0xb8adc7e95610f432ull, 0x8adc96b7e25f0143ull, 0x8baec9f71653d024ull};
  for (int i = 0; i < 32; ++i) p.kord[i] = kord[i];
  (void)hipFuncSetAttribute((const void*)fused_h3_linattn, hipFuncAttributeMaxDynamicSharedMemorySize, LDS_BYTES);
  fused_h3_linattn<<<256, 512, LDS_BYTES, stream>>>(p);
}
```

```cpp
#include <hip/hip_runtime.h>
#include <hip/hip_bf16.h>

typedef unsigned short u16;
typedef unsigned int u32;
typedef unsigned long long u64;
typedef float f32x2 __attribute__((ext_vector_type(2)));
typedef float f32x4 __attribute__((ext_vector_type(4)));
typedef u32 u32x2 __attribute__((ext_vector_type(2)));
typedef u32 u32x4 __attribute__((ext_vector_type(4)));
typedef short s16x4 __attribute__((ext_vector_type(4)));
typedef __bf16 bf16x2 __attribute__((ext_vector_type(2)));
typedef __bf16 bf16x8 __attribute__((ext_vector_type(8)));
typedef __amdgpu_buffer_rsrc_t rsrc_t;
#define LAS __attribute__((address_space(3)))
#define DI __device__ __forceinline__
#define INL __attribute__((always_inline))
#if defined(__HIP_DEVICE_COMPILE__)
#define NOPK __attribute__((target("no-packed-fp32-ops")))
#else
#define NOPK
#endif

#define CODE_PF 25600
#define NSG 2
#define A2D 44
#define WIN 5
#define CAS 0
#define QO 2
#define P1AT 1
#define CG 1
#define APOLL 1
#define P1 0
#define P2 0
#define STG 0
#define STAMPS 0

#define KTABLE \
  0xc8b49ae7df236150ull, 0x8bc9d5ae264f3701ull, 0x8cbde974af530612ull, 0xb8c7af9d6e514023ull, \
  0xb9c8eda5f7026134ull, 0xb8c0da1e7f694235ull, 0x8cdabfe925714306ull, 0x8ba9c31e0fd42657ull, \
  0xcb98e74a321d506full, 0x89cbade7542361f0ull, 0xcb89e57d43a60f21ull, 0xb8a9cde7f4605132ull, \
  0xbce9a7d80f651243ull, 0xbca87e9f1d056324ull, 0xcb8a9df7e1026345ull, 0xcb89ea1d0732f546ull, \
  0x89ca7b436d1520feull, 0x89cdab5720614e3full, 0x89bced7a634152f0ull, 0x8ab97ed56c42f031ull, \
  0xb987acd6e5f10432ull, 0x8cba7d961ef50243ull, 0x8cd9fa76b21e0534ull, 0x8a9be1cd67f02345ull, \
  0xc89ba73246f15e0dull, 0x98abc54d26713f0eull, 0x8ca9bde47356012full, 0xba9876cf5d4e3120ull, \
  0x9abce875d602f431ull, 0xb8adc7e95610f432ull, 0x8adc96b7e25f0143ull, 0x8baec9f71653d024ull
#define K1A 0x442432f650332105ull
#define K1B 0x2433220043433f3full

constexpr u32 WS_EPOCH = 0;
constexpr u32 WS_F1 = 65536;
constexpr u32 WS_F2 = WS_F1 + 65536;
constexpr u32 WS_FA = WS_F2 + 65536;
constexpr u32 WS_FY = WS_FA + 131072;
constexpr u32 WS_Z = 512u << 10;
constexpr u32 WS_PART = 1u << 20;
constexpr u32 WS_O = 2u << 20;
constexpr u32 WS_KVT = 6u << 20;
constexpr u32 WS_YT = 10u << 20;
constexpr u32 WS_BYTES = 14u << 20;
constexpr u32 WS_DBG = 32u << 20;
constexpr u32 OOB = 0x40000000u;

constexpr u32 SLOT = 40960;
constexpr u32 L_X1 = 98304;
constexpr u32 L_X2 = L_X1 + 4 * 2304;
constexpr u32 CX_STRIDE = 4640, CH_STRIDE = 8320, C_E = 4160;
constexpr u32 L_FLAG = 163840 - 128;
constexpr u32 L_CH = L_FLAG - 4 * CH_STRIDE;
constexpr u32 L_CX = L_CH - 4 * CX_STRIDE;
constexpr u32 LDS_BYTES = 163840;
static_assert(L_X2 + 4096 + 256 <= L_CX && L_CH >= 3 * SLOT, "lds map");
enum { W_WO1 = 0, W_WO2, W_STAGED, W_PST, W_X1 = 4, W_X2 = 8, W_CNT1 = 12, W_CNT2, W_CNTY, W_GOT1, W_GOT2, W_PHI };

constexpr int AUX_SC1 = 16;
constexpr u32 SPIN_CAP = 1u << 16;

DI float f_(u32 x) { return __builtin_bit_cast(float, x); }
DI u32 u_(float x) { return __builtin_bit_cast(u32, x); }
DI f32x4 mfma(u32x4 a, u32x4 b, f32x4 c) {
  return __builtin_amdgcn_mfma_f32_16x16x32_bf16(__builtin_bit_cast(bf16x8, a), __builtin_bit_cast(bf16x8, b), c, 0, 0, 0);
}
DI void mfma_ip(f32x4& c, u32x4 a, u32x4 b) { asm volatile("v_mfma_f32_16x16x32_bf16 %0, %1, %2, %0" : "+v"(c) : "v"(a), "v"(b)); }
DI u32 pk2(float lo, float hi) { return __builtin_bit_cast(u32, __builtin_convertvector(f32x2{lo, hi}, bf16x2)); }
DI u32x2 pack4(f32x4 a) { return u32x2{pk2(a[0], a[1]), pk2(a[2], a[3])}; }
DI u32x4 pack8(f32x4 a, f32x4 b) { return u32x4{pk2(a[0], a[1]), pk2(a[2], a[3]), pk2(b[0], b[1]), pk2(b[2], b[3])}; }
DI float blo(u32 x) { return f_(x << 16); }
DI float bhi(u32 x) { return f_(x & 0xffff0000u); }
#define SCHED() __builtin_amdgcn_sched_barrier(0)
#define FENCE() asm volatile("" ::: "memory")
#define BAR() do { FENCE(); SCHED(); __builtin_amdgcn_s_barrier(); SCHED(); FENCE(); } while (0)
#define PIN(x) asm volatile("" : "+v"(x))
#define SPIN(x) asm volatile("" : "+s"(x))
template <int N> DI void waitv() { asm volatile("s_waitcnt vmcnt(%0)" ::"n"(N) : "memory"); }
#define WAITLG(n) __builtin_amdgcn_s_waitcnt(0xC07F | ((n) << 8))
DI void waitl() { FENCE(); WAITLG(0); FENCE(); }
DI void waitcnt(int vm, int lg) {
#define WC(V, G) if (vm == V && lg == G) __builtin_amdgcn_s_waitcnt(((V) & 15) | 0x70 | ((G) << 8) | (((V) >> 4) << 14));
  WC(0, 0) WC(2, 8) WC(4, 8) WC(6, 8) WC(8, 8) WC(10, 8) WC(12, 8) WC(14, 8)
}
template <class T> DI LAS T* L(u32 a) { return (LAS T*)(size_t)a; }
DI u32 opaque(u32 a) { PIN(a); return a; }
DI rsrc_t mkrsrc(const void* p, u32 bytes) { return __builtin_amdgcn_make_buffer_rsrc((void*)p, 0, bytes, 0x20000); }
template <int LI, int SI> DI void dma(rsrc_t r, u32 lds, u32 voff, u32 soff) {
  u32 t;
  asm volatile("s_add_u32 m0, %1, %2\n\ts_add_u32 %0, %5, %6\n\tbuffer_load_dwordx4 %3, %4, %0 offen lds"
               : "=&s"(t) : "s"(lds), "i"(LI), "v"(voff), "s"(r), "s"(soff), "i"(SI) : "memory", "scc");
}
DI u32x4 ld128(rsrc_t r, u32 voff, u32 soff) { return __builtin_amdgcn_raw_buffer_load_b128(r, voff, soff, 0); }
DI u32x4 ld128_sc(rsrc_t r, u32 voff, u32 soff) { return __builtin_amdgcn_raw_buffer_load_b128(r, voff, soff, AUX_SC1); }
DI void st128_sc(u32x4 v, rsrc_t r, u32 voff) { __builtin_amdgcn_raw_buffer_store_b128(v, r, voff, 0, AUX_SC1); }

template <int W> DI void lds_set(u32 vf, u32 v1) {
  asm volatile("s_mov_b64 exec, 1\n\tds_write_b32 %0, %1 offset:%2\n\ts_mov_b64 exec, -1" :: "v"(vf), "v"(v1), "i"(4 * W) : "memory");
}
template <int W> DI void lds_inc(u32 vf, u32 v1) {
  asm volatile("s_mov_b64 exec, 1\n\tds_add_u32 %0, %1 offset:%2\n\ts_mov_b64 exec, -1" :: "v"(vf), "v"(v1), "i"(4 * W) : "memory");
}
template <int W> DI u32 lds_get(u32 vf) {
  u32 t;
  asm volatile("ds_read_b32 %0, %1 offset:%2\n\ts_waitcnt lgkmcnt(0)" : "=v"(t) : "v"(vf), "i"(4 * W) : "memory");
  return __builtin_amdgcn_readfirstlane(t);
}
template <int W, int WANT, int SL> DI void lds_await(u32 vf) {
  u32 t, s, n;
  asm volatile("s_mov_b32 %2, %6\n\ts_branch 1f\n"
               "0:\n\ts_sleep %7\n"
               "1:\n\tds_read_b32 %0, %3 offset:%4\n\ts_sub_u32 %2, %2, 1\n\ts_waitcnt lgkmcnt(0)\n\ts_cbranch_scc1 2f\n\t"
               "v_readfirstlane_b32 %1, %0\n\ts_cmp_lt_u32 %1, %5\n\ts_cbranch_scc1 0b\n"
               "2:" : "=&v"(t), "=&s"(s), "=&s"(n) : "v"(vf), "i"(4 * W), "i"(WANT), "i"(SPIN_CAP << 3), "i"(SL) : "memory", "scc");
}
template <int W> DI bool drain_last(u32 vf, u32 v1) {
  u32 old;
  asm volatile("s_waitcnt vmcnt(0)\n\ts_mov_b64 exec, 1\n\tds_add_rtn_u32 %0, %1, %2 offset:%3\n\ts_mov_b64 exec, -1\n\ts_waitcnt lgkmcnt(0)"
               : "=&v"(old) : "v"(vf), "v"(v1), "i"(4 * W) : "memory");
  return __builtin_amdgcn_readfirstlane(old) == 3;
}
DI void post(rsrc_t ws, u32 voff, u32x2 tag) { __builtin_amdgcn_raw_buffer_store_b64(tag, ws, voff, 0, AUX_SC1); }
#define POLL_ASM(F, A, R) "s_mov_b64 exec, 0xffff\n\tv_mov_b64 " F ", 0\n\tbuffer_atomic_add_x2 " F ", " A ", " R ", 0 offen sc0 sc1\n\ts_mov_b64 exec, -1"
DI void poll(u32x2& f, rsrc_t ws, u32 voff) { asm volatile(POLL_ASM("%0", "%1", "%2") : "=&v"(f) : "v"(voff), "s"(ws) : "memory"); }
DI bool missing(u32x2 f, u64 tag) { return (__builtin_amdgcn_ballot_w64(__builtin_bit_cast(u64, f) != tag) & 0xffff) != 0; }
template <int SL> DI void await(rsrc_t ws, u32 voff, u64 tag) {
  u32x2 f; u32 n;
  asm volatile("s_mov_b32 %1, %5\n\ts_branch 1f\n"
               "0:\n\ts_sleep %6\n"
               "1:\n\t" POLL_ASM("%0", "%2", "%3") "\n\ts_sub_u32 %1, %1, 1\n\ts_waitcnt vmcnt(0)\n\ts_cbranch_scc1 2f\n\t"
               "v_cmp_ne_u64 vcc, %4, %0\n\ts_and_b64 vcc, vcc, 0xffff\n\ts_cbranch_vccnz 0b\n"
               "2:" : "=&v"(f), "=&s"(n) : "v"(voff), "s"(ws), "s"(tag), "i"(SPIN_CAP), "i"(SL) : "memory", "scc", "vcc");
}

DI void await2(u32x2& fa, u32x2& fb, rsrc_t ws, u32 voff, u64 tag) {
  u32 n;
  asm volatile("s_mov_b32 %2, %6\n"
               "0:\n\t" POLL_ASM("%1", "%3", "%4") "\n\ts_waitcnt vmcnt(1)\n\tv_cmp_ne_u64 vcc, %5, %0\n\ts_and_b64 vcc, vcc, 0xffff\n\ts_cbranch_vccz 2f\n\t"
               POLL_ASM("%0", "%3", "%4") "\n\ts_sub_u32 %2, %2, 1\n\ts_cbranch_scc1 2f\n\ts_waitcnt vmcnt(1)\n\tv_cmp_ne_u64 vcc, %5, %1\n\ts_and_b64 vcc, vcc, 0xffff\n\ts_cbranch_vccnz 0b\n"
               "2:" : "+v"(fa), "+v"(fb), "=&s"(n) : "v"(voff), "s"(ws), "s"(tag), "i"(SPIN_CAP) : "memory", "scc", "vcc");
}

#define STAMPINS "s_memrealtime"
#define STAMP(i)
#define STAMP_OUT()

struct Params {
  const u16 *x, *wqkv;
  char* ws;
  const u16 *wo, *q, *k, *v, *sk, *h, *dd;
  u16* out;
  u64 kord[32];
};

DI s16x4 tr_read(u32 a) { return __builtin_amdgcn_ds_read_tr16_b64_v4i16(L<s16x4>(a)); }
DI u32x4 join(s16x4 lo, s16x4 hi) { return __builtin_bit_cast(u32x4, __builtin_shufflevector(lo, hi, 0, 1, 2, 3, 4, 5, 6, 7)); }
struct XFr { u32x4 x[4][2]; };
struct WFr { s16x4 w[6][2][2]; };
DI void rdx(XFr& f, const u32 (&b)[4], int i) { f.x[i >> 1][i & 1] = *L<u32x4>(b[i & 1] + (i >> 1) * 2048); }
DI void rdw(WFr& f, const u32 (&b)[4], int i) {
  const int n = i >> 2, sg = ((n >> 1) + 1) % 3;
  f.w[n][(i >> 1) & 1][i & 1] = tr_read(b[2 + (n & 1)] + sg * 8192 + ((i >> 1) & 1) * 4096 + (i & 1) * 512);
}
DI void mm1(f32x4 (&acc)[4][6], const XFr& x, const WFr& w, int i) {
  const int n = i >> 3, mt = (n & 1) ? 3 - ((i >> 1) & 3) : (i >> 1) & 3, ks = (i ^ (i >> 1)) & 1;
  const u32x4 wv = join(w.w[n][ks][0], w.w[n][ks][1]);
  if (n >= 4) mfma_ip(acc[mt][n], wv, x.x[mt][ks]);
  else mfma_ip(acc[mt][n], x.x[mt][ks], wv);
}
constexpr float EPS = 1e-6f;
DI float phi(float t) {
  const float e = __builtin_amdgcn_fmed3f(__builtin_amdgcn_exp2f(__builtin_fmaf(t, 1.44269504088896f, -EPS * 1.44269504088896f)), 0.f, 1.f);
  return e + __builtin_bit_cast(float, __builtin_elementwise_max(__builtin_bit_cast(int, t), __builtin_bit_cast(int, EPS)));
}

struct CFr { u32x2 a[8][2]; u32x4 b[8]; };
DI void ldconv1(CFr& f, const u32 (&hb)[4], u32 xb, u32 off, int k) {
  const int i = k / 3, e = k % 3;
  if (e == 0) f.b[i] = *L<u32x4>(xb + off + (7 - i) * 512);
  else { LAS u32* hp = L<u32>(hb[i >> 1] + off + (i & 1) * 512 + (e - 1) * 8); f.a[i][e - 1] = u32x2{hp[0], hp[1]}; }
}
DI void mmconv1(f32x4 (&acc)[8], const CFr& f, int k) {
  const int n = (k >= 1) + (k >= 3) + (k >= 6) + (k >= 10) + (k >= 15) + (k >= 21) + (k >= 28);
  k -= n * (n + 1) / 2;
  mfma_ip(acc[n], u32x4{f.a[k][0].x, f.a[k][0].y, f.a[k][1].x, f.a[k][1].y}, f.b[n - k]);
}

__global__ NOPK __launch_bounds__(512, 2) void fused_h3_linattn(Params p) {
  const int bid = __builtin_amdgcn_workgroup_id_x(), xcd = bid & 7, j = bid >> 3;
  const int rb = (xcd & 1) * 8 + (j & 7), cb = (xcd >> 1) * 4 + (j >> 3);
  const int cq = (cb & 12) | ((cb + 1) & 3);
  const int rb2 = (xcd >> 1) * 4 + (j & 3), cb2 = (xcd & 1) * 8 + (j >> 2);
  const int hx = (((j >> 2) + 2 * (j & 3)) & 7) * 4;
  const int wave = __builtin_amdgcn_readfirstlane(__builtin_amdgcn_workitem_id_x() >> 6), lane = __builtin_amdgcn_workitem_id_x() & 63;
  const int r8 = lane >> 3, c8 = lane & 7;
  STAMP(0);
  const u16 *pwo = p.wo, *pq_ = p.q, *pk_ = p.k, *pv_ = p.v, *psk = p.sk, *ph_ = p.h, *pdd = p.dd;
  u16* pout = p.out;
  const rsrc_t ws = mkrsrc(p.ws, WS_BYTES), rx = mkrsrc(p.x, 2048 * 1024 * 2), rw = mkrsrc(p.wqkv, 1024 * 3072 * 2);
  const u32 qd = (cq - cb) * 128;
  u32 vx[2], vw[2];
#pragma unroll
  for (int e = 0; e < 2; ++e) {
    vx[e] = r8 * 2048 + ((c8 ^ (4 * e + (r8 >> 1))) * 16);
    vw[e] = r8 * 6144 + ((c8 ^ ((((r8 >> 1) & 1) | (e << 1)) << 1)) * 16);
  }
  {
    const u32 s = (rb - cb) & 15, sx = (rb * 128 + wave * 16) * 2048 + s * 128, sw = (s * 64 + wave * 8) * 6144 + cb * 128;
    const u32 vwp = vw[0] ^ ((wave & 1) * 64);
    dma<SLOT, 0>(rx, wave * 2048, vx[0], sx);
    dma<SLOT + 16384 + 8192, 2048>(rw, wave * 1024, vwp, sw);
    dma<SLOT + 16384, 0>(rw, wave * 1024, vwp, sw + qd);
    dma<SLOT + 16384 + 16384, 4096>(rw, wave * 1024, vwp, sw);
    dma<SLOT + 1024, 16384>(rx, wave * 2048, vx[1], sx);
  }
  STAMP(24);
  asm volatile("" : "+s"(pwo), "+s"(pq_), "+s"(pk_), "+s"(pv_), "+s"(psk), "+s"(ph_), "+s"(pdd), "+s"(pout));
  const int chl = (rb >> 3) * 32 + (wave & 3) * 8 + (rb & 7);
  u64 ep0, tag, kord0;
  u32 dv0, dvw;
  asm volatile("s_load_dwordx2 %0, %3, 0\n\ts_load_dwordx2 %1, %4, %5\n\ts_load_dword %2, %6, %7" : "=&s"(ep0), "=&s"(kord0), "=&s"(dv0)
               : "s"(p.ws), "s"(__builtin_amdgcn_kernarg_segment_ptr()), "s"((u32)offsetof(Params, kord) + j * 8), "s"(pdd), "s"(((cb * 64 + chl) >> 1) * 4) : "memory");
#define GET_TAG() do { \
    asm volatile("s_waitcnt lgkmcnt(0)\n\ts_mov_b64 %0, %2\n\ts_mov_b32 %1, %4" : "=&s"(tag), "=&s"(dvw) : "s"(ep0), "s"(kord0), "s"(dv0) : "memory"); \
    tag = tag * 6364136223846793005ull + 1442695040888963407ull; SPIN(tag); } while (0)
#define TAG2 (u32x2{(u32)tag, (u32)(tag >> 32)})
  const rsrc_t rwo = mkrsrc(pwo, 1024 * 1024 * 2);
#define VWO (r8 * 2048 + ((c8 ^ (((r8 >> 1) & 3) << 1)) * 16))
  const u32 vf = opaque(L_FLAG), v1 = opaque(1);

  if (wave >= 4) {
    const int hw = wave - 4, aw = hw & 1, ca = (cb + aw + CAS) & 15;
    const int R2 = rb * 128 + r8 * 16 + aw * 8;
    const u32 cx = L_CX + hw * CX_STRIDE, chr = L_CH + hw * CH_STRIDE;
    u32x4 qq[8];
    if (hw < 2) {
      const u32 sx0 = (rb * 128 + hw * 64) * 2048, sw0 = (hw * 32) * 6144 + cb * 128;
      const int kbase = (xcd & 1) * 8 - (xcd >> 1) * 4;
      u64 kord = (((j & 16) ? K1B : K1A) >> (4 * (j & 15))) << 4;
      auto issue = [&](int s_, u32 base, int half) INL {
        const u32 s = ((u32)(kord >> (4 * s_)) + kbase) & 15;
        const u32 sx = sx0 + s * 128 + half * 65536, sw = sw0 + s * (64 * 6144) + half * 98304, sq = sw + qd;
        const u32 lx = base + hw * 8192 + half * 4096, lw = base + 16384 + hw * 4096 + half * 2048;
        dma<0, 0>(rx, lx, vx[0], sx); dma<1024, 16384>(rx, lx, vx[1], sx);
        dma<8192, 2048>(rw, lw, vw[0], sw); dma<9216, 2048 + 49152>(rw, lw, vw[1], sw);
        dma<0, 0>(rw, lw, vw[0], sq); dma<1024, 49152>(rw, lw, vw[1], sq);
        dma<16384, 4096>(rw, lw, vw[0], sw); dma<17408, 4096 + 49152>(rw, lw, vw[1], sw);
        dma<2048, 32768>(rx, lx, vx[0], sx); dma<3072, 49152>(rx, lx, vx[1], sx);
      };
      issue(1, 2 * SLOT, 0);
      GET_TAG();
      asm volatile("s_mov_b64 %0, %1" : "=s"(kord) : "s"(kord0) : "memory");
      waitv<10>(); BAR();
      issue(1, 2 * SLOT, 1);
      u32 base = 0;
#pragma unroll 1
      for (int s = 2; s < 16; ++s) {
        issue(s, base, 0); issue(s, base, 1);
        base = base == 2 * SLOT ? 0 : base + SLOT;
        waitv<20>(); BAR();
      }
    } else {
      const int R0 = rb * 128 + aw * 64 + r8 * 8;
      const rsrc_t rk = mkrsrc(pk_, 2048 * 1024 * 2), rv = mkrsrc(pv_, 2048 * 1024 * 2), rs = mkrsrc(psk, 1024 * 4 * 2);
      const u32 vo = R0 * 2048 + ca * 128 + c8 * 16;
      const u32 vh = R0 == 0 ? OOB : vo - 3 * 2048;
      const u32 kvt_vo = WS_KVT + ca * 262144 + (c8 >> 2) * 131072 + (255 - (R0 >> 3)) * 64 + (c8 & 3) * 16;
      u32x4 kk[11], vv[8], tw[4];
      const u32x2 t2 = __builtin_amdgcn_raw_buffer_load_b64(rs, ca * 512 + lane * 8, 0, 0);
      GET_TAG();
      waitv<1>(); BAR();
#define AB(i) do { BAR(); if (STG) STAMP(8 + (i)); } while (0)
      *L<u32x2>(chr + 64 + lane * 8) = t2;
      FENCE();
#pragma unroll
      for (int i = 0; i < 4; ++i) tw[i] = *L<u32x4>(chr + 64 + c8 * 64 + i * 16);
      const int sg = NSG > 1 ? (j + (j >> 3) + xcd) % NSG : 0;
#pragma unroll 1
      for (int i = 0; i <= sg; ++i) AB(i);
      __builtin_amdgcn_s_sleep(8);
#pragma unroll
      for (int i = 0; i < 8; ++i) kk[3 + i] = ld128(rk, vo, i * 2048);
#pragma unroll
      for (int i = 0; i < 3; ++i) kk[i] = ld128(rk, vh, i * 2048);
      AB(sg + 1);
      float wf[8][4], ks[8][8], kf[11];
#pragma unroll
      for (int c = 0; c < 8; ++c) {
        if (!(c & 1)) PIN(tw[c >> 1]);
#pragma unroll
        for (int t = 0; t < 4; ++t) { const u32 d = tw[c >> 1][(c & 1) * 2 + (t >> 1)]; wf[c][t] = (t & 1) ? bhi(d) : blo(d); PIN(wf[c][t]); }
      }
#pragma unroll
      for (int u = 0; u < 24; ++u) {
        if (u % 3 == 0) {
          if (u == 9) {
#pragma unroll
            for (int i = 0; i < 8; ++i) vv[i] = ld128(rv, vo, i * 2048);
          }
          AB(sg + 2 + u / 3);
        }
        if (u < 16) {
          const int c = u >> 1, hf = u & 1;
#pragma unroll
          for (int i = hf ? 7 : 0; i < (hf ? 11 : 7); ++i) { PIN(kk[i]); const u32 d = kk[i][c >> 1]; kf[i] = (c & 1) ? bhi(d) : blo(d); }
#pragma unroll
          for (int i = hf * 4; i < hf * 4 + 4; ++i) {
            if (hf && i < 7) PIN(kf[i]);
            float a = kf[i] * wf[c][3];
            a = __builtin_fmaf(kf[i + 1], wf[c][2], a); a = __builtin_fmaf(kf[i + 2], wf[c][1], a); a = __builtin_fmaf(kf[i + 3], wf[c][0], a);
            ks[c][i] = a; PIN(ks[c][i]);
          }
        } else {
          const int c = u - 16;
          float o[8];
#pragma unroll
          for (int i = 0; i < 8; ++i) { PIN(vv[i]); const u32 d = vv[i][c >> 1]; o[i] = ks[c][i] * ((c & 1) ? bhi(d) : blo(d)); }
          st128_sc(u32x4{pk2(o[7], o[6]), pk2(o[5], o[4]), pk2(o[3], o[2]), pk2(o[1], o[0])}, ws, kvt_vo + c * 16384);
        }
      }
      AB(sg + 10);
      const u32 pa = opaque(lane < 16 ? WS_FA + ((ca * 16 + lane) * 2 + (rb < 8)) * 256 + ((rb & 7) * 2 + aw) * 8 : OOB);
      __builtin_amdgcn_s_sleep(6);
      waitv<0>();
      post(ws, pa, TAG2);
#pragma unroll 1
      for (int i = sg + 11; i <= 13; ++i) AB(i);
    }
    {
      u32x4 hh4[4], xs[4];
      const u32 fa = WS_FA + ((cb * 16 + rb) * 2 + (hw >> 1)) * 256 + (lane & 15) * 8;
      u32x2 fl;
      poll(fl, ws, fa);
      const rsrc_t rh = mkrsrc(ph_, 1024 * 2048 * 2), rq = mkrsrc(pq_, 2048 * 1024 * 2);
#pragma unroll
      for (int n = 0; n < 4; ++n) hh4[n] = ld128(rh, lane * 16 + n * 1024, (cb * 64 + chl) * 4096);
      auto ldq = [&]() INL {
        if (hw >= 2) {
#pragma unroll
          for (int i = 0; i < 8; ++i) asm volatile("buffer_load_dwordx4 %0, %1, %2, %3 offen" : "=v"(qq[i]) : "v"(R2 * 2048 + ca * 128 + c8 * 16), "s"(rq), "s"(i * 2048) : "memory");
        }
      };
      if (QO == 2) ldq();
      if (lane < 34) *L<u32x4>(lane < 30 ? cx + 4096 + lane * 16 : chr + (lane >> 1 & 1) * C_E + (lane & 1) * 16) = u32x4{0, 0, 0, 0};
      const u32 hr = opaque(chr + lane * 16), xw = opaque(L_CX + (lane & 3) * CX_STRIDE + (hw * 64 + (lane >> 2)) * 16);
      if (hw < 2) waitv<5>();
      BAR();
      if (STG) STAMP(22);
      if (QO == 2 && hw >= 2) waitv<12>(); else waitv<4>();
      PIN(fl);
      if (missing(fl, tag)) await<4>(ws, fa, tag);
      STAMP(23);
#pragma unroll
      for (int n = 0; n < 4; ++n) xs[n] = ld128_sc(ws, lane * 16 + n * 1024, WS_KVT + cb * 262144 + rb * 16384 + hw * 4096);
      if (QO == 0) ldq();
      SCHED();
#pragma unroll
      for (int n = 0; n < 4; ++n) *L<u32x4>(hr + 32 + n * 1024) = hh4[n];
      FENCE();
      u32 dp[4];
#pragma unroll
      for (int n = 0; n < 4; ++n) dp[n] = *L<u32>(hr + 28 + n * 1024);
#pragma unroll
      for (int n = 0; n < 4; ++n) {
        const u32x4 d = hh4[n];
        *L<u32x4>(hr + C_E + 32 + n * 1024) =
            u32x4{__builtin_amdgcn_alignbit(d[0], dp[n], 16), __builtin_amdgcn_alignbit(d[1], d[0], 16),
                  __builtin_amdgcn_alignbit(d[2], d[1], 16), __builtin_amdgcn_alignbit(d[3], d[2], 16)};
      }
#pragma unroll
      for (int n = 0; n < 4; ++n) *L<u32x4>(xw + n * 256) = xs[n];
      lds_inc<W_STAGED>(vf, v1);
      if (QO == 1) ldq();
    }
    const int b16 = lane & 15, kg = lane >> 4;
    u32 hb[4], xb = opaque(cx + (254 - 224 - 2 * b16 + kg) * 16);
#pragma unroll
    for (int i = 0; i < 4; ++i) hb[i] = opaque(chr + ((b16 & 1) ? 0 : C_E) + 4 + 4 * (b16 >> 1) + 16 * kg + i * 1024);
    f32x4 acc[8];
#pragma unroll
    for (int n = 0; n < 8; ++n) { acc[n] = f32x4{0, 0, 0, 0}; PIN(acc[n]); }
    lds_await<W_STAGED, 4, 1>(vf);
    STAMP(6);
    CFr C0, C1;
#pragma unroll
    for (int k = 0; k < 24; ++k) ldconv1(C0, hb, xb, 0, k);
    if (CG == 0) lds_await<W_PST, 4, 1>(vf); else if (CG == 1) lds_await<W_CNT1, 4, 1>(vf); else if (CG == 2) lds_await<W_PHI, 4, 1>(vf);
    waitl();
    STAMP(1);
#define RESIDUE(CUR, NXT, OFF) do { \
      _Pragma("unroll") for (int k = 0; k < 36; ++k) { \
        if (k == 0) WAITLG(9); else if (k == 15) WAITLG(10); \
        mmconv1(acc, CUR, k); \
        if (k % 3 != 2) ldconv1(NXT, hb, xb, OFF, k - k / 3); \
        SCHED(); \
      } \
    } while (0)
#pragma unroll 1
    for (int it = 0; it < 3; ++it) {
      RESIDUE(C0, C1, 64); RESIDUE(C1, C0, 128);
      xb = opaque(xb + 128);
#pragma unroll
      for (int i = 0; i < 4; ++i) hb[i] = opaque(hb[i] + 128);
    }
    RESIDUE(C0, C1, 64);
    u32x2 xv[8];
    const u32 xe = opaque(cx + (252 - 16 * b16 - 4 * kg) * 2);
#pragma unroll
    for (int k = 0; k < 36; ++k) {
      if (k == 0) WAITLG(9); else if (k == 15) WAITLG(0);
      mmconv1(acc, C1, k);
      if (k >= 16 && k < 24) xv[k - 16] = *L<u32x2>(xe + (23 - k) * 512);
      SCHED();
    }
    asm volatile("s_nop 7" : "+v"(acc[7]));
    SCHED();
    STAMP(2);
    {
      const float dv = blo(dvw >> ((chl & 1) * 16));
      u32x2 y[8];
#pragma unroll
      for (int n = 0; n < 8; ++n)
        y[n] = u32x2{pk2(__builtin_fmaf(bhi(xv[n].y), dv, acc[n][0]), __builtin_fmaf(blo(xv[n].y), dv, acc[n][1])),
                     pk2(__builtin_fmaf(bhi(xv[n].x), dv, acc[n][2]), __builtin_fmaf(blo(xv[n].x), dv, acc[n][3]))};
      const u32 yvo = WS_YT + cb * 262144 + ((kg & 1) * 2 + (b16 >> 3)) * 16384 + chl * 256 + (kg >> 1) * 128 + (b16 & 7) * 16;
#pragma unroll
      for (int n = 0; n < 8; n += 2) {
        auto s0 = __builtin_amdgcn_permlane16_swap(y[n].x, y[n + 1].x, false, false);
        auto s1 = __builtin_amdgcn_permlane16_swap(y[n].y, y[n + 1].y, false, false);
        st128_sc(u32x4{s0[0], s1[0], s0[1], s1[1]}, ws, yvo + n * 32768);
      }
      const u32 so = ((24 + hw * 2) ^ hx) * 65536 + cb2 * 128, vwo = VWO;
      dma<0, 0>(rwo, chr, vwo, so); dma<1024, 16384>(rwo, chr, vwo, so); dma<2048, 32768>(rwo, chr, vwo, so); dma<3072, 49152>(rwo, chr, vwo, so);
      dma<4096, 65536>(rwo, chr, vwo, so); dma<5120, 81920>(rwo, chr, vwo, so); dma<6144, 98304>(rwo, chr, vwo, so); dma<7168, 114688>(rwo, chr, vwo, so);
    }
    if (drain_last<W_CNTY>(vf, v1)) post(ws, lane < 32 ? WS_FY + (cb * 32 + lane) * 256 + rb * 8 : OOB, TAG2);
    STAMP(3);
    if (hw >= 2) {
      lds_await<W_GOT2, 1, 8>(vf);
      __builtin_amdgcn_s_sleep(A2D);
      await<4>(ws, WS_FY + ((ca * 16 + rb) * 2 + aw) * 256 + (lane & 15) * 8, tag);
      STAMP(4);
      u32x4 yy[8];
      const u32 yo = WS_YT + ca * 262144 + rb * 16384 + r8 * 2048 + aw * 128 + c8 * 16;
#pragma unroll
      for (int c = 0; c < 8; ++c) yy[c] = ld128_sc(ws, yo + c * 256, 0);
      const u32 tw_ = opaque(cx + r8 * 144 + c8 * 16), tr_ = opaque(cx + c8 * 144 + r8 * 16);
#pragma unroll
      for (int hf = 0; hf < 2; ++hf) {
#pragma unroll
        for (int c = 0; c < 4; ++c) *L<u32x4>(tw_ + c * 1152) = yy[hf * 4 + c];
        FENCE();
#pragma unroll
        for (int c = 0; c < 4; ++c) yy[hf * 4 + c] = *L<u32x4>(tr_ + c * 1152);
        FENCE();
      }
      const rsrc_t ro = mkrsrc(pout, 2048 * 2048 * 2);
#pragma unroll
      for (int i = 0; i < 8; ++i) {
        PIN(qq[i]);
        u32x4 o;
#pragma unroll
        for (int d = 0; d < 4; ++d) {
          const u32 y0 = yy[2 * d][i >> 1], y1 = yy[2 * d + 1][i >> 1];
          o[d] = pk2(((i & 1) ? bhi(y0) : blo(y0)) * blo(qq[i][d]), ((i & 1) ? bhi(y1) : blo(y1)) * bhi(qq[i][d]));
        }
        st128_sc(o, ro, (R2 + i) * 4096 + ca * 128 + c8 * 16);
      }
    } else { STAMP(4); }
    STAMP(5);
    STAMP_OUT();
    return;
  }

  __builtin_amdgcn_s_setprio(1);
  if (wave == 0 && lane < 32) *L<u32>(L_FLAG + 4 * lane) = 0;
  const int mh = wave & 1, hh = wave >> 1;
  const int r16 = lane & 15, g = lane >> 4, q4 = r16 >> 2, p4 = lane & 3;
  u32 cpf;
  {
    u64 pc;
    asm volatile("s_getpc_b64 %0" : "=s"(pc));
    asm volatile("buffer_load_dword %0, %1, %2, 0 offen" : "=&v"(cpf)
                 : "v"(lane < 8 ? (j * 8 + lane) * 128 : OOB), "s"(mkrsrc((const void*)(pc & ~127ull), wave == 2 ? CODE_PF : 0)) : "memory");
  }
  f32x4 acc[4][6];
  u32 b[4];
  {
    const u32 xo = (64 * mh + r16) * 128 + ((g ^ (r16 >> 1)) * 16);
    const int f = ((q4 >> 1) & 1) | ((g & 1) << 1);
    const u32 wo = 16384 + (8 * g + q4) * 128 + (((4 * hh + 2 * mh + (p4 >> 1)) ^ (f << 1)) * 16) + 8 * (p4 & 1);
    b[0] = opaque(SLOT + xo); b[1] = opaque(SLOT + (xo ^ 64)); b[2] = opaque(SLOT + wo); b[3] = opaque(SLOT + (wo ^ 32));
  }
#pragma unroll
  for (int a = 0; a < 4; ++a)
#pragma unroll
    for (int n = 0; n < 6; ++n) { const float i0 = (n & 6) == 2 ? 0.f : EPS; acc[a][n] = f32x4{i0, i0, i0, i0}; PIN(acc[a][n]); }
  const u32 vfm = opaque(L_FLAG + 4 * wave), vfp = opaque(L_FLAG + 4 * (wave ^ 1));
  const u32 x1w = opaque(L_X1 + wave * 2304 + lane * 16), x1r = opaque(L_X1 + (wave ^ 1) * 2304 + lane * 16);
  const u32 z1w = opaque(L_X1 + wave * 2304 + 2048 + g * 16), z1r = opaque(L_X1 + (wave ^ 1) * 2304 + 2048 + g * 16);
  const u32 a_part = opaque(WS_PART + ((cb * 2 + hh) * 2 + mh) * 16384 + rb * 1024 + lane * 16);
  const u32 a_z = opaque(r16 == 0 ? WS_Z + (cb * 2 + hh) * 2048 + rb * 128 + mh * 64 + g * 16 : OOB);
  const u32 a_post1 = opaque(lane < 16 ? WS_F1 + (cb * 16 + lane) * 256 + rb * 8 : OOB);
  u32x4 ones = {0x3f803f80u, 0x3f803f80u, 0x3f803f80u, 0x3f803f80u};
  PIN(ones);
  GET_TAG();
  u32x2 vtag = TAG2;
  PIN(vtag);
  XFr X0, X1; WFr W;
  waitv<1>(); BAR();
  STAMP(1);
#pragma unroll
  for (int i = 0; i < 20; ++i) { if (i < 2) rdx(X0, b, i); else if (i < 6) rdw(W, b, i - 2); else if (i < 12) rdx(X0, b, i - 4); else rdw(W, b, i - 8); SCHED(); }
  WAITLG(4); SCHED();
  int slot = 1;
#define HALF0(XC) do { \
    u32 d_; \
    _Pragma("unroll") for (int i = 0; i < 24; ++i) { \
      mm1(acc, XC, W, i); \
      if (i < 12) rdw(W, b, 12 + i); \
      else if (i == 12) { SPIN(slot); d_ = slot == 2 ? -2 * SLOT : SLOT; SPIN(d_); } \
      else if (i == 19) { SPIN(slot); slot = slot == 2 ? 0 : slot + 1; SPIN(slot); } \
      else if (i >= 14 && i < 18) b[i - 14] = opaque(b[i - 14] + d_); \
      SCHED(); \
    } \
    waitl(); BAR(); \
  } while (0)
#define HALF1(XC, XN) do { \
    _Pragma("unroll") for (int i = 24; i < 48; ++i) { \
      mm1(acc, XC, W, i); \
      if (i < 40) { if (i & 1) rdw(W, b, (i - 24) >> 1); else rdx(XN, b, (i - 24) >> 1); } \
      else if (i < 44) rdw(W, b, i - 32); \
      SCHED(); \
    } \
    WAITLG(4); SCHED(); \
  } while (0)
#pragma unroll 1
  for (int s = 0; s < 7; ++s) { HALF0(X0); HALF1(X0, X1); HALF0(X1); HALF1(X1, X0); }
  HALF0(X0);
#pragma unroll
  for (int i = 0; i < 8; ++i) { mm1(acc, X0, W, 24 + i); if (i < 4) rdw(W, b, i); rdx(X1, b, i); SCHED(); }
#pragma unroll
  for (int i = 0; i < 32; ++i) {
    mm1(acc, X1, W, i);
    if (i < 12) rdw(W, b, 4 + i);
    else {
#pragma unroll
      for (int e = (i - 12) * 8 / 5; e < (i - 11) * 8 / 5; ++e) {
        const int mt = e < 16 ? e >> 2 : 7 - (e >> 2);
        float t = phi(acc[mt][e >> 4][e & 3]); PIN(t); acc[mt][e >> 4][e & 3] = t;
      }
    }
    SCHED();
  }
  STAMP(2);
  {
    u32x4 pkf[2][2], vfr[2][2];
#pragma unroll
    for (int u = 0; u < 2; ++u)
#pragma unroll
      for (int t = 0; t < 2; ++t) { pkf[u][t] = pack8(acc[2 * u][t], acc[2 * u + 1][t]); PIN(pkf[u][t]); }
    SCHED();
    f32x4 kvp[2][2], zp[2];
    const f32x4 zero = {0, 0, 0, 0};
#pragma unroll
    for (int et = 1; et >= 0; --et) {
#pragma unroll
      for (int u = 1; u >= 0; --u) {
        zp[et] = mfma(pkf[u][et], ones, u ? zero : zp[et]);
        vfr[u][et] = pack8(acc[2 * u][2 + et], acc[2 * u + 1][2 + et]);
#pragma unroll
        for (int dt = 0; dt < 2; ++dt) kvp[dt][et] = mfma(pkf[u][dt], vfr[u][et], u ? zero : kvp[dt][et]);
      }
      SCHED();
    }
#pragma unroll
    for (int dt = 0; dt < 2; ++dt) *L<f32x4>(x1w + dt * 1024) = kvp[dt][1];
    *L<f32x4>(z1w) = zp[1];
    lds_set<W_X1>(vfm, v1);
    f32x4 o0, o1, oz;
    {
      u32 fl, s, n;
      asm volatile("s_mov_b32 %5, %9\n"
                   "1:\n\tds_read_b32 %0, %6 offset:%10\n\tds_read_b128 %1, %7\n\tds_read_b128 %2, %7 offset:1024\n\tds_read_b128 %3, %8\n\t"
                   "s_sub_u32 %5, %5, 1\n\ts_waitcnt lgkmcnt(3)\n\ts_cbranch_scc1 2f\n\tv_readfirstlane_b32 %4, %0\n\ts_cmp_eq_u32 %4, 0\n\ts_cbranch_scc1 1b\n"
                   "2:\n\ts_waitcnt lgkmcnt(0)"
                   : "=&v"(fl), "=&v"(o0), "=&v"(o1), "=&v"(oz), "=&s"(s), "=&s"(n)
                   : "v"(vfp), "v"(x1r), "v"(z1r), "i"(SPIN_CAP << 3), "i"(4 * W_X1) : "memory", "scc");
    }
    st128_sc(pack8(kvp[0][0] + o1, kvp[1][0] + o0), ws, a_part);
    st128_sc(__builtin_bit_cast(u32x4, zp[0] + oz), ws, a_z);
  }
  SCHED();
  STAMP(10);
  {
    WFr W2;
#pragma unroll
    for (int i = 0; i < 16; ++i) { mm1(acc, X0, W, 32 + i); if (i < 8) rdw(W2, b, 16 + i); else if (i == 8) lds_inc<W_PST>(vf, v1); SCHED(); }
#pragma unroll
    for (int i = 0; i < 16; ++i) { mm1(acc, X1, W2, 32 + i); SCHED(); }
  }
  const bool last1 = drain_last<W_CNT1>(vf, v1);
  if (last1) post(ws, a_post1, vtag);
  asm volatile("s_nop 3" : "+v"(cpf));
  STAMP(3);
  SCHED();
  u32x4 pq[4];
  const u32 head = cq * 2 + hh, l16 = opaque(lane * 16), a_poll1 = opaque(WS_F1 + (cq * 16 + rb) * 256 + r16 * 8);
  u32x2 f1, g1;
  asm volatile("" : "=v"(f1), "=v"(g1));
#pragma unroll
  for (int mt = 0; mt < 4; ++mt) {
    if (mt == P1AT) { if (last1) asm volatile(POLL_ASM("%0", "%1", "%2") : "+v"(f1) : "v"(a_poll1), "s"(ws) : "memory"); SCHED(); }
    pq[mt] = u32x4{pk2(phi(acc[mt][4][0]), phi(acc[mt][4][1])), pk2(phi(acc[mt][4][2]), phi(acc[mt][4][3])),
                   pk2(phi(acc[mt][5][0]), phi(acc[mt][5][1])), pk2(phi(acc[mt][5][2]), phi(acc[mt][5][3]))};
    PIN(pq[mt]); SCHED();
  }
  u32 s_z = WS_Z + head * 2048, s_part = WS_PART + (head * 2 + mh) * 16384 + (rb & 7) * 2048;
  SPIN(s_z); SPIN(s_part);
  int wst = 0;
  const u32 vwo = opaque(VWO), wsrc = wave * 16384 + cb2 * 128;
#define WO1() do { dma<0, 0>(rwo, wst * 4096 + wave * 1024, vwo, (wst ^ hx) * 65536 + wsrc); ++wst; } while (0)
  SCHED();
  if (CG == 2) lds_inc<W_PHI>(vf, v1);
  if (last1) {
    if (P1) {
      bool got = false;
#pragma unroll 1
      while (wst < 24) {
        asm volatile(POLL_ASM("%0", "%1", "%2") : "+v"(g1) : "v"(a_poll1), "s"(ws) : "memory");
        asm volatile("s_waitcnt vmcnt(1)" : "+v"(f1) :: "memory");
        if (!missing(f1, tag)) { got = true; break; }
        WO1(); WO1();
        asm volatile(POLL_ASM("%0", "%1", "%2") : "+v"(f1) : "v"(a_poll1), "s"(ws) : "memory");
        asm volatile("s_waitcnt vmcnt(3)" : "+v"(g1) :: "memory");
        if (!missing(g1, tag)) { got = true; break; }
        WO1();
      }
      if (!got) await2(f1, g1, ws, a_poll1, tag);
    } else {
      asm volatile("s_waitcnt vmcnt(0)" : "+v"(f1) :: "memory");
      if (missing(f1, tag)) {
#pragma unroll 1
        for (u32 spins = 0; spins < SPIN_CAP; ++spins) {
          u32x2 f;
          poll(f, ws, a_poll1);
          if (wst < 24) { WO1(); WO1(); WO1(); waitv<3>(); } else waitv<0>();
          PIN(f);
          if (!missing(f, tag)) break;
        }
      }
    }
    lds_set<W_GOT1>(vf, v1);
  } else {
    lds_await<W_PST, 4, 0>(vf);
#pragma unroll 1
    while (wst < 24) { if (lds_get<W_GOT1>(vf)) break; WO1(); }
    if (wst == 24) lds_await<W_GOT1, 1, 0>(vf);
  }
  STAMP(4);
  {
    u32x4 pr[16], zr[2];
#pragma unroll
    for (int n = 0; n < 2; ++n) zr[n] = ld128_sc(ws, l16 + n * 1024, s_z);
#pragma unroll
    for (int r = 0; r < 8; ++r) pr[r] = ld128_sc(ws, l16 + (r & 1) * 1024, s_part ^ ((r >> 1) * 2048));
    SCHED();
    u32x2 eye = {p4 < 2 ? 0x3f80u << (16 * p4) : 0u, p4 < 2 ? 0u : 0x3f80u << (16 * (p4 & 1))};
    PIN(eye);
    f32x4 eps4 = {EPS, EPS, EPS, EPS};
    PIN(eps4);
    const u32 zw2 = opaque(L_X2 + 4096 + wave * 64 + (lane & 3) * 16 + (((lane >> 2) & 1) ^ mh) * 8 + g * 2), zr2 = opaque(L_X2 + 4096 + wave * 64 + g * 16);
    const u32 xw2 = opaque(L_X2 + wave * 1024 + lane * 16), xr2 = opaque(L_X2 + (wave ^ 1) * 1024 + lane * 16);
    const u32 a_o = opaque(WS_O + rb * 262144 + mh * 131072 + head * 2048 + lane * 16), a_o2 = opaque(a_o + 65536);
    SCHED();
#pragma unroll
    for (int r = 8; r < 16; ++r) pr[r] = ld128_sc(ws, l16 + (r & 1) * 1024, s_part ^ ((r >> 1) * 2048));
    SCHED();
    const f32x4 zero = {0, 0, 0, 0};
    f32x4 sum[2] = {zero, zero};
    auto add = [&](int r) INL {
#pragma unroll
      for (int t = 0; t < 2; ++t)
        sum[t] = __builtin_amdgcn_mfma_f32_4x4x4bf16_1k(__builtin_bit_cast(s16x4, eye), __builtin_bit_cast(s16x4, u32x2{pr[r][2 * t], pr[r][2 * t + 1]}), sum[t], 0, 0, 0);
    };
    f32x4 zs = __builtin_bit_cast(f32x4, zr[0]) + __builtin_bit_cast(f32x4, zr[1]);
#pragma unroll
    for (int r = 0; r < 4; ++r) zs[r] += f_(__builtin_amdgcn_update_dpp(0, u_(zs[r]), 0x128, 0xf, 0xf, false));
#pragma unroll
    for (int r = 0; r < 4; r += 2) {
      auto a = __builtin_amdgcn_permlane16_swap(u_(zs[r]), u_(zs[r + 1]), false, false);
      zs[r >> 1] = f_(a[0]) + f_(a[1]);
    }
    {
      auto a = __builtin_amdgcn_permlane32_swap(u_(zs[0]), u_(zs[1]), false, false);
      *L<u16>(zw2) = (u16)pk2(f_(a[0]) + f_(a[1]), 0.f);
    }
    asm volatile("" :: "v"(f1), "v"(g1) : "memory");
    u32x4 zf = *L<u32x4>(zr2);
    SCHED();
#pragma unroll
    for (int r = 0; r < 8; ++r) add(r);
    asm volatile("" :: "v"(sum[0]), "v"(sum[1]));
    SCHED();
    PIN(zf);
    f32x4 den[4];
#pragma unroll
    for (int mt = 0; mt < 4; ++mt) den[mt] = mfma(zf, pq[mt], eps4);
    SCHED();
#pragma unroll
    for (int r = 8; r < 12; ++r) add(r);
    SCHED();
    float inv[4];
#pragma unroll
    for (int mt = 0; mt < 4; ++mt) { inv[mt] = __builtin_amdgcn_rcpf(den[mt][0]); asm volatile("" : "+v"(inv[mt]) : "v"(den[mt])); }
    SCHED();
#pragma unroll
    for (int r = 12; r < 16; ++r) add(r);
    const u32x4 kvm = pack8(sum[0], sum[1]);
    *L<u32x2>(xw2 + 8) = u32x2{kvm[0], kvm[1]}; *L<u32x2>(xw2) = u32x2{kvm[2], kvm[3]};
    lds_set<W_X2>(vfm, v1);
    STAMP(11);
    f32x4 nm[4];
#pragma unroll
    for (int mt = 0; mt < 4; ++mt) nm[mt] = mfma(kvm, pq[mt], zero);
    SCHED();
    u32x4 kvo; u32 fl;
    asm volatile("ds_read_b32 %0, %2 offset:%4\n\tds_read_b128 %1, %3" : "=&v"(fl), "=&v"(kvo) : "v"(vfp), "v"(xr2), "i"(4 * W_X2) : "memory");
    SCHED();
    u32x2 om[4];
#pragma unroll
    for (int mt = 0; mt < 4; ++mt) { om[mt] = pack4(nm[mt] * inv[mt]); PIN(om[mt]); }
    SCHED();
    {
      u32 s, n;
      asm volatile("s_mov_b32 %3, %6\n\ts_branch 1f\n"
                   "0:\n\tds_read_b32 %0, %4 offset:%7\n\tds_read_b128 %1, %5\n"
                   "1:\n\ts_sub_u32 %3, %3, 1\n\ts_waitcnt lgkmcnt(0)\n\ts_cbranch_scc1 2f\n\tv_readfirstlane_b32 %2, %0\n\ts_cmp_eq_u32 %2, 0\n\ts_cbranch_scc1 0b\n"
                   "2:" : "+v"(fl), "+v"(kvo), "=&s"(s), "=&s"(n) : "v"(vfp), "v"(xr2), "i"(SPIN_CAP << 3), "i"(4 * W_X2) : "memory", "scc");
    }
#pragma unroll
    for (int mt = 0; mt < 4; ++mt) nm[mt] = mfma(kvo, pq[mt], zero);
    SCHED();
#pragma unroll
    for (int mt = 0; mt < 4; ++mt) {
      const u32x2 ot = pack4(nm[mt] * inv[mt]);
      st128_sc(u32x4{om[mt].x, om[mt].y, ot.x, ot.y}, ws, ((mt >> 1) ? a_o2 : a_o) + (mt & 1) * 1024);
      SCHED();
    }
  }
  STAMP(5);
  const u32 a_post2 = opaque(lane < 16 ? WS_F2 + (rb * 16 + lane) * 256 + cb * 8 : OOB), a_poll2 = opaque(WS_F2 + (rb2 * 16 + cb2) * 256 + r16 * 8);
  u32 s_o = WS_O + rb2 * 262144 + wave * 65536 + hx * 2048;
  SPIN(s_o);
  u32 wb[4][2];
  {
    const int fw = (2 * g + (q4 >> 1)) & 3;
#pragma unroll
    for (int t = 0; t < 4; ++t)
#pragma unroll
      for (int e = 0; e < 2; ++e) wb[t][e] = opaque((16 * (e ^ hh) + 4 * g + q4) * 128 + ((t ^ fw) * 32) + p4 * 8);
  }
  f32x4 oc[2][4];
#pragma unroll
  for (int m = 0; m < 2; ++m)
#pragma unroll
    for (int t = 0; t < 4; ++t) { oc[m][t] = f32x4{0, 0, 0, 0}; PIN(oc[m][t]); }
  const bool last2 = drain_last<W_CNT2>(vf, v1);
  if (last2) post(ws, a_post2, vtag);
  else if (wst < 24) lds_await<W_CNT2, 4, 0>(vf);
  STAMP(6);
#pragma unroll 1
  while (wst < 24) WO1();
  u32x2 fa2, fb2;
  asm volatile("" : "=v"(fa2), "=v"(fb2));
  if (last2) {
    if (P2) {
      asm volatile(POLL_ASM("%0", "%1", "%2") "\n\ts_sleep %3" : "+v"(fa2) : "v"(a_poll2), "s"(ws), "i"(P2) : "memory");
      await2(fa2, fb2, ws, a_poll2, tag);
    } else await<0>(ws, a_poll2, tag);
    lds_set<W_GOT2>(vf, v1);
    lds_inc<W_WO1>(vf, v1);
  } else { waitv<0>(); lds_inc<W_WO1>(vf, v1); lds_await<W_GOT2, 1, 0>(vf); }
  STAMP(7);
  {
    u32x4 of[WIN][2];
    auto ldo = [&](int st, int m) INL { of[st % WIN][m] = ld128_sc(ws, l16 + ((st & 1) * 2 + m) * 1024, s_o ^ ((st & ~1) * 2048)); };
#pragma unroll
    for (int st = 0; st < WIN; ++st) { ldo(st, 0); ldo(st, 1); SCHED(); }
    lds_await<W_WO1, 4, 0>(vf);
    const bool wo2 = lds_get<W_CNTY>(vf) >= 4;
    s16x4 wf[3][4][2];
    auto ldw = [&](int st, int i) INL {
      const u32 off = st < 24 ? (st & 15) * 4096 : ((st - 24) >> 1) * CH_STRIDE + (st & 1) * 4096;
      wf[st % 3][i >> 1][i & 1] = tr_read(wb[i >> 1][i & 1] + off);
    };
#pragma unroll
    for (int i = 0; i < 16; ++i) ldw(i >> 3, i & 7);
    const rsrc_t ro = mkrsrc(pout, 2048 * 2048 * 2);
    const u32 sw_ = opaque(L_X1 + (wave ^ 1) * 2304 + r16 * 144 + g * 8), sr_ = opaque(L_X1 + (wave ^ 1) * 2304 + r8 * 144 + c8 * 16);
    u32 oo[4];
#pragma unroll
    for (int e = 0; e < 4; ++e) oo[e] = opaque((rb2 * 128 + wave * 32 + e * 8 + r8) * 4096 + 2048 + cb2 * 128 + c8 * 16);
#pragma unroll
    for (int st = 0; st < 32; ++st) {
      if (st == 14 || st == 22) {
#pragma unroll
        for (int i = 0; i < 8; ++i) wb[i >> 1][i & 1] = opaque(wb[i >> 1][i & 1] + (st == 14 ? 65536 : L_CH - 65536));
        if (st == 22 && !wo2) lds_await<W_CNTY, 4, 0>(vf);
      }
      SCHED();
      waitcnt(2 * ((st + WIN < 32 ? WIN : 32 - st) - 1), st < 31 ? 8 : 0);
      if (st == 0) asm volatile("" :: "v"(fa2), "v"(fb2));
      SCHED();
#pragma unroll
      for (int i = 0; i < 8; ++i) {
        const int m = i >> 2, t = m ? 3 - (i & 3) : i;
        mfma_ip(oc[m][t], join(wf[st % 3][t][0], wf[st % 3][t][1]), of[st % WIN][m]);
        if ((i & 3) == 3) { if (st + WIN < 32) ldo(st + WIN, m); }
        else if (st < 30) { ldw(st + 2, m * 4 + (i & 3)); if ((i & 3) == 2) ldw(st + 2, m * 4 + 3); }
        SCHED();
      }
    }
    asm volatile("s_nop 3" : "+v"(oc[0][3]));
    SCHED();
    STAMP(8);
    u32x4 r[4];
#pragma unroll
    for (int m = 0; m < 2; ++m) {
#pragma unroll
      for (int t = 0; t < 4; ++t) { const int tt = m ? 3 - t : t; *L<u32x2>(sw_ + tt * 32) = pack4(oc[m][tt]); SCHED(); }
      FENCE();
      r[2 * m] = *L<u32x4>(sr_); r[2 * m + 1] = *L<u32x4>(sr_ + 1152);
      FENCE(); SCHED();
    }
#pragma unroll
    for (int e = 0; e < 4; ++e) st128_sc(r[e], ro, oo[e]);
    if (bid == 0 && wave == 0) post(ws, lane == 0 ? WS_EPOCH : OOB, vtag);
  }
  STAMP(9);
  STAMP_OUT();
}

extern "C" void kernel_launch(void* const* d_in, const int* in_sizes, int n_in, void* d_out, int out_size, void* d_ws, size_t ws_size,
                              hipStream_t stream) {
  Params p;
  p.q = (const u16*)d_in[0]; p.k = (const u16*)d_in[1]; p.v = (const u16*)d_in[2]; p.sk = (const u16*)d_in[3];
  p.h = (const u16*)d_in[4]; p.dd = (const u16*)d_in[5]; p.x = (const u16*)d_in[6]; p.wqkv = (const u16*)d_in[7];
  p.wo = (const u16*)d_in[8]; p.out = (u16*)d_out; p.ws = (char*)d_ws;
  const u64 kord[32] = {KTABLE};
  for (int i = 0; i < 32; ++i) p.kord[i] = kord[i];
  (void)hipFuncSetAttribute((const void*)fused_h3_linattn, hipFuncAttributeMaxDynamicSharedMemorySize, LDS_BYTES);
  fused_h3_linattn<<<256, 512, LDS_BYTES, stream>>>(p);
}
```

```cpp
#include <hip/hip_runtime.h>
#include <hip/hip_bf16.h>

typedef unsigned short u16;
typedef unsigned int u32;
typedef unsigned long long u64;
typedef float f32x2 __attribute__((ext_vector_type(2)));
typedef float f32x4 __attribute__((ext_vector_type(4)));
typedef u32 u32x2 __attribute__((ext_vector_type(2)));
typedef u32 u32x4 __attribute__((ext_vector_type(4)));
typedef short s16x4 __attribute__((ext_vector_type(4)));
typedef __bf16 bf16x2 __attribute__((ext_vector_type(2)));
typedef __bf16 bf16x8 __attribute__((ext_vector_type(8)));
typedef __amdgpu_buffer_rsrc_t rsrc_t;
#define LAS __attribute__((address_space(3)))
#define DI __device__ __forceinline__
#define INL __attribute__((always_inline))
#if defined(__HIP_DEVICE_COMPILE__)
#define NOPK __attribute__((target("no-packed-fp32-ops")))
#else
#define NOPK
#endif

constexpr int CODE_BYTES = 26880;
constexpr int WIN = 6;

#define KTABLE \
  0xef3abc89d1026574ull, 0xeab5c89df7012346ull, 0x8bfcae79d2503164ull, 0xacfebd5179308462ull, \
  0x8fbe1a50c9d37642ull, 0xfec1ad58930b7642ull, 0xeac8f91db6735042ull, 0xe8b1dfca53097264ull, \
  0xfeb8a31c596d0742ull, 0xbec8a5f910732d46ull, 0xf8ecb1da59732064ull, 0xae18f5b7d3c96042ull, \
  0x81ef5ba9637dc420ull, 0xfea158d0b3c76924ull, 0xc1f8ebd97a650324ull, 0x18efbda53c720496ull, \
  0xfeb38c10d952a764ull, 0xbeacf518d9037624ull, 0xfeb8acd952107346ull, 0xaef15bdc78306924ull, \
  0xfe8b3c1957a04d62ull, 0xeafb8c510d936724ull, 0xcbfe18795ad36024ull, 0xefb81c5d0a934276ull, \
  0xfb3e8c1d0a596724ull, 0xbfae8dc917530264ull, 0xf8bcad952e714036ull, 0xefab1d8c50796342ull, \
  0x8fbd1e57ac496320ull, 0xfa8ed1bc53790624ull, 0xcf8b1d3e796a2504ull, 0xe8bfad1c03572946ull
#define K0A 0x6440246242222464ull
#define K0B 0x6440264464424644ull
#define K1A 0x9222464464446647ull
#define K1B 0x4022436272262426ull

constexpr u32 WS_EPOCH = 0;
constexpr u32 WS_F1 = 65536;
constexpr u32 WS_F2 = WS_F1 + 65536;
constexpr u32 WS_FA = WS_F2 + 65536;
constexpr u32 WS_FY = WS_FA + 131072;
constexpr u32 WS_Z = 512u << 10;
constexpr u32 WS_PART = 1u << 20;
constexpr u32 WS_O = 2u << 20;
constexpr u32 WS_KVT = 6u << 20;
constexpr u32 WS_YT = 10u << 20;
constexpr u32 WS_BYTES = 14u << 20;
constexpr u32 OOB = 0x40000000u;

constexpr u32 SLOT = 40960;
constexpr u32 L_X1 = 98304;
constexpr u32 L_X2 = L_X1 + 4 * 2304;
constexpr u32 CX_STRIDE = 4640, CH_STRIDE = 8320, C_E = 4160;
constexpr u32 L_FLAG = 163840 - 128;
constexpr u32 L_CH = L_FLAG - 4 * CH_STRIDE;
constexpr u32 L_CX = L_CH - 4 * CX_STRIDE;
constexpr u32 LDS_BYTES = 163840;
static_assert(L_X2 + 4096 + 256 <= L_CX && L_CH >= 3 * SLOT, "lds map");
enum { W_WO1 = 0, W_B2, W_STAGED, W_PST, W_X1 = 4, W_X2 = 8, W_CNT1 = 12, W_CNT2, W_CNTY, W_GOT1, W_GOT2 };

constexpr int AUX_SC1 = 16, AUX_SC1_NT = 18;
constexpr u32 SPIN_CAP = 1u << 16;

DI float f_(u32 x) { return __builtin_bit_cast(float, x); }
DI u32 u_(float x) { return __builtin_bit_cast(u32, x); }
DI f32x4 mfma(u32x4 a, u32x4 b, f32x4 c) {
  return __builtin_amdgcn_mfma_f32_16x16x32_bf16(__builtin_bit_cast(bf16x8, a), __builtin_bit_cast(bf16x8, b), c, 0, 0, 0);
}
DI void mfma_ip(f32x4& c, u32x4 a, u32x4 b) { asm volatile("v_mfma_f32_16x16x32_bf16 %0, %1, %2, %0" : "+v"(c) : "v"(a), "v"(b)); }
DI u32 pk2(float lo, float hi) { return __builtin_bit_cast(u32, __builtin_convertvector(f32x2{lo, hi}, bf16x2)); }
DI u32x2 pack4(f32x4 a) { return u32x2{pk2(a[0], a[1]), pk2(a[2], a[3])}; }
DI u32x4 pack8(f32x4 a, f32x4 b) { return u32x4{pk2(a[0], a[1]), pk2(a[2], a[3]), pk2(b[0], b[1]), pk2(b[2], b[3])}; }
DI float blo(u32 x) { return f_(x << 16); }
DI float bhi(u32 x) { return f_(x & 0xffff0000u); }
#define SCHED() __builtin_amdgcn_sched_barrier(0)
#define FENCE() asm volatile("" ::: "memory")
#define BAR() do { FENCE(); SCHED(); __builtin_amdgcn_s_barrier(); SCHED(); FENCE(); } while (0)
#define PIN(x) asm volatile("" : "+v"(x))
#define SPIN(x) asm volatile("" : "+s"(x))
template <int N> DI void waitv() { asm volatile("s_waitcnt vmcnt(%0)" ::"n"(N) : "memory"); }
#define WAITLG(n) __builtin_amdgcn_s_waitcnt(0xC07F | ((n) << 8))
DI void waitl() { FENCE(); WAITLG(0); FENCE(); }
DI void waitcnt(int vm, int lg) {
#define WC(V, G) if (vm == V && lg == G) __builtin_amdgcn_s_waitcnt(((V) & 15) | 0x70 | ((G) << 8) | (((V) >> 4) << 14));
  WC(8, 8) WC(10, 8) WC(8, 15)
}
template <class T> DI LAS T* L(u32 a) { return (LAS T*)(size_t)a; }
DI u32 opaque(u32 a) { PIN(a); return a; }
DI rsrc_t mkrsrc(const void* p, u32 bytes) { return __builtin_amdgcn_make_buffer_rsrc((void*)p, 0, bytes, 0x20000); }
template <int LI, int SI> DI void dma(rsrc_t r, u32 lds, u32 voff, u32 soff) {
  u32 t;
  asm volatile("s_add_u32 m0, %1, %2\n\ts_add_u32 %0, %5, %6\n\tbuffer_load_dwordx4 %3, %4, %0 offen lds"
               : "=&s"(t) : "s"(lds), "i"(LI), "v"(voff), "s"(r), "s"(soff), "i"(SI) : "memory", "scc");
}
DI u32x4 ld128(rsrc_t r, u32 voff, u32 soff) { return __builtin_amdgcn_raw_buffer_load_b128(r, voff, soff, 0); }
DI u32x4 ld128_sc(rsrc_t r, u32 voff, u32 soff) { return __builtin_amdgcn_raw_buffer_load_b128(r, voff, soff, AUX_SC1); }
DI void st128_sc(u32x4 v, rsrc_t r, u32 voff) { __builtin_amdgcn_raw_buffer_store_b128(v, r, voff, 0, AUX_SC1); }

template <int W> DI void lds_set(u32 vf, u32 v1) {
  asm volatile("s_mov_b64 exec, 1\n\tds_write_b32 %0, %1 offset:%2\n\ts_mov_b64 exec, -1" :: "v"(vf), "v"(v1), "i"(4 * W) : "memory");
}
template <int W> DI void lds_inc(u32 vf, u32 v1) {
  asm volatile("s_mov_b64 exec, 1\n\tds_add_u32 %0, %1 offset:%2\n\ts_mov_b64 exec, -1" :: "v"(vf), "v"(v1), "i"(4 * W) : "memory");
}
template <int W> DI u32 lds_get(u32 vf) {
  u32 t;
  asm volatile("ds_read_b32 %0, %1 offset:%2\n\ts_waitcnt lgkmcnt(0)" : "=v"(t) : "v"(vf), "i"(4 * W) : "memory");
  return __builtin_amdgcn_readfirstlane(t);
}
template <int W, int WANT, int SL> DI void lds_await(u32 vf) {
  u32 t, s, n;
  asm volatile("s_mov_b32 %2, %6\n\ts_branch 1f\n"
               "0:\n\ts_sleep %7\n"
               "1:\n\tds_read_b32 %0, %3 offset:%4\n\ts_sub_u32 %2, %2, 1\n\ts_waitcnt lgkmcnt(0)\n\ts_cbranch_scc1 2f\n\t"
               "v_readfirstlane_b32 %1, %0\n\ts_cmp_lt_u32 %1, %5\n\ts_cbranch_scc1 0b\n"
               "2:" : "=&v"(t), "=&s"(s), "=&s"(n) : "v"(vf), "i"(4 * W), "i"(WANT), "i"(SPIN_CAP << 3), "i"(SL) : "memory", "scc");
}
template <int W> DI bool drain_last(u32 vf, u32 v1) {
  u32 old;
  asm volatile("s_waitcnt vmcnt(0)\n\ts_mov_b64 exec, 1\n\tds_add_rtn_u32 %0, %1, %2 offset:%3\n\ts_mov_b64 exec, -1\n\ts_waitcnt lgkmcnt(0)"
               : "=&v"(old) : "v"(vf), "v"(v1), "i"(4 * W) : "memory");
  return __builtin_amdgcn_readfirstlane(old) == 3;
}
DI void post(rsrc_t ws, u32 voff, u32x2 tag) { __builtin_amdgcn_raw_buffer_store_b64(tag, ws, voff, 0, AUX_SC1); }
#define POLL_ASM(F, A, R) "s_mov_b64 exec, 0xffff\n\tv_mov_b64 " F ", 0\n\tbuffer_atomic_add_x2 " F ", " A ", " R ", 0 offen sc0 sc1\n\ts_mov_b64 exec, -1"
DI void poll(u32x2& f, rsrc_t ws, u32 voff) { asm volatile(POLL_ASM("%0", "%1", "%2") : "=&v"(f) : "v"(voff), "s"(ws) : "memory"); }
DI bool missing(u32x2 f, u64 tag) { return (__builtin_amdgcn_ballot_w64(__builtin_bit_cast(u64, f) != tag) & 0xffff) != 0; }
template <int SL> DI void await(rsrc_t ws, u32 voff, u64 tag) {
  u32x2 f; u32 n;
  asm volatile("s_mov_b32 %1, %5\n\ts_branch 1f\n"
               "0:\n\ts_sleep %6\n"
               "1:\n\t" POLL_ASM("%0", "%2", "%3") "\n\ts_sub_u32 %1, %1, 1\n\ts_waitcnt vmcnt(0)\n\ts_cbranch_scc1 2f\n\t"
               "v_cmp_ne_u64 vcc, %4, %0\n\ts_and_b64 vcc, vcc, 0xffff\n\ts_cbranch_vccnz 0b\n"
               "2:" : "=&v"(f), "=&s"(n) : "v"(voff), "s"(ws), "s"(tag), "i"(SPIN_CAP), "i"(SL) : "memory", "scc", "vcc");
}

struct Params {
  const u16 *x, *wqkv;
  char* ws;
  const u16 *wo, *q, *k, *v, *sk, *h, *dd;
  u16* out;
  u64 kord[32];
};
static_assert(offsetof(Params, ws) == 16 && offsetof(Params, out) == 80, "the kernel reads the block by offset");

DI s16x4 tr_read(u32 a) { return __builtin_amdgcn_ds_read_tr16_b64_v4i16(L<s16x4>(a)); }
DI u32x4 join(s16x4 lo, s16x4 hi) { return __builtin_bit_cast(u32x4, __builtin_shufflevector(lo, hi, 0, 1, 2, 3, 4, 5, 6, 7)); }
struct XFr { u32x4 x[4][2]; };
struct WFr { s16x4 w[6][2][2]; };
DI void rdx(XFr& f, const u32 (&b)[4], int i) { f.x[i >> 1][i & 1] = *L<u32x4>(b[i & 1] + (i >> 1) * 2048); }
DI void rdw(WFr& f, const u32 (&b)[4], int i) {
  const int n = i >> 2, sg = ((n >> 1) + 1) % 3;
  f.w[n][(i >> 1) & 1][i & 1] = tr_read(b[2 + (n & 1)] + sg * 8192 + ((i >> 1) & 1) * 4096 + (i & 1) * 512);
}
DI void mm1(f32x4 (&acc)[4][6], const XFr& x, const WFr& w, int i) {
  const int n = i >> 3, mt = (n & 1) ? 3 - ((i >> 1) & 3) : (i >> 1) & 3, ks = (i ^ (i >> 1)) & 1;
  const u32x4 wv = join(w.w[n][ks][0], w.w[n][ks][1]);
  if (n >= 4) mfma_ip(acc[mt][n], wv, x.x[mt][ks]);
  else mfma_ip(acc[mt][n], x.x[mt][ks], wv);
}
constexpr float EPS = 1e-6f;
DI float phi(float t) {
  const float e = __builtin_amdgcn_fmed3f(__builtin_amdgcn_exp2f(__builtin_fmaf(t, 1.44269504088896f, -EPS * 1.44269504088896f)), 0.f, 1.f);
  return e + __builtin_bit_cast(float, __builtin_elementwise_max(__builtin_bit_cast(int, t), __builtin_bit_cast(int, EPS)));
}

struct CFr { u32x2 a[8][2]; u32x4 b[8]; };
DI void ldconv1(CFr& f, const u32 (&hb)[4], u32 xb, u32 off, int k) {
  const int i = k / 3, e = k % 3;
  if (e == 0) f.b[i] = *L<u32x4>(xb + off + (7 - i) * 512);
  else { LAS u32* hp = L<u32>(hb[i >> 1] + off + (i & 1) * 512 + (e - 1) * 8); f.a[i][e - 1] = u32x2{hp[0], hp[1]}; }
}
DI void mmconv1(f32x4 (&acc)[8], const CFr& f, int k) {
  const int n = (k >= 1) + (k >= 3) + (k >= 6) + (k >= 10) + (k >= 15) + (k >= 21) + (k >= 28);
  k -= n * (n + 1) / 2;
  mfma_ip(acc[n], u32x4{f.a[k][0].x, f.a[k][0].y, f.a[k][1].x, f.a[k][1].y}, f.b[n - k]);
}

__global__ NOPK __launch_bounds__(512, 2) void fused_h3_linattn(Params p) {
  int bid = __builtin_amdgcn_workgroup_id_x(), tid = __builtin_amdgcn_workitem_id_x();
  typedef u64 u64x2 __attribute__((ext_vector_type(2)));
  typedef u64 u64x8 __attribute__((ext_vector_type(8)));
  u64x2 a0; u64x8 a1; u64 a2;
  asm volatile("s_load_dwordx4 %0, %4, 0\n\ts_load_dwordx16 %1, %4, 16\n\ts_load_dwordx2 %2, %4, 80"
               : "=&s"(a0), "=&s"(a1), "=&s"(a2), "+s"(bid) : "s"(__builtin_amdgcn_kernarg_segment_ptr()));
  PIN(tid);
  const int xcd = bid & 7, j = bid >> 3;
  const int rb = (xcd & 1) * 8 + (j & 7), cb = (xcd >> 1) * 4 + (j >> 3);
  const int cq = (cb & 12) | ((cb + 1) & 3);
  const int rb2 = (xcd >> 1) * 4 + (j & 3), cb2 = (xcd & 1) * 8 + (j >> 2);
  const int hx = (j & 3) * 8;
  const int wave = __builtin_amdgcn_readfirstlane(tid >> 6) & 7, lane = tid & 63;
  const int r8 = lane >> 3, c8 = lane & 7;
  u32 cpf = 0;
  if (wave == 3) {
    u64 pc;
    asm volatile("s_getpc_b64 %0" : "=s"(pc));
    asm volatile("s_mov_b64 exec, 0xff\n\tbuffer_load_dword %0, %1, %2, 0 offen\n\ts_mov_b64 exec, -1" : "+v"(cpf)
                 : "v"((j * 8 + lane) * 128), "s"(mkrsrc((const void*)(pc & ~127ull), CODE_BYTES)) : "memory");
  }
  u32 qd = (cq - cb) * 128;
  const int kbase = (xcd & 1) * 8 + (xcd >> 1) * 4;
  u32 vx[2], vw[2];
#pragma unroll
  for (int e = 0; e < 2; ++e) {
    vx[e] = opaque(r8 * 2048 + ((c8 ^ (4 * e + (r8 >> 1))) * 16));
    vw[e] = r8 * 6144 + ((c8 ^ ((((r8 >> 1) & 1) | (e << 1)) << 1)) * 16);
  }
  const u32 s0 = ((u32)(((j & 16) ? K0B : K0A) >> (4 * (j & 15))) + kbase) & 15, vwp = opaque(vw[0] ^ ((wave & 1) * 64));
  u32 sx = (rb * 128 + wave * 16) * 2048 + s0 * 128, sw = (s0 * 64 + wave * 8) * 6144 + cb * 128, sq = sw + qd, lx = wave * 2048, lw = wave * 1024;
  asm volatile("s_waitcnt lgkmcnt(0)" : "+s"(a0), "+s"(a1), "+s"(a2), "+s"(sx), "+s"(sw), "+s"(sq), "+s"(lx), "+s"(lw));
  const rsrc_t rx = mkrsrc((const void*)a0[0], 2048 * 1024 * 2), rw = mkrsrc((const void*)a0[1], 1024 * 3072 * 2);
  dma<SLOT, 0>(rx, lx, vx[0], sx);
  dma<SLOT + 16384 + 8192, 2048>(rw, lw, vwp, sw);
  dma<SLOT + 16384, 0>(rw, lw, vwp, sq);
  dma<SLOT + 16384 + 16384, 4096>(rw, lw, vwp, sw);
  dma<SLOT + 1024, 16384>(rx, lx, vx[1], sx);
  char* pws = (char*)a1[0];
  const u16 *pwo = (const u16*)a1[1], *pq_ = (const u16*)a1[2], *pk_ = (const u16*)a1[3], *pv_ = (const u16*)a1[4], *psk = (const u16*)a1[5],
            *ph_ = (const u16*)a1[6], *pdd = (const u16*)a1[7];
  u16* pout = (u16*)a2;
  const rsrc_t ws = mkrsrc(pws, WS_BYTES);
  const int chl = (rb >> 3) * 32 + (wave & 3) * 8 + (rb & 7);
  u64 ep0, tag, kord0;
  u32 dv0, dvw;
  asm volatile("s_load_dwordx2 %0, %3, 0\n\ts_load_dwordx2 %1, %4, %5\n\ts_load_dword %2, %6, %7" : "=&s"(ep0), "=&s"(kord0), "=&s"(dv0)
               : "s"(pws), "s"(__builtin_amdgcn_kernarg_segment_ptr()), "s"((u32)offsetof(Params, kord) + j * 8), "s"(pdd), "s"(((cb * 64 + chl) >> 1) * 4) : "memory");
#define GET_TAG() do { \
    asm volatile("s_waitcnt lgkmcnt(0)\n\ts_mov_b64 %0, %2\n\ts_mov_b32 %1, %4" : "=&s"(tag), "=&s"(dvw) : "s"(ep0), "s"(kord0), "s"(dv0) : "memory"); \
    tag = tag * 6364136223846793005ull + 1442695040888963407ull; SPIN(tag); } while (0)
#define TAG2 (u32x2{(u32)tag, (u32)(tag >> 32)})
  const rsrc_t rwo = mkrsrc(pwo, 1024 * 1024 * 2);
#define VWO (r8 * 2048 + ((c8 ^ (((r8 >> 1) & 3) << 1)) * 16))
  const u32 vf = opaque(L_FLAG), v1 = opaque(1);

  if (wave >= 4) {
    const int hw = wave - 4, aw = hw & 1, ca = (cb + aw + 2) & 15;
    const int R2 = rb * 128 + r8 * 16 + aw * 8;
    const u32 cx = L_CX + hw * CX_STRIDE, chr = L_CH + hw * CH_STRIDE;
    u32x4 qq[8];
    if (hw < 2) {
      const u32 sx0 = (rb * 128 + hw * 64) * 2048, sw0 = (hw * 32) * 6144 + cb * 128;
      u64 kord = (((j & 16) ? K1B : K1A) >> (4 * (j & 15))) << 4;
      auto issue = [&](int s_, u32 base, int half) INL {
        const u32 s = ((u32)(kord >> (4 * s_)) + kbase) & 15;
        const u32 sx = sx0 + s * 128 + half * 65536, sw = sw0 + s * (64 * 6144) + half * 98304, sq = sw + qd;
        const u32 lx = base + hw * 8192 + half * 4096, lw = base + 16384 + hw * 4096 + half * 2048;
        dma<0, 0>(rx, lx, vx[0], sx); dma<1024, 16384>(rx, lx, vx[1], sx);
        dma<8192, 2048>(rw, lw, vw[0], sw); dma<9216, 2048 + 49152>(rw, lw, vw[1], sw);
        dma<0, 0>(rw, lw, vw[0], sq); dma<1024, 49152>(rw, lw, vw[1], sq);
        dma<16384, 4096>(rw, lw, vw[0], sw); dma<17408, 4096 + 49152>(rw, lw, vw[1], sw);
        dma<2048, 32768>(rx, lx, vx[0], sx); dma<3072, 49152>(rx, lx, vx[1], sx);
      };
      issue(1, 2 * SLOT, 0);
      GET_TAG();
      asm volatile("s_mov_b64 %0, %1" : "=s"(kord) : "s"(kord0) : "memory");
      waitv<10>(); BAR();
      issue(1, 2 * SLOT, 1);
      u32 base = 0;
#pragma unroll 1
      for (int s = 2; s < 16; ++s) {
        issue(s, base, 0); issue(s, base, 1);
        base = base == 2 * SLOT ? 0 : base + SLOT;
        waitv<20>(); BAR();
      }
    } else {
      const int R0 = rb * 128 + aw * 64 + r8 * 8;
      const rsrc_t rk = mkrsrc(pk_, 2048 * 1024 * 2), rv = mkrsrc(pv_, 2048 * 1024 * 2), rs = mkrsrc(psk, 1024 * 4 * 2);
      const u32 vo = R0 * 2048 + ca * 128 + c8 * 16;
      const u32 vh = R0 == 0 ? OOB : vo - 3 * 2048;
      const u32 kvt_vo = WS_KVT + ca * 262144 + (c8 >> 2) * 131072 + (255 - (R0 >> 3)) * 64 + (c8 & 3) * 16;
      u32x4 kk[11], vv[8], tw[4];
      const u32x2 t2 = __builtin_amdgcn_raw_buffer_load_b64(rs, ca * 512 + lane * 8, 0, 0);
      GET_TAG();
      waitv<1>(); BAR();
#define AB(i) BAR()
      *L<u32x2>(chr + 64 + lane * 8) = t2;
      FENCE();
#pragma unroll
      for (int i = 0; i < 4; ++i) tw[i] = *L<u32x4>(chr + 64 + c8 * 64 + i * 16);
      const int sg = (j + (j >> 3)) & 1;
      AB(0);
      if (sg) { __builtin_amdgcn_s_sleep(16); AB(1); }
      __builtin_amdgcn_s_sleep(8);
#pragma unroll
      for (int i = 0; i < 8; ++i) kk[3 + i] = ld128(rk, vo, i * 2048);
#pragma unroll
      for (int i = 0; i < 3; ++i) kk[i] = ld128(rk, vh, i * 2048);
      AB(sg + 1);
      float wf[8][4], ks[8][8], kf[11];
#pragma unroll
      for (int c = 0; c < 8; ++c) {
        if (!(c & 1)) PIN(tw[c >> 1]);
#pragma unroll
        for (int t = 0; t < 4; ++t) { const u32 d = tw[c >> 1][(c & 1) * 2 + (t >> 1)]; wf[c][t] = (t & 1) ? bhi(d) : blo(d); PIN(wf[c][t]); }
      }
#pragma unroll
      for (int u = 0; u < 24; ++u) {
        if (u % 3 == 0) {
          if (u == 3) {
#pragma unroll
            for (int i = 0; i < 8; ++i) vv[i] = ld128(rv, vo, i * 2048);
          }
          if (u == 0) __builtin_amdgcn_s_sleep(12); else if (u <= 9) __builtin_amdgcn_s_sleep(4);
          AB(sg + 2 + u / 3);
        }
        if (u < 16) {
          const int c = u >> 1, hf = u & 1;
#pragma unroll
          for (int i = hf ? 7 : 0; i < (hf ? 11 : 7); ++i) { PIN(kk[i]); const u32 d = kk[i][c >> 1]; kf[i] = (c & 1) ? bhi(d) : blo(d); }
#pragma unroll
          for (int i = hf * 4; i < hf * 4 + 4; ++i) {
            if (hf && i < 7) PIN(kf[i]);
            float a = kf[i] * wf[c][3];
            a = __builtin_fmaf(kf[i + 1], wf[c][2], a); a = __builtin_fmaf(kf[i + 2], wf[c][1], a); a = __builtin_fmaf(kf[i + 3], wf[c][0], a);
            ks[c][i] = a; PIN(ks[c][i]);
          }
        } else {
          const int c = u - 16;
          float o[8];
#pragma unroll
          for (int i = 0; i < 8; ++i) { PIN(vv[i]); const u32 d = vv[i][c >> 1]; o[i] = ks[c][i] * ((c & 1) ? bhi(d) : blo(d)); }
          st128_sc(u32x4{pk2(o[7], o[6]), pk2(o[5], o[4]), pk2(o[3], o[2]), pk2(o[1], o[0])}, ws, kvt_vo + c * 16384);
        }
      }
      AB(sg + 10);
      const u32 pa = opaque(lane < 16 ? WS_FA + ((ca * 16 + lane) * 2 + (rb < 8)) * 256 + ((rb & 7) * 2 + aw) * 8 : OOB);
      __builtin_amdgcn_s_sleep(6);
      waitv<0>();
      post(ws, pa, TAG2);
      if (!sg) AB(11);
      AB(12); AB(13);
    }
    {
      u32x4 hh4[4], xs[4];
      const u32 fa = WS_FA + ((cb * 16 + rb) * 2 + (hw >> 1)) * 256 + (lane & 15) * 8;
      u32x2 fl;
      poll(fl, ws, fa);
      const rsrc_t rh = mkrsrc(ph_, 1024 * 2048 * 2), rq = mkrsrc(pq_, 2048 * 1024 * 2);
#pragma unroll
      for (int n = 0; n < 4; ++n) hh4[n] = ld128(rh, lane * 16 + n * 1024, (cb * 64 + chl) * 4096);
      if (lane < 34) *L<u32x4>(lane < 30 ? cx + 4096 + lane * 16 : chr + (lane >> 1 & 1) * C_E + (lane & 1) * 16) = u32x4{0, 0, 0, 0};
      const u32 hr = opaque(chr + lane * 16), xw = opaque(L_CX + (lane & 3) * CX_STRIDE + (hw * 64 + (lane >> 2)) * 16);
      if (hw < 2) waitv<5>();
      BAR();
      if (hw >= 2) {
#pragma unroll
        for (int i = 0; i < 8; ++i) asm volatile("buffer_load_dwordx4 %0, %1, %2, %3 offen" : "=v"(qq[i]) : "v"(R2 * 2048 + ca * 128 + c8 * 16), "s"(rq), "s"(i * 2048) : "memory");
        waitv<12>();
      } else waitv<4>();
      PIN(fl);
      if (missing(fl, tag)) await<4>(ws, fa, tag);
#pragma unroll
      for (int n = 0; n < 4; ++n) xs[n] = ld128_sc(ws, lane * 16 + n * 1024, WS_KVT + cb * 262144 + rb * 16384 + hw * 4096);
      SCHED();
#pragma unroll
      for (int n = 0; n < 4; ++n) *L<u32x4>(hr + 32 + n * 1024) = hh4[n];
      FENCE();
      u32 dp[4];
#pragma unroll
      for (int n = 0; n < 4; ++n) dp[n] = *L<u32>(hr + 28 + n * 1024);
#pragma unroll
      for (int n = 0; n < 4; ++n) {
        const u32x4 d = hh4[n];
        *L<u32x4>(hr + C_E + 32 + n * 1024) =
            u32x4{__builtin_amdgcn_alignbit(d[0], dp[n], 16), __builtin_amdgcn_alignbit(d[1], d[0], 16),
                  __builtin_amdgcn_alignbit(d[2], d[1], 16), __builtin_amdgcn_alignbit(d[3], d[2], 16)};
      }
#pragma unroll
      for (int n = 0; n < 4; ++n) *L<u32x4>(xw + n * 256) = xs[n];
      lds_inc<W_STAGED>(vf, v1);
    }
    const int b16 = lane & 15, kg = lane >> 4;
    u32 hb[4], xb = opaque(cx + (254 - 224 - 2 * b16 + kg) * 16);
#pragma unroll
    for (int i = 0; i < 4; ++i) hb[i] = opaque(chr + ((b16 & 1) ? 0 : C_E) + 4 + 4 * (b16 >> 1) + 16 * kg + i * 1024);
    f32x4 acc[8];
#pragma unroll
    for (int n = 0; n < 8; ++n) { acc[n] = f32x4{0, 0, 0, 0}; PIN(acc[n]); }
    lds_await<W_STAGED, 4, 1>(vf);
    CFr C0, C1;
#pragma unroll
    for (int k = 0; k < 24; ++k) ldconv1(C0, hb, xb, 0, k);
    waitl();
#define RESIDUE(CUR, NXT, OFF) do { \
      _Pragma("unroll") for (int k = 0; k < 36; ++k) { \
        if (k == 0) WAITLG(9); else if (k == 15) WAITLG(10); \
        mmconv1(acc, CUR, k); \
        if (k % 3 != 2) ldconv1(NXT, hb, xb, OFF, k - k / 3); \
        SCHED(); \
      } \
    } while (0)
#pragma unroll 1
    for (int it = 0; it < 3; ++it) {
      RESIDUE(C0, C1, 64); RESIDUE(C1, C0, 128);
      xb = opaque(xb + 128);
#pragma unroll
      for (int i = 0; i < 4; ++i) hb[i] = opaque(hb[i] + 128);
    }
    RESIDUE(C0, C1, 64);
    u32x2 xv[8];
    const u32 xe = opaque(cx + (252 - 16 * b16 - 4 * kg) * 2);
#pragma unroll
    for (int k = 0; k < 36; ++k) {
      if (k == 0) WAITLG(9); else if (k == 15) WAITLG(0);
      mmconv1(acc, C1, k);
      if (k >= 16 && k < 24) xv[k - 16] = *L<u32x2>(xe + (23 - k) * 512);
      SCHED();
    }
    asm volatile("s_nop 7" : "+v"(acc[7]));
    SCHED();
    {
      const float dv = blo(dvw >> ((chl & 1) * 16));
      u32x2 y[8];
#pragma unroll
      for (int n = 0; n < 8; ++n)
        y[n] = u32x2{pk2(__builtin_fmaf(bhi(xv[n].y), dv, acc[n][0]), __builtin_fmaf(blo(xv[n].y), dv, acc[n][1])),
                     pk2(__builtin_fmaf(bhi(xv[n].x), dv, acc[n][2]), __builtin_fmaf(blo(xv[n].x), dv, acc[n][3]))};
      const u32 yvo = WS_YT + cb * 262144 + ((kg & 1) * 2 + (b16 >> 3)) * 16384 + chl * 256 + (kg >> 1) * 128 + (b16 & 7) * 16;
#pragma unroll
      for (int n = 0; n < 8; n += 2) {
        auto s0 = __builtin_amdgcn_permlane16_swap(y[n].x, y[n + 1].x, false, false);
        auto s1 = __builtin_amdgcn_permlane16_swap(y[n].y, y[n + 1].y, false, false);
        st128_sc(u32x4{s0[0], s1[0], s0[1], s1[1]}, ws, yvo + n * 32768);
      }
    }
    {
      const u32 so = ((24 + hw * 2) ^ hx) * 65536 + cb2 * 128, vwo = VWO;
      dma<0, 0>(rwo, chr, vwo, so); dma<1024, 16384>(rwo, chr, vwo, so); dma<2048, 32768>(rwo, chr, vwo, so); dma<3072, 49152>(rwo, chr, vwo, so);
      dma<4096, 65536>(rwo, chr, vwo, so); dma<5120, 81920>(rwo, chr, vwo, so); dma<6144, 98304>(rwo, chr, vwo, so); dma<7168, 114688>(rwo, chr, vwo, so);
    }
    if (drain_last<W_CNTY>(vf, v1)) post(ws, lane < 32 ? WS_FY + (cb * 32 + lane) * 256 + rb * 8 : OOB, TAG2);
    if (hw >= 2) {
      lds_await<W_B2, 1, 4>(vf);
      await<4>(ws, WS_FY + ((ca * 16 + rb) * 2 + aw) * 256 + (lane & 15) * 8, tag);
      u32x4 yy[8];
      const u32 yo = WS_YT + ca * 262144 + rb * 16384 + r8 * 2048 + aw * 128 + c8 * 16;
#pragma unroll
      for (int c = 0; c < 8; ++c) yy[c] = ld128_sc(ws, yo + c * 256, 0);
      const u32 tw_ = opaque(cx + r8 * 144 + c8 * 16), tr_ = opaque(cx + c8 * 144 + r8 * 16);
#pragma unroll
      for (int hf = 0; hf < 2; ++hf) {
#pragma unroll
        for (int c = 0; c < 4; ++c) *L<u32x4>(tw_ + c * 1152) = yy[hf * 4 + c];
        FENCE();
#pragma unroll
        for (int c = 0; c < 4; ++c) yy[hf * 4 + c] = *L<u32x4>(tr_ + c * 1152);
        FENCE();
      }
      const rsrc_t ro = mkrsrc(pout, 2048 * 2048 * 2);
#pragma unroll
      for (int i = 0; i < 8; ++i) {
        PIN(qq[i]);
        u32x4 o;
#pragma unroll
        for (int d = 0; d < 4; ++d) {
          const u32 y0 = yy[2 * d][i >> 1], y1 = yy[2 * d + 1][i >> 1];
          o[d] = pk2(((i & 1) ? bhi(y0) : blo(y0)) * blo(qq[i][d]), ((i & 1) ? bhi(y1) : blo(y1)) * bhi(qq[i][d]));
        }
        __builtin_amdgcn_raw_buffer_store_b128(o, ro, (R2 + i) * 4096 + ca * 128 + c8 * 16, 0, AUX_SC1_NT);
      }
    }
    return;
  }

  __builtin_amdgcn_s_setprio(1);
  if (wave == 0 && lane < 32) *L<u32>(L_FLAG + 4 * lane) = 0;
  const int mh = wave & 1, hh = wave >> 1;
  const int r16 = lane & 15, g = lane >> 4, q4 = r16 >> 2, p4 = lane & 3;
  f32x4 acc[4][6];
  u32 b[4];
  {
    const u32 xo = (64 * mh + r16) * 128 + ((g ^ (r16 >> 1)) * 16);
    const int f = ((q4 >> 1) & 1) | ((g & 1) << 1);
    const u32 wo = 16384 + (8 * g + q4) * 128 + (((4 * hh + 2 * mh + (p4 >> 1)) ^ (f << 1)) * 16) + 8 * (p4 & 1);
    b[0] = opaque(SLOT + xo); b[1] = opaque(SLOT + (xo ^ 64)); b[2] = opaque(SLOT + wo); b[3] = opaque(SLOT + (wo ^ 32));
  }
#pragma unroll
  for (int a = 0; a < 4; ++a)
#pragma unroll
    for (int n = 0; n < 6; ++n) { const float i0 = (n & 6) == 2 ? 0.f : EPS; acc[a][n] = f32x4{i0, i0, i0, i0}; PIN(acc[a][n]); }
  const u32 vfm = opaque(L_FLAG + 4 * wave), vfp = opaque(L_FLAG + 4 * (wave ^ 1));
  const u32 x1w = opaque(L_X1 + wave * 2304 + lane * 16), x1r = opaque(L_X1 + (wave ^ 1) * 2304 + lane * 16);
  const u32 z1w = opaque(L_X1 + wave * 2304 + 2048 + g * 16), z1r = opaque(L_X1 + (wave ^ 1) * 2304 + 2048 + g * 16);
  const u32 a_part = opaque(WS_PART + ((cb * 2 + hh) * 2 + mh) * 16384 + rb * 1024 + lane * 16);
  const u32 a_z = opaque(r16 == 0 ? WS_Z + (cb * 2 + hh) * 2048 + rb * 128 + mh * 64 + g * 16 : OOB);
  const u32 a_post1 = opaque(lane < 16 ? WS_F1 + (cb * 16 + lane) * 256 + rb * 8 : OOB);
  u32x4 ones = {0x3f803f80u, 0x3f803f80u, 0x3f803f80u, 0x3f803f80u};
  PIN(ones);
  GET_TAG();
  u32x2 vtag = TAG2;
  PIN(vtag);
  XFr X0, X1; WFr W;
  waitv<0>(); PIN(cpf); BAR();
#pragma unroll
  for (int i = 0; i < 20; ++i) { if (i < 2) rdx(X0, b, i); else if (i < 6) rdw(W, b, i - 2); else if (i < 12) rdx(X0, b, i - 4); else rdw(W, b, i - 8); SCHED(); }
  WAITLG(4); SCHED();
  int slot = 1;
#define HALF0(XC) do { \
    u32 d_; \
    _Pragma("unroll") for (int i = 0; i < 24; ++i) { \
      mm1(acc, XC, W, i); \
      if (i < 12) rdw(W, b, 12 + i); \
      else if (i == 12) { SPIN(slot); d_ = slot == 2 ? -2 * SLOT : SLOT; SPIN(d_); } \
      else if (i == 19) { SPIN(slot); slot = slot == 2 ? 0 : slot + 1; SPIN(slot); } \
      else if (i >= 14 && i < 18) b[i - 14] = opaque(b[i - 14] + d_); \
      SCHED(); \
    } \
    waitl(); BAR(); \
  } while (0)
#define HALF1(XC, XN) do { \
    _Pragma("unroll") for (int i = 24; i < 48; ++i) { \
      mm1(acc, XC, W, i); \
      if (i < 40) { if (i & 1) rdw(W, b, (i - 24) >> 1); else rdx(XN, b, (i - 24) >> 1); } \
      else if (i < 44) rdw(W, b, i - 32); \
      SCHED(); \
    } \
    WAITLG(4); SCHED(); \
  } while (0)
#pragma unroll 1
  for (int s = 0; s < 7; ++s) { HALF0(X0); HALF1(X0, X1); HALF0(X1); HALF1(X1, X0); }
  HALF0(X0);
#pragma unroll
  for (int i = 0; i < 8; ++i) { mm1(acc, X0, W, 24 + i); if (i < 4) rdw(W, b, i); rdx(X1, b, i); SCHED(); }
#pragma unroll
  for (int i = 0; i < 32; ++i) {
    mm1(acc, X1, W, i);
    if (i < 12) rdw(W, b, 4 + i);
    else {
#pragma unroll
      for (int e = (i - 12) * 8 / 5; e < (i - 11) * 8 / 5; ++e) {
        const int mt = e < 16 ? e >> 2 : 7 - (e >> 2);
        float t = phi(acc[mt][e >> 4][e & 3]); PIN(t); acc[mt][e >> 4][e & 3] = t;
      }
    }
    SCHED();
  }
  {
    u32x4 pkf[2][2], vfr[2][2];
#pragma unroll
    for (int u = 0; u < 2; ++u)
#pragma unroll
      for (int t = 0; t < 2; ++t) { pkf[u][t] = pack8(acc[2 * u][t], acc[2 * u + 1][t]); PIN(pkf[u][t]); }
    SCHED();
    f32x4 kvp[2][2], zp[2];
    const f32x4 zero = {0, 0, 0, 0};
#pragma unroll
    for (int et = 1; et >= 0; --et) {
#pragma unroll
      for (int u = 1; u >= 0; --u) {
        zp[et] = mfma(pkf[u][et], ones, u ? zero : zp[et]);
        vfr[u][et] = pack8(acc[2 * u][2 + et], acc[2 * u + 1][2 + et]);
#pragma unroll
        for (int dt = 0; dt < 2; ++dt) kvp[dt][et] = mfma(pkf[u][dt], vfr[u][et], u ? zero : kvp[dt][et]);
      }
      SCHED();
    }
#pragma unroll
    for (int dt = 0; dt < 2; ++dt) *L<f32x4>(x1w + dt * 1024) = kvp[dt][1];
    *L<f32x4>(z1w) = zp[1];
    lds_set<W_X1>(vfm, v1);
    f32x4 o0, o1, oz;
    {
      u32 fl, s, n;
      asm volatile("s_mov_b32 %5, %9\n"
                   "1:\n\tds_read_b32 %0, %6 offset:%10\n\tds_read_b128 %1, %7\n\tds_read_b128 %2, %7 offset:1024\n\tds_read_b128 %3, %8\n\t"
                   "s_sub_u32 %5, %5, 1\n\ts_waitcnt lgkmcnt(3)\n\ts_cbranch_scc1 2f\n\tv_readfirstlane_b32 %4, %0\n\ts_cmp_eq_u32 %4, 0\n\ts_cbranch_scc1 1b\n"
                   "2:\n\ts_waitcnt lgkmcnt(0)"
                   : "=&v"(fl), "=&v"(o0), "=&v"(o1), "=&v"(oz), "=&s"(s), "=&s"(n)
                   : "v"(vfp), "v"(x1r), "v"(z1r), "i"(SPIN_CAP << 3), "i"(4 * W_X1) : "memory", "scc");
    }
    st128_sc(pack8(kvp[0][0] + o1, kvp[1][0] + o0), ws, a_part);
    st128_sc(__builtin_bit_cast(u32x4, zp[0] + oz), ws, a_z);
  }
  SCHED();
  {
    WFr W2;
#pragma unroll
    for (int i = 0; i < 16; ++i) { mm1(acc, X0, W, 32 + i); if (i < 8) rdw(W2, b, 16 + i); else if (i == 8) lds_inc<W_PST>(vf, v1); SCHED(); }
#pragma unroll
    for (int i = 0; i < 16; ++i) { mm1(acc, X1, W2, 32 + i); SCHED(); }
  }
  const bool last1 = drain_last<W_CNT1>(vf, v1);
  if (last1) post(ws, a_post1, vtag);
  asm volatile("s_nop 3");
  SCHED();
  u32x4 pq[4];
  const u32 head = cq * 2 + hh, l16 = opaque(lane * 16), a_poll1 = opaque(WS_F1 + (cq * 16 + rb) * 256 + r16 * 8);
  u32x2 f1;
  asm volatile("" : "=v"(f1));
  int wst = 0;
  const u32 vwo = opaque(VWO), wsrc = wave * 16384 + cb2 * 128;
#define WO1() do { dma<0, 0>(rwo, wst * 4096 + wave * 1024, vwo, (wst ^ hx) * 65536 + wsrc); ++wst; } while (0)
#pragma unroll
  for (int mt = 0; mt < 4; ++mt) {
    if (mt == 3) { if (last1) asm volatile(POLL_ASM("%0", "%1", "%2") : "+v"(f1) : "v"(a_poll1), "s"(ws) : "memory"); SCHED(); }
#pragma unroll
    for (int d = 0; d < 4; ++d) {
      u32 t = pk2(phi(acc[mt][4 + (d >> 1)][(d & 1) * 2]), phi(acc[mt][4 + (d >> 1)][(d & 1) * 2 + 1]));
      PIN(t); pq[mt][d] = t;
      if (((mt * 4 + d + 1) * 10) / 16 != ((mt * 4 + d) * 10) / 16) WO1();
      SCHED();
    }
  }
  u32 s_z = WS_Z + head * 2048, s_part = WS_PART + (head * 2 + mh) * 16384 + (rb & 4) * 2048;
  SPIN(s_z); SPIN(s_part);
  SCHED();
  if (last1) {
    waitv<3>(); PIN(f1);
    if (missing(f1, tag)) {
#pragma unroll 1
      for (u32 spins = 0; spins < SPIN_CAP; ++spins) {
        u32x2 f;
        poll(f, ws, a_poll1);
        if (wst < 22) { WO1(); WO1(); WO1(); waitv<3>(); } else waitv<0>();
        PIN(f);
        if (!missing(f, tag)) break;
      }
    }
    lds_set<W_GOT1>(vf, v1);
  } else {
    lds_await<W_PST, 4, 0>(vf);
#pragma unroll 1
    while (wst < 24) { if (lds_get<W_GOT1>(vf)) break; WO1(); }
    if (wst == 24) lds_await<W_GOT1, 1, 4>(vf);
  }
  {
    u32x4 pr[16], zr[2];
#pragma unroll
    for (int n = 0; n < 2; ++n) zr[n] = ld128_sc(ws, l16 + n * 1024, s_z);
#pragma unroll
    for (int r = 0; r < 8; ++r) pr[r] = ld128_sc(ws, l16 + (r & 1) * 1024, s_part ^ ((r >> 1) * 2048));
    SCHED();
    u32x2 eye = {p4 < 2 ? 0x3f80u << (16 * p4) : 0u, p4 < 2 ? 0u : 0x3f80u << (16 * (p4 & 1))};
    PIN(eye);
    f32x4 eps4 = {EPS, EPS, EPS, EPS};
    PIN(eps4);
    const u32 zw2 = opaque(L_X2 + 4096 + wave * 64 + (lane & 3) * 16 + (((lane >> 2) & 1) ^ mh) * 8 + g * 2), zr2 = opaque(L_X2 + 4096 + wave * 64 + g * 16);
    const u32 xw2 = opaque(L_X2 + wave * 1024 + lane * 16), xr2 = opaque(L_X2 + (wave ^ 1) * 1024 + lane * 16);
    const u32 a_o = opaque(WS_O + rb * 262144 + mh * 131072 + head * 2048 + lane * 16), a_o2 = opaque(a_o + 65536);
    SCHED();
#pragma unroll
    for (int r = 8; r < 16; ++r) pr[r] = ld128_sc(ws, l16 + (r & 1) * 1024, s_part ^ ((r >> 1) * 2048));
    SCHED();
    const f32x4 zero = {0, 0, 0, 0};
    f32x4 sum[2] = {zero, zero};
    auto add = [&](int r) INL {
#pragma unroll
      for (int t = 0; t < 2; ++t)
        sum[t] = __builtin_amdgcn_mfma_f32_4x4x4bf16_1k(__builtin_bit_cast(s16x4, eye), __builtin_bit_cast(s16x4, u32x2{pr[r][2 * t], pr[r][2 * t + 1]}), sum[t], 0, 0, 0);
    };
    f32x4 zs = __builtin_bit_cast(f32x4, zr[0]) + __builtin_bit_cast(f32x4, zr[1]);
#pragma unroll
    for (int r = 0; r < 4; ++r) zs[r] += f_(__builtin_amdgcn_update_dpp(0, u_(zs[r]), 0x128, 0xf, 0xf, false));
#pragma unroll
    for (int r = 0; r < 4; r += 2) {
      auto a = __builtin_amdgcn_permlane16_swap(u_(zs[r]), u_(zs[r + 1]), false, false);
      zs[r >> 1] = f_(a[0]) + f_(a[1]);
    }
    {
      auto a = __builtin_amdgcn_permlane32_swap(u_(zs[0]), u_(zs[1]), false, false);
      *L<u16>(zw2) = (u16)pk2(f_(a[0]) + f_(a[1]), 0.f);
    }
    FENCE();
    u32x4 zf = *L<u32x4>(zr2);
    SCHED();
#pragma unroll
    for (int r = 0; r < 8; ++r) add(r);
    asm volatile("" :: "v"(sum[0]), "v"(sum[1]));
    SCHED();
    PIN(zf);
    f32x4 den[4];
#pragma unroll
    for (int mt = 0; mt < 4; ++mt) den[mt] = mfma(zf, pq[mt], eps4);
    SCHED();
#pragma unroll
    for (int r = 8; r < 12; ++r) add(r);
    SCHED();
    float inv[4];
#pragma unroll
    for (int mt = 0; mt < 4; ++mt) { inv[mt] = __builtin_amdgcn_rcpf(den[mt][0]); asm volatile("" : "+v"(inv[mt]) : "v"(den[mt])); }
    SCHED();
#pragma unroll
    for (int r = 12; r < 16; ++r) add(r);
    const u32x4 kvm = pack8(sum[0], sum[1]);
    *L<u32x2>(xw2 + 8) = u32x2{kvm[0], kvm[1]}; *L<u32x2>(xw2) = u32x2{kvm[2], kvm[3]};
    lds_set<W_X2>(vfm, v1);
    f32x4 nm[4];
#pragma unroll
    for (int mt = 0; mt < 4; ++mt) nm[mt] = mfma(kvm, pq[mt], zero);
    SCHED();
    u32x4 kvo; u32 fl;
    asm volatile("ds_read_b32 %0, %2 offset:%4\n\tds_read_b128 %1, %3" : "=&v"(fl), "=&v"(kvo) : "v"(vfp), "v"(xr2), "i"(4 * W_X2) : "memory");
    SCHED();
    u32x2 om[4];
#pragma unroll
    for (int mt = 0; mt < 4; ++mt) { om[mt] = pack4(nm[mt] * inv[mt]); PIN(om[mt]); }
    SCHED();
    {
      u32 s, n;
      asm volatile("s_mov_b32 %3, %6\n\ts_branch 1f\n"
                   "0:\n\tds_read_b32 %0, %4 offset:%7\n\tds_read_b128 %1, %5\n"
                   "1:\n\ts_sub_u32 %3, %3, 1\n\ts_waitcnt lgkmcnt(0)\n\ts_cbranch_scc1 2f\n\tv_readfirstlane_b32 %2, %0\n\ts_cmp_eq_u32 %2, 0\n\ts_cbranch_scc1 0b\n"
                   "2:" : "+v"(fl), "+v"(kvo), "=&s"(s), "=&s"(n) : "v"(vfp), "v"(xr2), "i"(SPIN_CAP << 3), "i"(4 * W_X2) : "memory", "scc");
    }
#pragma unroll
    for (int mt = 0; mt < 4; ++mt) nm[mt] = mfma(kvo, pq[mt], zero);
    SCHED();
#pragma unroll
    for (int mt = 0; mt < 4; ++mt) {
      const u32x2 ot = pack4(nm[mt] * inv[mt]);
      st128_sc(u32x4{om[mt].x, om[mt].y, ot.x, ot.y}, ws, ((mt >> 1) ? a_o2 : a_o) + (mt & 1) * 1024);
      SCHED();
    }
  }
  const u32 a_post2 = opaque(lane < 16 ? WS_F2 + (rb * 16 + lane) * 256 + cb * 8 : OOB), a_poll2 = opaque(WS_F2 + (rb2 * 16 + cb2) * 256 + r16 * 8);
  u32 s_o = WS_O + rb2 * 262144 + wave * 65536 + hx * 2048;
  SPIN(s_o);
  u32 wb[4][2];
  {
    const int fw = (2 * g + (q4 >> 1)) & 3;
#pragma unroll
    for (int t = 0; t < 4; ++t)
#pragma unroll
      for (int e = 0; e < 2; ++e) wb[t][e] = opaque((16 * (e ^ hh) + 4 * g + q4) * 128 + ((t ^ fw) * 32) + p4 * 8);
  }
  f32x4 oc[2][4];
#pragma unroll
  for (int m = 0; m < 2; ++m)
#pragma unroll
    for (int t = 0; t < 4; ++t) { oc[m][t] = f32x4{0, 0, 0, 0}; PIN(oc[m][t]); }
  const bool last2 = drain_last<W_CNT2>(vf, v1);
  if (last2) post(ws, a_post2, vtag);
  else if (wst < 24) lds_await<W_CNT2, 4, 0>(vf);
#pragma unroll 1
  while (wst < 24) WO1();
  if (last2) {
    await<0>(ws, a_poll2, tag);
    lds_set<W_GOT2>(vf, v1);
    lds_inc<W_WO1>(vf, v1);
  } else { waitv<0>(); lds_inc<W_WO1>(vf, v1); lds_await<W_GOT2, 1, 4>(vf); }
  {
    u32x4 of[WIN][2];
    auto ldo = [&](int st, int m) INL { of[st % WIN][m] = ld128_sc(ws, l16 + ((st & 1) * 2 + m) * 1024, s_o ^ ((st & ~1) * 2048)); };
#pragma unroll
    for (int i = 0; i < 2 * WIN; ++i) { ldo((i >> 2) * 2 + (i & 1), (i >> 1) & 1); SCHED(); }
    lds_await<W_WO1, 4, 0>(vf);
    const bool wo2 = lds_get<W_CNTY>(vf) >= 4;
    s16x4 wf[4][4][2];
    auto ldw = [&](int st, int i) INL {
      const u32 off = st < 24 ? (st & 15) * 4096 : ((st - 24) >> 1) * CH_STRIDE + (st & 1) * 4096;
      wf[st & 3][i >> 1][i & 1] = tr_read(wb[i >> 1][i & 1] + off);
    };
#pragma unroll
    for (int i = 0; i < 16; ++i) ldw((i >> 1) & 1, (i >> 2) * 2 + (i & 1));
    const rsrc_t ro = mkrsrc(pout, 2048 * 2048 * 2);
    const u32 sw_ = opaque(L_X1 + (wave ^ 1) * 2304 + r16 * 144 + g * 8), sr_ = opaque(L_X1 + (wave ^ 1) * 2304 + r8 * 144 + c8 * 16);
    u32 oo[4];
#pragma unroll
    for (int e = 0; e < 4; ++e) oo[e] = opaque((rb2 * 128 + wave * 32 + e * 8 + r8) * 4096 + 2048 + cb2 * 128 + c8 * 16);
#pragma unroll
    for (int st = 0; st < 32; st += 2) {
      if (st == 14 || st == 22) {
#pragma unroll
        for (int i = 0; i < 8; ++i) wb[i >> 1][i & 1] = opaque(wb[i >> 1][i & 1] + (st == 14 ? 65536 : L_CH - 65536));
        if (st == 22 && !wo2) lds_await<W_CNTY, 4, 0>(vf);
      }
      SCHED();
#pragma unroll
      for (int i = 0; i < 16; ++i) {
        const int m = i >> 3, t = m ? 3 - ((i >> 1) & 3) : (i >> 1) & 3, s2 = st + ((i ^ (i >> 1)) & 1);
        if (st == 12 && i == 12) lds_set<W_B2>(vf, v1);
        if (st + WIN < 32) { if (i == 0) waitcnt(st ? 2 * WIN - 4 : 2 * WIN - 2, 8); else if (i == 4) WAITLG(4); else if (i == 8) waitcnt(2 * WIN - 4, 15); SCHED(); }
        mfma_ip(oc[m][t], join(wf[s2 & 3][t][0], wf[s2 & 3][t][1]), of[s2 % WIN][m]);
        const int r = i - (i >> 2);
        if ((i & 3) == 3) {
          const int q = i >> 2;
          const int sl = q < 2 ? st - 2 + WIN + q : st + WIN + (q - 2), ml = q < 2 ? 1 : 0;
          if (sl < 32 && sl >= WIN) ldo(sl, ml);
        } else if (st < 30) {
#pragma unroll
          for (int k = r * 4 / 3; k < (r + 1) * 4 / 3; ++k) ldw(st + 2 + ((k >> 1) & 1), (k >> 2) * 2 + (k & 1));
        }
        SCHED();
      }
    }
    asm volatile("s_nop 3" : "+v"(oc[0][3]));
    SCHED();
    u32x4 r[4];
#pragma unroll
    for (int m = 0; m < 2; ++m) {
#pragma unroll
      for (int t = 0; t < 4; ++t) { const int tt = m ? 3 - t : t; *L<u32x2>(sw_ + tt * 32) = pack4(oc[m][tt]); SCHED(); }
      FENCE();
      r[2 * m] = *L<u32x4>(sr_); r[2 * m + 1] = *L<u32x4>(sr_ + 1152);
      FENCE(); SCHED();
    }
#pragma unroll
    for (int e = 0; e < 4; ++e) __builtin_amdgcn_raw_buffer_store_b128(r[e], ro, oo[e], 0, AUX_SC1_NT);
    if (bid == 0 && wave == 0) post(ws, lane == 0 ? WS_EPOCH : OOB, vtag);
  }
}

extern "C" void kernel_launch(void* const* d_in, const int* in_sizes, int n_in, void* d_out, int out_size, void* d_ws, size_t ws_size,
                              hipStream_t stream) {
  Params p;
  p.q = (const u16*)d_in[0]; p.k = (const u16*)d_in[1]; p.v = (const u16*)d_in[2]; p.sk = (const u16*)d_in[3];
  p.h = (const u16*)d_in[4]; p.dd = (const u16*)d_in[5]; p.x = (const u16*)d_in[6]; p.wqkv = (const u16*)d_in[7];
  p.wo = (const u16*)d_in[8]; p.out = (u16*)d_out; p.ws = (char*)d_ws;
  const u64 kord[32] = {KTABLE};
  for (int i = 0; i < 32; ++i) p.kord[i] = kord[i];
  (void)hipFuncSetAttribute((const void*)fused_h3_linattn, hipFuncAttributeMaxDynamicSharedMemorySize, LDS_BYTES);
  fused_h3_linattn<<<256, 512, LDS_BYTES, stream>>>(p);
}
```
